```python
import torch

def _softmax(x, axis=-1):
    return torch.softmax(x, dim=axis)

def _gelu(x):
    return torch.nn.functional.gelu(x, approximate='tanh')

def _silu(x):
    return torch.nn.functional.silu(x)

def _sigmoid(x):
    return torch.sigmoid(x)

def _layernorm(x, axis=-1, eps=1e-05):
    m = x.mean(dim=axis, keepdim=True)
    v = x.var(dim=axis, keepdim=True, unbiased=False)
    return (x - m) / torch.sqrt(v + eps)

def _scatter_add(tgt, idx, src, axis=0):
    return tgt.clone().index_add_(axis, idx, src)

def _segment_sum(src, seg, n):
    out = torch.zeros((n,) + src.shape[1:], dtype=src.dtype)
    return out.index_add_(0, seg, src)

def _pad(x, pw):
    flat = tuple((v for pair in reversed(pw) for v in pair))
    return torch.nn.functional.pad(x, flat)
kernel_name = 'adaLN_cond__mha_block_cached__dense_activation_silu__prompt'

def setup_inputs():

    _w = lambda a: (lambda t: t.to(torch.bfloat16) if t.is_floating_point() else t)(torch.as_tensor(a))

    def _A_setup():
        B, Sq, D = (1, 2048, 448)
        H, Sk, Dh = (16, 2048, 256)
        return {'x': _w((lambda *s: torch.randn(*s, dtype=torch.bfloat16) * 0.1)(B, Sq, D)), 'k_cache': _w((lambda *s: torch.randn(*s, dtype=torch.bfloat16) * 0.8)(B, H, Sk, Dh)), 'v_cache': _w((lambda *s: torch.randn(*s, dtype=torch.bfloat16) * 3.2)(B, H, Sk, Dh)), 'wq': _w((lambda *s: torch.randn(*s, dtype=torch.bfloat16) * 0.8)(D, H * Dh) * D ** (-0.5)), 'wo': _w((lambda *s: torch.randn(*s, dtype=torch.bfloat16) * 3.2)(H * Dh, D) * (H * Dh) ** (-0.5))}
    _A_in = _A_setup()

    def _B_setup():
        B, S, D, F = (1, 2048, 448, 1792)
        return {'x': _w((lambda *s: torch.randn(*s, dtype=torch.bfloat16) * 0.1)(B, S, D)), 'w1': _w((lambda *s: torch.randn(*s, dtype=torch.bfloat16) * 3.2)(D, F) * D ** (-0.5)), 'w2': _w((lambda *s: torch.randn(*s, dtype=torch.bfloat16) * 3.2)(F, D) * F ** (-0.5))}
    _B_in = _B_setup()
    return {'A_x': _A_in['x'], 'A_k_cache': _A_in['k_cache'], 'A_v_cache': _A_in['v_cache'], 'A_wq': _A_in['wq'], 'A_wo': _A_in['wo'], 'B_x': _B_in['x'], 'B_w1': _B_in['w1'], 'B_w2': _B_in['w2'], 'c': _w((lambda *s: torch.randn(*s, dtype=torch.bfloat16) * 0.1)(1, 448) * 8.0), 'w_mod': _w((lambda *s: torch.randn(*s, dtype=torch.bfloat16) * 0.1)(448, 6 * 448) * 448 ** (-0.5) * 8.0)}

def reference(A_x, A_k_cache, A_v_cache, A_wq, A_wo, B_x, B_w1, B_w2, c, w_mod):

    def _A_ref(x, k_cache, v_cache, wq, wo):
        B, Sq, D = x.shape
        H, Dh = (16, 256)
        q = (lambda x, *d: x.permute(*d))((x @ wq).reshape(B, Sq, H, Dh), 0, 2, 1, 3)
        scores = torch.einsum('bhqd,bhkd->bhqk', q, k_cache) * Dh ** (-0.5)
        p = _softmax(scores, axis=-1)
        o = torch.einsum('bhqk,bhkd->bhqd', p, v_cache)
        return (lambda x, *d: x.permute(*d))(o, 0, 2, 1, 3).reshape(B, Sq, H * Dh) @ wo

    def _B_ref(x, w1, w2):
        h = torch.einsum('bsd,df->bsf', x, w1)
        h = _silu(h)
        out = torch.einsum('bsf,fd->bsd', h, w2)
        return out
    x = A_x
    mod = (_silu(c) @ w_mod).reshape(c.shape[0], 6, 1, -1)
    sa, ca, ga, sb, cb, gb = (mod[:, 0].clone(), mod[:, 1].clone(), mod[:, 2].clone(), mod[:, 3].clone(), mod[:, 4].clone(), mod[:, 5].clone())
    h = _layernorm(x, axis=-1) * (1 + ca) + sa
    x = x + ga * _A_ref(h, A_k_cache, A_v_cache, A_wq, A_wo)
    h = _layernorm(x, axis=-1) * (1 + cb) + sb
    x = x + gb * _B_ref(h, B_w1, B_w2)
    return x
```

```cpp
#include <hip/hip_runtime.h>
#include <hip/hip_bf16.h>
typedef unsigned short u16;
__device__ __forceinline__ float bf2f(u16 x){ return __uint_as_float(((unsigned)x)<<16); }
__device__ __forceinline__ u16 f2bf(float f){ unsigned u=__float_as_uint(f); u += 0x7fffu + ((u>>16)&1u); return (u16)(u>>16); }
__device__ __forceinline__ float rbf(float f){ return bf2f(f2bf(f)); }
constexpr int S=2048, D=448, H=16, DH=256, F=1792;
__global__ void k_mod(const u16* c, const u16* wm, u16* mod){
  int n = blockIdx.x*blockDim.x+threadIdx.x; if(n>=6*D) return;
  float acc=0; for(int k=0;k<D;k++){ float x=bf2f(c[k]); float s=rbf(x/(1.f+__expf(-x))); acc+=s*bf2f(wm[k*6*D+n]); }
  mod[n]=f2bf(acc);
}
__global__ void k_ln(const u16* x, const u16* mod, int so, int co, u16* h){
  int r=blockIdx.x; int l=threadIdx.x; float v[7]; float s=0;
  for(int i=0;i<7;i++){ v[i]=bf2f(x[r*D+l+64*i]); s+=v[i]; }
  for(int o=32;o;o>>=1) s+=__shfl_xor(s,o);
  float m=s/D; float q=0; for(int i=0;i<7;i++){ q+=(v[i]-m)*(v[i]-m);} for(int o=32;o;o>>=1) q+=__shfl_xor(q,o);
  float rs=rsqrtf(q/D+1e-5f);
  for(int i=0;i<7;i++){ int c=l+64*i; float y=(v[i]-m)*rs*(1.f+bf2f(mod[co*D+c]))+bf2f(mod[so*D+c]); h[r*D+c]=f2bf(y);} }
__global__ void k_gemm(const u16* A,const u16* B,u16* C,int M,int N,int K,int mode,const u16* res,const u16* g){
  __shared__ float As[16][17], Bs[16][17];
  int tx=threadIdx.x&15, ty=threadIdx.x>>4; int m=blockIdx.y*16+ty, n=blockIdx.x*16+tx; float acc=0;
  for(int k0=0;k0<K;k0+=16){ As[ty][tx]=bf2f(A[(long)m*K+k0+tx]); Bs[ty][tx]=bf2f(B[(long)(k0+ty)*N+n]); __syncthreads();
    for(int k=0;k<16;k++) acc+=As[ty][k]*Bs[k][tx]; __syncthreads(); }
  if(mode==1){ acc=rbf(acc); acc=acc/(1.f+__expf(-acc)); }
  if(mode==2){ acc=bf2f(res[(long)m*N+n])+bf2f(g[n])*rbf(acc); }
  C[(long)m*N+n]=f2bf(acc);
}
__global__ void k_attn(const u16* Q,const u16* Kc,const u16* Vc,u16* O){
  __shared__ float q[DH]; __shared__ float p[S]; __shared__ float red[256];
  int r=blockIdx.x,h=blockIdx.y,t=threadIdx.x; q[t]=bf2f(Q[(long)r*H*DH+h*DH+t]); __syncthreads();
  float mx=-1e30f; for(int k=t;k<S;k+=256){ const u16* kr=Kc+((long)h*S+k)*DH; float a=0; for(int d=0;d<DH;d++) a+=q[d]*bf2f(kr[d]); a*=0.0625f; p[k]=a; mx=fmaxf(mx,a);}
  red[t]=mx; __syncthreads(); for(int o=128;o;o>>=1){ if(t<o) red[t]=fmaxf(red[t],red[t+o]); __syncthreads(); } mx=red[0]; __syncthreads();
  float s=0; for(int k=t;k<S;k+=256){ float e=__expf(p[k]-mx); p[k]=e; s+=e;} red[t]=s; __syncthreads(); for(int o=128;o;o>>=1){ if(t<o) red[t]+=red[t+o]; __syncthreads(); } s=red[0];
  float a=0; for(int k=0;k<S;k++) a+=p[k]*bf2f(Vc[((long)h*S+k)*DH+t]); O[(long)r*H*DH+h*DH+t]=f2bf(a/s);
}
extern "C" void kernel_launch(void* const* d_in, const int* in_sizes, int n_in, void* d_out, int out_size, void* d_ws, size_t ws_size, hipStream_t stream){
  const u16 *x=(u16*)d_in[0],*kc=(u16*)d_in[1],*vc=(u16*)d_in[2],*wq=(u16*)d_in[3],*wo=(u16*)d_in[4],*w1=(u16*)d_in[6],*w2=(u16*)d_in[7],*c=(u16*)d_in[8],*wm=(u16*)d_in[9];
  char* ws=(char*)d_ws; u16* mod=(u16*)ws; u16* h=(u16*)(ws+(1<<20)); u16* Q=(u16*)(ws+(4<<20)); u16* O=(u16*)(ws+(24<<20)); u16* x1=(u16*)(ws+(44<<20)); u16* Hh=(u16*)(ws+(48<<20));
  k_mod<<<(6*D+63)/64,64,0,stream>>>(c,wm,mod);
  k_ln<<<S,64,0,stream>>>(x,mod,0,1,h);
  k_gemm<<<dim3(H*DH/16,S/16),256,0,stream>>>(h,wq,Q,S,H*DH,D,0,nullptr,nullptr);
  k_attn<<<dim3(S,H),256,0,stream>>>(Q,kc,vc,O);
  k_gemm<<<dim3(D/16,S/16),256,0,stream>>>(O,wo,x1,S,D,H*DH,2,x,mod+2*D);
  k_ln<<<S,64,0,stream>>>(x1,mod,3,4,h);
  k_gemm<<<dim3(F/16,S/16),256,0,stream>>>(h,w1,Hh,S,F,D,1,nullptr,nullptr);
  k_gemm<<<dim3(D/16,S/16),256,0,stream>>>(Hh,w2,(u16*)d_out,S,D,F,2,x1,mod+5*D);
}
```

```cpp
#include <hip/hip_runtime.h>
__global__ __launch_bounds__(256, 1) void fused_block(const void* x_, const void* k_, const void* v_, const void* wq_, const void* wo_, const void* w1_, const void* w2_, const void* c_, const void* wm_, const void* out_, const void* ws_) {
  const unsigned xg = blockIdx.x & 7, jj = blockIdx.x >> 3;
  const unsigned wave = __builtin_amdgcn_readfirstlane(threadIdx.x >> 6);
  asm volatile(R"ASM(
L_cbeg%=:
s_load_dwordx8 s[8:15], %[ka], 0x0
s_load_dwordx8 s[16:23], %[ka], 0x20
s_load_dwordx4 s[24:27], %[ka], 0x40
s_load_dwordx2 s[28:29], %[ka], 0x50
s_mov_b32 s80, %[wave]
s_mov_b32 s34, %[xg]
s_mov_b32 s35, %[jj]
v_mbcnt_lo_u32_b32 v15, -1, 0
v_mbcnt_hi_u32_b32 v15, -1, v15
s_getpc_b64 s[88:89]
L_pccbeg%=:
s_add_u32 s88, s88, L_cbeg%=-L_pccbeg%=
s_addc_u32 s89, s89, -1
s_and_b32 s89, s89, 0xffff
s_mov_b32 s90, L_end%=-L_cbeg%=
s_mov_b32 s91, 131072
s_lshl_b32 s53, s35, 2
s_add_u32 s53, s53, s80
s_lshl_b32 s53, s53, 10
v_lshlrev_b32 v3, 7, v15
v_lshrrev_b32 v2, 3, v15
v_min_u32 v2, 1, v2
v_lshl_add_u32 v3, v2, 31, v3
buffer_load_dword a201, v3, s[88:91], s53 offen offset:0
s_mov_b32 s97, 0xffff0000
s_lshl_b32 s53, s80, 6
v_add_u32 v24, s53, v15
v_lshrrev_b32 v16, 2, v24
v_and_b32 v17, 3, v24
v_lshlrev_b32 v27, 1, v16
v_mul_u32_u24 v16, 5376, v16
v_lshl_add_u32 v26, v17, 4, v16
s_lshl_b32 s99, s35, 3
s_add_u32 s99, s99, s34
s_cmp_lt_u32 s99, 84
s_cselect_b32 s54, 0, 0x80000000
v_or_b32 v26, s54, v26
v_lshlrev_b32 v1, 4, v15
v_add_u32 v47, 0, v15
v_mul_u32_u24 v47, 1171, v47
v_lshrrev_b32 v47, 17, v47
v_lshlrev_b32 v47, 4, v47
v_xor_b32 v32, v1, v47
v_xor_b32 v39, 64, v32
v_add_u32 v47, 64, v15
v_mul_u32_u24 v47, 1171, v47
v_lshrrev_b32 v47, 17, v47
v_lshlrev_b32 v47, 4, v47
v_xor_b32 v33, v1, v47
v_xor_b32 v40, 64, v33
v_add_u32 v47, 128, v15
v_mul_u32_u24 v47, 1171, v47
v_lshrrev_b32 v47, 17, v47
v_lshlrev_b32 v47, 4, v47
v_xor_b32 v34, v1, v47
v_xor_b32 v41, 64, v34
v_add_u32 v47, 192, v15
v_mul_u32_u24 v47, 1171, v47
v_lshrrev_b32 v47, 17, v47
v_lshlrev_b32 v47, 4, v47
v_xor_b32 v35, v1, v47
v_xor_b32 v42, 64, v35
v_add_u32 v47, 256, v15
v_mul_u32_u24 v47, 1171, v47
v_lshrrev_b32 v47, 17, v47
v_lshlrev_b32 v47, 4, v47
v_xor_b32 v36, v1, v47
v_xor_b32 v43, 64, v36
v_add_u32 v47, 320, v15
v_mul_u32_u24 v47, 1171, v47
v_lshrrev_b32 v47, 17, v47
v_lshlrev_b32 v47, 4, v47
v_xor_b32 v37, v1, v47
v_xor_b32 v44, 64, v37
v_add_u32 v47, 384, v15
v_mul_u32_u24 v47, 1171, v47
v_lshrrev_b32 v47, 17, v47
v_lshlrev_b32 v47, 4, v47
v_xor_b32 v38, v1, v47
v_xor_b32 v45, 64, v38
v_lshrrev_b32 v18, 4, v24
v_lshlrev_b32 v18, 2, v18
v_lshl_add_u32 v212, v17, 9, v18
v_add_u32 v212, 114688, v212
s_and_b32 s53, s35, 1
s_lshr_b32 s54, s34, 0
s_lshl_b32 s54, s54, 1
s_add_u32 s32, s53, s54
s_and_b32 s53, s34, 0
s_lshl_b32 s53, s53, 4
s_lshr_b32 s33, s35, 1
s_add_u32 s33, s33, s53
s_and_b32 s81, s80, 1
s_lshr_b32 s82, s80, 1
s_lshl_b32 s53, s81, 1
s_add_u32 s53, s53, s82
s_mul_i32 s98, s53, 28672
s_lshl_b32 s53, s99, 6
s_mul_i32 s54, s33, 114688
s_waitcnt lgkmcnt(0)
s_add_u32 s84, s8, s54
s_addc_u32 s85, s9, 0
s_and_b32 s85, s85, 0xffff
s_mov_b32 s86, 114688
s_mov_b32 s87, 131072
s_add_u32 s92, s24, 0
s_addc_u32 s93, s25, 0
s_and_b32 s93, s93, 0xffff
s_mov_b32 s94, 2408448
s_mov_b32 s95, 131072
s_add_u32 s56, s22, 0
s_addc_u32 s57, s23, 0
s_and_b32 s57, s57, 0xffff
s_mov_b32 s58, 896
s_mov_b32 s59, 131072
buffer_load_dwordx4 v[128:131], v26, s[92:95], s53 offen offset:0
s_add_u32 s53, s53, 344064
buffer_load_dwordx4 v[132:135], v26, s[92:95], s53 offen offset:0
s_add_u32 s53, s53, 344064
buffer_load_dwordx4 v[136:139], v26, s[92:95], s53 offen offset:0
s_add_u32 s53, s53, 344064
buffer_load_dwordx4 v[140:143], v26, s[92:95], s53 offen offset:0
s_add_u32 s53, s53, 344064
buffer_load_dwordx4 v[144:147], v26, s[92:95], s53 offen offset:0
s_add_u32 s53, s53, 344064
buffer_load_dwordx4 v[148:151], v26, s[92:95], s53 offen offset:0
s_add_u32 s53, s53, 344064
buffer_load_dwordx4 v[152:155], v26, s[92:95], s53 offen offset:0
buffer_load_ushort v192, v27, s[56:59], 0 offen offset:0
buffer_load_ushort v193, v27, s[56:59], 0 offen offset:128
buffer_load_ushort v194, v27, s[56:59], 0 offen offset:256
buffer_load_ushort v195, v27, s[56:59], 0 offen offset:384
buffer_load_ushort v196, v27, s[56:59], 0 offen offset:512
buffer_load_ushort v197, v27, s[56:59], 0 offen offset:640
buffer_load_ushort v198, v27, s[56:59], 0 offen offset:768
s_add_u32 s60, s28, 0
s_addc_u32 s61, s29, 0
s_and_b32 s61, s61, 0xffff
s_mov_b32 s62, 67108864
s_mov_b32 s63, 131072
v_mov_b32 v27, 0
buffer_load_dwordx2 v[216:217], v27, s[60:63], 0 offen offset:0 sc1
s_add_u32 m0, s98, 0
s_add_u32 s47, s98, 0
buffer_load_dwordx4 v32, s[84:87], s47 offen offset:0 lds
buffer_load_dwordx4 v33, s[84:87], s47 offen offset:1024 lds
buffer_load_dwordx4 v34, s[84:87], s47 offen offset:2048 lds
buffer_load_dwordx4 v35, s[84:87], s47 offen offset:3072 lds
s_add_u32 m0, s98, 4096
s_add_u32 s47, s98, 4096
buffer_load_dwordx4 v36, s[84:87], s47 offen offset:0 lds
buffer_load_dwordx4 v37, s[84:87], s47 offen offset:1024 lds
buffer_load_dwordx4 v38, s[84:87], s47 offen offset:2048 lds
buffer_load_dwordx4 v39, s[84:87], s47 offen offset:3072 lds
s_add_u32 m0, s98, 8192
s_add_u32 s47, s98, 8192
buffer_load_dwordx4 v40, s[84:87], s47 offen offset:0 lds
buffer_load_dwordx4 v41, s[84:87], s47 offen offset:1024 lds
buffer_load_dwordx4 v42, s[84:87], s47 offen offset:2048 lds
buffer_load_dwordx4 v43, s[84:87], s47 offen offset:3072 lds
s_add_u32 m0, s98, 12288
s_add_u32 s47, s98, 12288
buffer_load_dwordx4 v44, s[84:87], s47 offen offset:0 lds
buffer_load_dwordx4 v45, s[84:87], s47 offen offset:1024 lds
s_cmp_lt_u32 s99, 28
s_cbranch_scc0 L_noearly%=
s_waitcnt vmcnt(14)
v_readfirstlane_b32 s76, v216
v_readfirstlane_b32 s77, v217
s_nop 3
s_add_u32 s76, s76, 1
s_addc_u32 s77, s77, 0
s_lshl_b32 s78, s76, 16
s_lshr_b64 s[52:53], s[76:77], 16
s_mov_b32 s79, s52
s_call_b64 s[30:31], L_modsub%=
L_noearly%=:
s_lshl_b32 s53, s32, 9
s_add_u32 s88, s14, s53
s_addc_u32 s89, s15, 0
s_and_b32 s89, s89, 0xffff
s_mov_b32 s90, 3670016
s_mov_b32 s91, 131072
v_lshrrev_b32 v16, 5, v15
v_and_b32 v17, 31, v15
v_lshlrev_b32 v18, 1, v16
v_xor_b32 v17, v17, v18
v_lshlrev_b32 v17, 4, v17
v_lshl_add_u32 v8, v16, 13, v17
s_mul_i32 s96, s80, 4224
s_lshl_b32 s83, s80, 16
buffer_load_dwordx4 v32, s[84:87], s47 offen offset:2048 lds
v_and_b32 v16, 15, v15
v_lshrrev_b32 v17, 4, v15
v_lshrrev_b32 v18, 1, v16
v_xor_b32 v17, v17, v18
v_mul_u32_u24 v16, 896, v16
v_add_u32 v16, s98, v16
v_lshl_add_u32 v14, v17, 4, v16
v_xor_b32 v17, 4, v17
v_lshl_add_u32 v0, v17, 4, v16
s_lshl_b32 s53, s32, 20
s_add_u32 s36, s10, s53
s_addc_u32 s37, s11, 0
s_add_u32 s36, s36, 4294967104
s_addc_u32 s37, s37, -1
s_and_b32 s37, s37, 0xffff
s_mov_b32 s38, 1048768
s_mov_b32 s39, 131072
s_add_u32 s40, s12, s53
s_addc_u32 s41, s13, 0
s_add_u32 s40, s40, 4294901568
s_addc_u32 s41, s41, -1
s_and_b32 s41, s41, 0xffff
s_mov_b32 s42, 1114304
s_mov_b32 s43, 131072
buffer_load_dwordx4 v33, s[84:87], s47 offen offset:3072 lds
v_and_b32 v16, 15, v15
v_lshrrev_b32 v17, 4, v15
v_bfe_u32 v18, v15, 2, 2
v_and_b32 v19, 3, v15
s_mul_i32 s53, s82, 17408
s_mov_b32 s54, 1088
v_lshrrev_b32 v20, 1, v16
v_mul_lo_u32 v20, v20, s54
v_and_b32 v21, 1, v16
v_lshl_add_u32 v20, v21, 9, v20
v_lshlrev_b32 v21, 1, v21
v_xor_b32 v21, v21, v17
v_lshl_add_u32 v20, v21, 4, v20
v_add_u32 v12, s53, v20
v_lshrrev_b32 v20, 1, v18
v_lshl_add_u32 v20, v17, 1, v20
v_mul_lo_u32 v20, v20, s54
v_and_b32 v21, 1, v18
v_lshl_add_u32 v20, v21, 9, v20
v_lshl_add_u32 v20, v19, 3, v20
s_add_u32 m0, s98, 16384
s_add_u32 s47, s98, 16384
buffer_load_dwordx4 v34, s[84:87], s47 offen offset:0 lds
v_add_u32 v20, s53, v20
v_add_u32 v20, 69632, v20
v_lshl_add_u32 v2, v21, 5, v20
v_xor_b32 v21, 1, v21
v_lshl_add_u32 v3, v21, 5, v20
v_lshrrev_b32 v22, 5, v15
v_and_b32 v20, 31, v15
v_lshlrev_b32 v21, 1, v22
v_xor_b32 v20, v20, v21
v_lshlrev_b32 v20, 4, v20
v_lshl_add_u32 v20, v22, 9, v20
v_add_u32 v4, 192, v20
v_add_u32 v5, 128, v20
v_add_u32 v6, 64, v20
v_add_u32 v7, 0, v20
s_mul_i32 s54, s81, 8704
s_add_u32 s45, s53, s54
s_add_u32 s46, s45, 69632
s_lshl_b32 s53, s82, 14
s_lshl_b32 s54, s81, 13
buffer_load_dwordx4 v35, s[84:87], s47 offen offset:1024 lds
s_add_u32 s44, s53, s54
s_add_u32 s49, s44, 4096
s_lshl_b32 s78, s99, 1
s_and_b32 s53, s80, 1
s_add_u32 s78, s78, s53
s_sub_u32 s79, s78, 224
s_lshr_b32 s53, s79, 2
s_and_b32 s53, s53, 63
s_mul_i32 s54, s53, 4682
s_lshr_b32 s54, s54, 16
s_mul_i32 s50, s54, 14
s_sub_u32 s50, s53, s50
s_and_b32 s55, s79, 3
s_lshl_b32 s48, s55, 2
s_add_u32 s48, s48, s54
s_mul_i32 s48, s48, 14
s_mul_i32 s54, s54, 448
s_cmp_ge_u32 s50, 7
s_cselect_b32 s53, 224, 0
s_cselect_b32 s31, 7, 0
s_cselect_b32 s30, 2, 0
s_add_u32 s54, s54, s53
s_sub_u32 s31, s50, s31
buffer_load_dwordx4 v36, s[84:87], s47 offen offset:2048 lds
s_cmp_ge_u32 s31, 3
s_cselect_b32 s50, 3, 0
s_cselect_b32 s53, 1, 0
s_sub_u32 s50, s31, s50
s_lshl_b32 s50, s50, 2
s_add_u32 s50, s50, s53
s_add_u32 s50, s50, s30
s_lshr_b32 s30, s30, 1
s_add_u32 s30, s30, 12
s_cmp_eq_u32 s31, 6
s_cselect_b32 s50, s30, s50
s_add_u32 s48, s48, s50
s_mul_i32 s48, s48, 7168
s_add_u32 s48, s48, 22577152
s_lshl_b32 s53, s31, 5
s_cmp_ge_u32 s31, 3
s_cselect_b32 s50, 16, 0
s_add_u32 s53, s53, s50
s_cmp_eq_u32 s31, 6
s_cselect_b32 s53, 96, s53
s_cselect_b32 s51, 86016, 0
s_add_u32 s54, s54, s53
s_mul_i32 s54, s54, 896
s_mul_i32 s55, s55, 224
s_add_u32 s30, s54, s55
s_and_b32 s53, s78, 15
buffer_load_dwordx4 v37, s[84:87], s47 offen offset:3072 lds
s_lshr_b32 s54, s78, 4
s_mul_i32 s55, s54, 7
s_add_u32 s50, s54, 36
s_cmp_lt_u32 s54, 6
s_cselect_b32 s55, s55, s50
s_cselect_b32 s58, 1, 8
s_mul_i32 s50, s53, 98
s_add_u32 s55, s55, s50
s_lshl_b32 s55, s55, 10
s_add_u32 s55, s55, 20971520
s_mul_i32 s54, s54, 114688
s_mul_i32 s53, s53, 224
s_add_u32 s53, s53, s54
s_cmp_lt_u32 s78, 224
s_cselect_b32 s30, s53, s30
s_cselect_b32 s48, s55, s48
s_cselect_b32 s51, 0, s51
s_cselect_b32 s58, s58, 1
s_cselect_b32 s52, 3360, 48
s_cselect_b32 s92, s18, s20
s_cselect_b32 s93, s19, s21
s_cmp_eq_u32 s52, 48
s_cselect_b32 s52, 672, s52
s_cmp_lt_u32 s78, 448
s_cselect_b32 s48, s48, 0x40000000
s_cselect_b32 s94, 1605632, 0
s_lshl_b32 s53, s33, 1
s_add_u32 s53, s53, s80
s_lshr_b32 s54, s53, 2
s_and_b32 s53, s53, 3
s_mul_i32 s55, s53, 56
s_add_u32 s55, s55, s54
s_add_u32 m0, s98, 20480
s_add_u32 s47, s98, 20480
buffer_load_dwordx4 v38, s[84:87], s47 offen offset:0 lds
s_lshl_b32 s55, s55, 10
s_mul_i32 s50, s32, 229376
s_add_u32 s55, s55, s50
s_add_u32 s55, s55, 16777216
s_mul_i32 s54, s54, 28672
s_add_u32 s54, s54, s50
s_mul_i32 s53, s53, 224
s_add_u32 s53, s53, s54
s_cmp_lt_u32 s80, 2
s_cselect_b32 s30, s53, s30
s_cselect_b32 s48, s55, s48
s_cselect_b32 s51, 0, s51
s_cselect_b32 s52, 672, s52
s_cselect_b32 s92, s16, s92
s_cselect_b32 s93, s17, s93
s_cselect_b32 s94, 3670016, s94
s_mov_b32 s78, 4
s_mov_b32 s79, 3584
s_cmp_lt_u32 s80, 2
s_cselect_b32 s50, 8, s58
s_lshl_b32 s50, s50, 10
s_add_u32 s92, s92, s30
s_addc_u32 s93, s93, 0
s_and_b32 s93, s93, 0xffff
s_sub_u32 s94, s94, s30
s_cselect_b32 s94, 0, s94
v_add_u32 v16, 0, v15
v_mul_u32_u24 v17, 4682, v16
v_lshrrev_b32 v17, 16, v17
v_lshlrev_b32 v16, 4, v16
buffer_load_dwordx4 v39, s[84:87], s47 offen offset:1024 lds
v_subrev_u32 v16, 0, v16
v_mad_u32_u24 v16, v17, s52, v16
v_accvgpr_write_b32 a40, v16
v_add_u32 v16, 64, v15
v_mul_u32_u24 v17, 4682, v16
v_lshrrev_b32 v17, 16, v17
v_lshlrev_b32 v16, 4, v16
v_subrev_u32 v16, 1024, v16
v_mad_u32_u24 v16, v17, s52, v16
v_accvgpr_write_b32 a41, v16
v_add_u32 v16, 128, v15
v_mul_u32_u24 v17, 4682, v16
v_lshrrev_b32 v17, 16, v17
v_lshlrev_b32 v16, 4, v16
v_subrev_u32 v16, 2048, v16
v_mad_u32_u24 v16, v17, s52, v16
v_accvgpr_write_b32 a42, v16
v_add_u32 v16, 192, v15
v_mul_u32_u24 v17, 4682, v16
v_lshrrev_b32 v17, 16, v17
buffer_load_dwordx4 v40, s[84:87], s47 offen offset:2048 lds
v_lshlrev_b32 v16, 4, v16
v_subrev_u32 v16, 3072, v16
v_mad_u32_u24 v16, v17, s52, v16
v_lshrrev_b32 v17, 5, v15
v_mad_u32_u24 v16, v17, s51, v16
v_accvgpr_write_b32 a43, v16
v_add_u32 v16, 256, v15
v_mul_u32_u24 v17, 4682, v16
v_lshrrev_b32 v17, 16, v17
v_lshlrev_b32 v16, 4, v16
v_subrev_u32 v16, 0, v16
v_mad_u32_u24 v16, v17, s52, v16
v_accvgpr_write_b32 a44, v16
v_add_u32 v16, 320, v15
v_mul_u32_u24 v17, 4682, v16
v_lshrrev_b32 v17, 16, v17
v_lshlrev_b32 v16, 4, v16
v_subrev_u32 v16, 1024, v16
v_mad_u32_u24 v16, v17, s52, v16
v_accvgpr_write_b32 a45, v16
buffer_load_dwordx4 v41, s[84:87], s47 offen offset:3072 lds
v_add_u32 v16, 384, v15
v_mul_u32_u24 v17, 4682, v16
v_lshrrev_b32 v17, 16, v17
v_lshlrev_b32 v16, 4, v16
v_subrev_u32 v16, 2048, v16
v_mad_u32_u24 v16, v17, s52, v16
v_accvgpr_write_b32 a46, v16
v_lshrrev_b32 v16, 4, v15
v_bfe_u32 v17, v15, 2, 2
v_mad_u32_u24 v16, v16, s78, v17
v_mul_u32_u24 v16, 224, v16
v_and_b32 v17, 3, v15
v_lshl_add_u32 v16, v17, 3, v16
s_mul_i32 s53, s80, 7168
s_add_u32 s53, s53, 34816
v_add_u32 v16, s53, v16
v_accvgpr_write_b32 a48, v16
v_add_u32 v16, s79, v16
v_accvgpr_write_b32 a49, v16
v_lshrrev_b32 v16, 4, v15
s_add_u32 m0, s98, 24576
s_add_u32 s47, s98, 24576
buffer_load_dwordx4 v42, s[84:87], s47 offen offset:0 lds
v_bfe_u32 v17, v15, 2, 2
v_and_b32 v18, 3, v15
v_mul_u32_u24 v19, 4224, v16
v_lshrrev_b32 v20, 1, v17
v_mul_u32_u24 v20, 1088, v20
v_add_u32 v19, v19, v20
v_and_b32 v20, 1, v17
v_lshl_add_u32 v19, v20, 9, v19
v_lshl_add_u32 v19, v18, 3, v19
v_add_u32 v19, 90112, v19
v_lshl_add_u32 v9, v20, 5, v19
v_xor_b32 v20, 1, v20
v_lshl_add_u32 v10, v20, 5, v19
v_lshlrev_b32 v11, 5, v16
v_add_u32 v11, 157696, v11
v_cmp_eq_u32 vcc, v16, v17
v_cmp_eq_u32 s[56:57], 1, v18
v_cmp_eq_u32 s[58:59], 2, v18
v_cndmask_b32 v13, 0, 1.0, vcc
v_cmp_eq_u32 vcc, 3, v18
buffer_load_dwordx4 v43, s[84:87], s47 offen offset:1024 lds
buffer_load_dwordx4 v44, s[84:87], s47 offen offset:2048 lds
buffer_load_dwordx4 v45, s[84:87], s47 offen offset:3072 lds
s_waitcnt vmcnt(28)
v_readfirstlane_b32 s76, v216
v_readfirstlane_b32 s77, v217
s_nop 3
s_add_u32 s76, s76, 1
s_addc_u32 s77, s77, 0
s_lshl_b32 s78, s76, 16
s_lshr_b64 s[52:53], s[76:77], 16
s_mov_b32 s79, s52
s_sub_u32 s53, s99, 28
s_cmp_lt_u32 s53, 56
s_cbranch_scc0 L_nolate%=
s_call_b64 s[30:31], L_modsub%=
L_nolate%=:
v_mov_b32 v28, 0x3f803f80
v_mov_b32 v29, 0x3f803f80
v_mov_b32 v30, 0x3f803f80
v_mov_b32 v31, 0x3f803f80
s_waitcnt vmcnt(14)
ds_read_b128 v[36:39], v14 offset:0
ds_read_b128 v[52:55], v0 offset:0
ds_read_b128 v[68:71], v14 offset:128
ds_read_b128 v[84:87], v0 offset:128
ds_read_b128 v[100:103], v14 offset:256
ds_read_b128 v[116:119], v0 offset:256
ds_read_b128 v[132:135], v14 offset:384
ds_read_b128 v[148:151], v0 offset:384
ds_read_b128 v[164:167], v14 offset:512
ds_read_b128 v[180:183], v0 offset:512
ds_read_b128 v[196:199], v14 offset:640
ds_read_b128 v[212:215], v0 offset:640
ds_read_b128 v[228:231], v14 offset:768
ds_read_b128 v[244:247], v0 offset:768
s_waitcnt lgkmcnt(13)
v_mfma_f32_16x16x32_bf16 v[16:19], v[28:31], v[36:39], 0
v_mfma_f32_16x16x32_bf16 a[0:3], v[36:39], v[36:39], 0
s_waitcnt lgkmcnt(12)
v_mfma_f32_16x16x32_bf16 v[16:19], v[28:31], v[52:55], v[16:19]
v_lshlrev_b32 v32, 16, v36
v_and_b32 v33, s97, v36
v_mfma_f32_16x16x32_bf16 a[0:3], v[52:55], v[52:55], a[0:3]
v_lshlrev_b32 v34, 16, v37
v_and_b32 v35, s97, v37
s_waitcnt lgkmcnt(11)
v_mfma_f32_16x16x32_bf16 v[16:19], v[28:31], v[68:71], v[16:19]
v_lshlrev_b32 v36, 16, v38
v_and_b32 v37, s97, v38
v_mfma_f32_16x16x32_bf16 a[0:3], v[68:71], v[68:71], a[0:3]
v_lshlrev_b32 v38, 16, v39
v_and_b32 v39, s97, v39
s_waitcnt lgkmcnt(10)
v_mfma_f32_16x16x32_bf16 v[16:19], v[28:31], v[84:87], v[16:19]
v_lshlrev_b32 v48, 16, v52
v_and_b32 v49, s97, v52
v_mfma_f32_16x16x32_bf16 a[0:3], v[84:87], v[84:87], a[0:3]
v_lshlrev_b32 v50, 16, v53
v_and_b32 v51, s97, v53
s_waitcnt lgkmcnt(9)
v_mfma_f32_16x16x32_bf16 v[16:19], v[28:31], v[100:103], v[16:19]
v_lshlrev_b32 v52, 16, v54
v_and_b32 v53, s97, v54
v_mfma_f32_16x16x32_bf16 a[0:3], v[100:103], v[100:103], a[0:3]
v_lshlrev_b32 v54, 16, v55
v_and_b32 v55, s97, v55
s_waitcnt lgkmcnt(8)
v_mfma_f32_16x16x32_bf16 v[16:19], v[28:31], v[116:119], v[16:19]
v_lshlrev_b32 v64, 16, v68
v_and_b32 v65, s97, v68
v_mfma_f32_16x16x32_bf16 a[0:3], v[116:119], v[116:119], a[0:3]
v_lshlrev_b32 v66, 16, v69
v_and_b32 v67, s97, v69
s_waitcnt lgkmcnt(7)
v_mfma_f32_16x16x32_bf16 v[16:19], v[28:31], v[132:135], v[16:19]
v_lshlrev_b32 v68, 16, v70
v_and_b32 v69, s97, v70
v_mfma_f32_16x16x32_bf16 a[0:3], v[132:135], v[132:135], a[0:3]
v_lshlrev_b32 v70, 16, v71
v_and_b32 v71, s97, v71
s_waitcnt lgkmcnt(6)
v_mfma_f32_16x16x32_bf16 v[16:19], v[28:31], v[148:151], v[16:19]
v_lshlrev_b32 v80, 16, v84
v_and_b32 v81, s97, v84
v_mfma_f32_16x16x32_bf16 a[0:3], v[148:151], v[148:151], a[0:3]
v_lshlrev_b32 v82, 16, v85
v_and_b32 v83, s97, v85
s_waitcnt lgkmcnt(5)
v_mfma_f32_16x16x32_bf16 v[16:19], v[28:31], v[164:167], v[16:19]
v_lshlrev_b32 v84, 16, v86
v_and_b32 v85, s97, v86
v_mfma_f32_16x16x32_bf16 a[0:3], v[164:167], v[164:167], a[0:3]
v_lshlrev_b32 v86, 16, v87
v_and_b32 v87, s97, v87
s_waitcnt lgkmcnt(4)
v_mfma_f32_16x16x32_bf16 v[16:19], v[28:31], v[180:183], v[16:19]
v_lshlrev_b32 v96, 16, v100
v_and_b32 v97, s97, v100
v_mfma_f32_16x16x32_bf16 a[0:3], v[180:183], v[180:183], a[0:3]
v_lshlrev_b32 v98, 16, v101
v_and_b32 v99, s97, v101
s_waitcnt lgkmcnt(3)
v_mfma_f32_16x16x32_bf16 v[16:19], v[28:31], v[196:199], v[16:19]
v_lshlrev_b32 v100, 16, v102
v_and_b32 v101, s97, v102
v_mfma_f32_16x16x32_bf16 a[0:3], v[196:199], v[196:199], a[0:3]
v_lshlrev_b32 v102, 16, v103
v_and_b32 v103, s97, v103
s_waitcnt lgkmcnt(2)
v_mfma_f32_16x16x32_bf16 v[16:19], v[28:31], v[212:215], v[16:19]
v_lshlrev_b32 v112, 16, v116
v_and_b32 v113, s97, v116
v_mfma_f32_16x16x32_bf16 a[0:3], v[212:215], v[212:215], a[0:3]
v_lshlrev_b32 v114, 16, v117
v_and_b32 v115, s97, v117
s_waitcnt lgkmcnt(1)
v_mfma_f32_16x16x32_bf16 v[16:19], v[28:31], v[228:231], v[16:19]
v_lshlrev_b32 v116, 16, v118
v_and_b32 v117, s97, v118
v_mfma_f32_16x16x32_bf16 a[0:3], v[228:231], v[228:231], a[0:3]
v_lshlrev_b32 v118, 16, v119
v_and_b32 v119, s97, v119
s_waitcnt lgkmcnt(0)
v_mfma_f32_16x16x32_bf16 v[16:19], v[28:31], v[244:247], v[16:19]
v_lshlrev_b32 v128, 16, v132
v_and_b32 v129, s97, v132
v_mfma_f32_16x16x32_bf16 a[0:3], v[244:247], v[244:247], a[0:3]
v_lshlrev_b32 v130, 16, v133
v_and_b32 v131, s97, v133
s_waitcnt vmcnt(0)
ds_read_b128 v[44:47], v14 offset:14336
ds_read_b128 v[60:63], v0 offset:14336
ds_read_b128 v[76:79], v14 offset:14464
ds_read_b128 v[92:95], v0 offset:14464
ds_read_b128 v[108:111], v14 offset:14592
ds_read_b128 v[124:127], v0 offset:14592
ds_read_b128 v[140:143], v14 offset:14720
ds_read_b128 v[156:159], v0 offset:14720
ds_read_b128 v[172:175], v14 offset:14848
ds_read_b128 v[188:191], v0 offset:14848
ds_read_b128 v[204:207], v14 offset:14976
ds_read_b128 v[220:223], v0 offset:14976
ds_read_b128 v[236:239], v14 offset:15104
ds_read_b128 v[252:255], v0 offset:15104
s_waitcnt lgkmcnt(13)
v_mfma_f32_16x16x32_bf16 v[20:23], v[28:31], v[44:47], 0
v_lshlrev_b32 v132, 16, v134
v_and_b32 v133, s97, v134
v_mfma_f32_16x16x32_bf16 a[4:7], v[44:47], v[44:47], 0
v_lshlrev_b32 v134, 16, v135
v_and_b32 v135, s97, v135
s_waitcnt lgkmcnt(12)
v_mfma_f32_16x16x32_bf16 v[20:23], v[28:31], v[60:63], v[20:23]
v_lshlrev_b32 v144, 16, v148
v_and_b32 v145, s97, v148
v_mfma_f32_16x16x32_bf16 a[4:7], v[60:63], v[60:63], a[4:7]
v_lshlrev_b32 v146, 16, v149
v_and_b32 v147, s97, v149
s_add_u32 m0, s96, 140800
s_nop 0
buffer_load_dwordx4 v8, s[88:91], s83 offen offset:0 lds
s_waitcnt lgkmcnt(11)
v_mfma_f32_16x16x32_bf16 v[20:23], v[28:31], v[76:79], v[20:23]
v_lshlrev_b32 v148, 16, v150
v_and_b32 v149, s97, v150
v_mfma_f32_16x16x32_bf16 a[4:7], v[76:79], v[76:79], a[4:7]
v_lshlrev_b32 v150, 16, v151
v_and_b32 v151, s97, v151
s_add_u32 s83, s83, 15296
buffer_load_dwordx4 v8, s[88:91], s83 offen offset:1088 lds
s_waitcnt lgkmcnt(10)
v_mfma_f32_16x16x32_bf16 v[20:23], v[28:31], v[92:95], v[20:23]
v_lshlrev_b32 v160, 16, v164
v_and_b32 v161, s97, v164
v_mfma_f32_16x16x32_bf16 a[4:7], v[92:95], v[92:95], a[4:7]
v_lshlrev_b32 v162, 16, v165
v_and_b32 v163, s97, v165
s_add_u32 s83, s83, 15360
buffer_load_dwordx4 v8, s[88:91], s83 offen offset:2112 lds
s_waitcnt lgkmcnt(9)
v_mfma_f32_16x16x32_bf16 v[20:23], v[28:31], v[108:111], v[20:23]
v_lshlrev_b32 v164, 16, v166
v_and_b32 v165, s97, v166
v_mfma_f32_16x16x32_bf16 a[4:7], v[108:111], v[108:111], a[4:7]
v_lshlrev_b32 v166, 16, v167
v_and_b32 v167, s97, v167
s_add_u32 s83, s83, 15296
buffer_load_dwordx4 v8, s[88:91], s83 offen offset:3200 lds
s_add_u32 s83, s83, 216192
s_waitcnt lgkmcnt(8)
v_mfma_f32_16x16x32_bf16 v[20:23], v[28:31], v[124:127], v[20:23]
v_lshlrev_b32 v176, 16, v180
v_and_b32 v177, s97, v180
v_mfma_f32_16x16x32_bf16 a[4:7], v[124:127], v[124:127], a[4:7]
v_lshlrev_b32 v178, 16, v181
v_and_b32 v179, s97, v181
s_add_u32 m0, s96, 123904
s_nop 0
buffer_load_dwordx4 v8, s[88:91], s83 offen offset:0 lds
s_waitcnt lgkmcnt(7)
v_mfma_f32_16x16x32_bf16 v[20:23], v[28:31], v[140:143], v[20:23]
v_lshlrev_b32 v180, 16, v182
v_and_b32 v181, s97, v182
v_mfma_f32_16x16x32_bf16 a[4:7], v[140:143], v[140:143], a[4:7]
v_lshlrev_b32 v182, 16, v183
v_and_b32 v183, s97, v183
s_add_u32 s83, s83, 15296
buffer_load_dwordx4 v8, s[88:91], s83 offen offset:1088 lds
s_waitcnt lgkmcnt(6)
v_mfma_f32_16x16x32_bf16 v[20:23], v[28:31], v[156:159], v[20:23]
v_lshlrev_b32 v192, 16, v196
v_and_b32 v193, s97, v196
v_mfma_f32_16x16x32_bf16 a[4:7], v[156:159], v[156:159], a[4:7]
v_lshlrev_b32 v194, 16, v197
v_and_b32 v195, s97, v197
s_add_u32 s83, s83, 15360
buffer_load_dwordx4 v8, s[88:91], s83 offen offset:2112 lds
s_waitcnt lgkmcnt(5)
v_mfma_f32_16x16x32_bf16 v[20:23], v[28:31], v[172:175], v[20:23]
v_lshlrev_b32 v196, 16, v198
v_and_b32 v197, s97, v198
v_mfma_f32_16x16x32_bf16 a[4:7], v[172:175], v[172:175], a[4:7]
v_lshlrev_b32 v198, 16, v199
v_and_b32 v199, s97, v199
s_add_u32 s83, s83, 15296
buffer_load_dwordx4 v8, s[88:91], s83 offen offset:3200 lds
s_add_u32 s83, s83, 216192
s_waitcnt lgkmcnt(4)
v_mfma_f32_16x16x32_bf16 v[20:23], v[28:31], v[188:191], v[20:23]
v_lshlrev_b32 v208, 16, v212
v_and_b32 v209, s97, v212
v_mfma_f32_16x16x32_bf16 a[4:7], v[188:191], v[188:191], a[4:7]
v_lshlrev_b32 v210, 16, v213
v_and_b32 v211, s97, v213
s_waitcnt lgkmcnt(3)
v_mfma_f32_16x16x32_bf16 v[20:23], v[28:31], v[204:207], v[20:23]
v_lshlrev_b32 v212, 16, v214
v_and_b32 v213, s97, v214
v_mfma_f32_16x16x32_bf16 a[4:7], v[204:207], v[204:207], a[4:7]
v_lshlrev_b32 v214, 16, v215
v_and_b32 v215, s97, v215
s_waitcnt lgkmcnt(2)
v_mfma_f32_16x16x32_bf16 v[20:23], v[28:31], v[220:223], v[20:23]
v_lshlrev_b32 v224, 16, v228
v_and_b32 v225, s97, v228
v_mfma_f32_16x16x32_bf16 a[4:7], v[220:223], v[220:223], a[4:7]
v_lshlrev_b32 v226, 16, v229
v_and_b32 v227, s97, v229
s_waitcnt lgkmcnt(1)
v_mfma_f32_16x16x32_bf16 v[20:23], v[28:31], v[236:239], v[20:23]
v_lshlrev_b32 v228, 16, v230
v_and_b32 v229, s97, v230
v_mfma_f32_16x16x32_bf16 a[4:7], v[236:239], v[236:239], a[4:7]
v_lshlrev_b32 v230, 16, v231
v_and_b32 v231, s97, v231
s_waitcnt lgkmcnt(0)
v_mfma_f32_16x16x32_bf16 v[20:23], v[28:31], v[252:255], v[20:23]
v_lshlrev_b32 v240, 16, v244
v_and_b32 v241, s97, v244
v_mfma_f32_16x16x32_bf16 a[4:7], v[252:255], v[252:255], a[4:7]
v_lshlrev_b32 v242, 16, v245
v_and_b32 v243, s97, v245
v_mov_b32 v0, v24
v_min_u32 v14, 223, v24
v_lshlrev_b32 v14, 5, v14
s_mov_b32 s53, 4096
buffer_load_dwordx4 a[8:11], v14, s[60:63], s53 offen offset:0 sc1
buffer_load_dwordx4 a[12:15], v14, s[60:63], s53 offen offset:16 sc1
v_lshlrev_b32 v244, 16, v246
v_and_b32 v245, s97, v246
v_lshlrev_b32 v246, 16, v247
v_and_b32 v247, s97, v247
v_lshlrev_b32 v40, 16, v44
v_and_b32 v41, s97, v44
v_lshlrev_b32 v42, 16, v45
v_and_b32 v43, s97, v45
v_lshlrev_b32 v44, 16, v46
v_and_b32 v45, s97, v46
v_lshlrev_b32 v46, 16, v47
v_and_b32 v47, s97, v47
v_lshlrev_b32 v56, 16, v60
v_and_b32 v57, s97, v60
v_lshlrev_b32 v58, 16, v61
v_and_b32 v59, s97, v61
v_lshlrev_b32 v60, 16, v62
v_and_b32 v61, s97, v62
v_lshlrev_b32 v62, 16, v63
v_and_b32 v63, s97, v63
v_lshlrev_b32 v72, 16, v76
v_and_b32 v73, s97, v76
v_lshlrev_b32 v74, 16, v77
v_and_b32 v75, s97, v77
v_lshlrev_b32 v76, 16, v78
v_and_b32 v77, s97, v78
v_lshlrev_b32 v78, 16, v79
v_and_b32 v79, s97, v79
v_lshlrev_b32 v88, 16, v92
v_and_b32 v89, s97, v92
v_lshlrev_b32 v90, 16, v93
v_and_b32 v91, s97, v93
v_lshlrev_b32 v92, 16, v94
v_and_b32 v93, s97, v94
v_lshlrev_b32 v94, 16, v95
v_and_b32 v95, s97, v95
v_lshlrev_b32 v104, 16, v108
v_and_b32 v105, s97, v108
v_lshlrev_b32 v106, 16, v109
v_and_b32 v107, s97, v109
v_lshlrev_b32 v108, 16, v110
v_and_b32 v109, s97, v110
v_lshlrev_b32 v110, 16, v111
v_and_b32 v111, s97, v111
v_lshlrev_b32 v120, 16, v124
v_and_b32 v121, s97, v124
v_lshlrev_b32 v122, 16, v125
v_and_b32 v123, s97, v125
v_lshlrev_b32 v124, 16, v126
v_and_b32 v125, s97, v126
v_lshlrev_b32 v126, 16, v127
v_and_b32 v127, s97, v127
v_lshlrev_b32 v136, 16, v140
v_and_b32 v137, s97, v140
v_lshlrev_b32 v138, 16, v141
v_and_b32 v139, s97, v141
v_lshlrev_b32 v140, 16, v142
v_and_b32 v141, s97, v142
v_lshlrev_b32 v142, 16, v143
v_and_b32 v143, s97, v143
v_lshlrev_b32 v152, 16, v156
v_and_b32 v153, s97, v156
v_lshlrev_b32 v154, 16, v157
v_and_b32 v155, s97, v157
v_lshlrev_b32 v156, 16, v158
v_and_b32 v157, s97, v158
v_lshlrev_b32 v158, 16, v159
v_and_b32 v159, s97, v159
v_lshlrev_b32 v168, 16, v172
v_and_b32 v169, s97, v172
v_lshlrev_b32 v170, 16, v173
v_and_b32 v171, s97, v173
v_lshlrev_b32 v172, 16, v174
v_and_b32 v173, s97, v174
v_lshlrev_b32 v174, 16, v175
v_and_b32 v175, s97, v175
v_lshlrev_b32 v184, 16, v188
v_and_b32 v185, s97, v188
v_lshlrev_b32 v186, 16, v189
v_and_b32 v187, s97, v189
v_lshlrev_b32 v188, 16, v190
v_and_b32 v189, s97, v190
v_lshlrev_b32 v190, 16, v191
v_and_b32 v191, s97, v191
v_lshlrev_b32 v200, 16, v204
v_and_b32 v201, s97, v204
v_lshlrev_b32 v202, 16, v205
v_and_b32 v203, s97, v205
v_lshlrev_b32 v204, 16, v206
v_and_b32 v205, s97, v206
v_lshlrev_b32 v206, 16, v207
v_and_b32 v207, s97, v207
v_lshlrev_b32 v216, 16, v220
v_and_b32 v217, s97, v220
v_lshlrev_b32 v218, 16, v221
v_and_b32 v219, s97, v221
v_lshlrev_b32 v220, 16, v222
v_and_b32 v221, s97, v222
v_lshlrev_b32 v222, 16, v223
v_and_b32 v223, s97, v223
v_lshlrev_b32 v232, 16, v236
v_and_b32 v233, s97, v236
v_lshlrev_b32 v234, 16, v237
v_and_b32 v235, s97, v237
v_lshlrev_b32 v236, 16, v238
v_and_b32 v237, s97, v238
v_lshlrev_b32 v238, 16, v239
v_and_b32 v239, s97, v239
v_lshlrev_b32 v248, 16, v252
v_and_b32 v249, s97, v252
v_lshlrev_b32 v250, 16, v253
v_and_b32 v251, s97, v253
v_lshlrev_b32 v252, 16, v254
v_and_b32 v253, s97, v254
v_lshlrev_b32 v254, 16, v255
v_and_b32 v255, s97, v255
v_accvgpr_read_b32 v17, a0
v_accvgpr_read_b32 v18, a1
v_accvgpr_read_b32 v19, a2
v_accvgpr_read_b32 v26, a3
v_cndmask_b32 v17, v17, v18, s[56:57]
v_cndmask_b32 v17, v17, v19, s[58:59]
v_cndmask_b32 v17, v17, v26, vcc
v_mul_f32 v17, v17, v13
v_mul_f32 v16, 0x3b124925, v16
v_accvgpr_read_b32 v21, a4
v_accvgpr_read_b32 v22, a5
v_accvgpr_read_b32 v23, a6
v_accvgpr_read_b32 v30, a7
v_cndmask_b32 v21, v21, v22, s[56:57]
v_cndmask_b32 v21, v21, v23, s[58:59]
v_cndmask_b32 v21, v21, v30, vcc
v_mul_f32 v21, v21, v13
v_mul_f32 v20, 0x3b124925, v20
v_mov_b32 v18, v17
v_mov_b32 v22, v21
s_nop 0
v_permlane32_swap_b32 v17, v18
v_permlane32_swap_b32 v21, v22
s_nop 0
v_add_f32 v17, v17, v18
v_add_f32 v21, v21, v22
v_mov_b32 v18, v17
v_mov_b32 v22, v21
s_nop 0
v_permlane16_swap_b32 v17, v18
v_permlane16_swap_b32 v21, v22
s_nop 0
v_add_f32 v17, v17, v18
v_add_f32 v21, v21, v22
v_mul_f32 v17, 0x3b124925, v17
v_fma_f32 v17, -v16, v16, v17
v_max_f32 v17, 0, v17
v_add_f32 v17, 0x3727c5ac, v17
v_mul_f32 v21, 0x3b124925, v21
v_fma_f32 v21, -v20, v20, v21
v_max_f32 v21, 0, v21
v_add_f32 v21, 0x3727c5ac, v21
v_rsq_f32 v24, v17
v_rsq_f32 v28, v21
s_nop 0
v_mul_f32 v25, v16, v24
v_mul_f32 v29, v20, v28
v_pk_fma_f32 v[240:241], v[240:241], v[24:25], v[24:25] op_sel:[0,0,1] op_sel_hi:[1,0,1] neg_lo:[0,0,1] neg_hi:[0,0,1]
v_pk_fma_f32 v[242:243], v[242:243], v[24:25], v[24:25] op_sel:[0,0,1] op_sel_hi:[1,0,1] neg_lo:[0,0,1] neg_hi:[0,0,1]
v_pk_fma_f32 v[244:245], v[244:245], v[24:25], v[24:25] op_sel:[0,0,1] op_sel_hi:[1,0,1] neg_lo:[0,0,1] neg_hi:[0,0,1]
v_pk_fma_f32 v[246:247], v[246:247], v[24:25], v[24:25] op_sel:[0,0,1] op_sel_hi:[1,0,1] neg_lo:[0,0,1] neg_hi:[0,0,1]
v_pk_fma_f32 v[248:249], v[248:249], v[28:29], v[28:29] op_sel:[0,0,1] op_sel_hi:[1,0,1] neg_lo:[0,0,1] neg_hi:[0,0,1]
v_pk_fma_f32 v[250:251], v[250:251], v[28:29], v[28:29] op_sel:[0,0,1] op_sel_hi:[1,0,1] neg_lo:[0,0,1] neg_hi:[0,0,1]
v_pk_fma_f32 v[252:253], v[252:253], v[28:29], v[28:29] op_sel:[0,0,1] op_sel_hi:[1,0,1] neg_lo:[0,0,1] neg_hi:[0,0,1]
v_pk_fma_f32 v[254:255], v[254:255], v[28:29], v[28:29] op_sel:[0,0,1] op_sel_hi:[1,0,1] neg_lo:[0,0,1] neg_hi:[0,0,1]
v_pk_fma_f32 v[224:225], v[224:225], v[24:25], v[24:25] op_sel:[0,0,1] op_sel_hi:[1,0,1] neg_lo:[0,0,1] neg_hi:[0,0,1]
v_pk_fma_f32 v[226:227], v[226:227], v[24:25], v[24:25] op_sel:[0,0,1] op_sel_hi:[1,0,1] neg_lo:[0,0,1] neg_hi:[0,0,1]
v_pk_fma_f32 v[228:229], v[228:229], v[24:25], v[24:25] op_sel:[0,0,1] op_sel_hi:[1,0,1] neg_lo:[0,0,1] neg_hi:[0,0,1]
v_pk_fma_f32 v[230:231], v[230:231], v[24:25], v[24:25] op_sel:[0,0,1] op_sel_hi:[1,0,1] neg_lo:[0,0,1] neg_hi:[0,0,1]
v_pk_fma_f32 v[232:233], v[232:233], v[28:29], v[28:29] op_sel:[0,0,1] op_sel_hi:[1,0,1] neg_lo:[0,0,1] neg_hi:[0,0,1]
v_pk_fma_f32 v[234:235], v[234:235], v[28:29], v[28:29] op_sel:[0,0,1] op_sel_hi:[1,0,1] neg_lo:[0,0,1] neg_hi:[0,0,1]
v_pk_fma_f32 v[236:237], v[236:237], v[28:29], v[28:29] op_sel:[0,0,1] op_sel_hi:[1,0,1] neg_lo:[0,0,1] neg_hi:[0,0,1]
v_pk_fma_f32 v[238:239], v[238:239], v[28:29], v[28:29] op_sel:[0,0,1] op_sel_hi:[1,0,1] neg_lo:[0,0,1] neg_hi:[0,0,1]
v_pk_fma_f32 v[208:209], v[208:209], v[24:25], v[24:25] op_sel:[0,0,1] op_sel_hi:[1,0,1] neg_lo:[0,0,1] neg_hi:[0,0,1]
v_pk_fma_f32 v[210:211], v[210:211], v[24:25], v[24:25] op_sel:[0,0,1] op_sel_hi:[1,0,1] neg_lo:[0,0,1] neg_hi:[0,0,1]
v_pk_fma_f32 v[212:213], v[212:213], v[24:25], v[24:25] op_sel:[0,0,1] op_sel_hi:[1,0,1] neg_lo:[0,0,1] neg_hi:[0,0,1]
v_pk_fma_f32 v[214:215], v[214:215], v[24:25], v[24:25] op_sel:[0,0,1] op_sel_hi:[1,0,1] neg_lo:[0,0,1] neg_hi:[0,0,1]
v_pk_fma_f32 v[216:217], v[216:217], v[28:29], v[28:29] op_sel:[0,0,1] op_sel_hi:[1,0,1] neg_lo:[0,0,1] neg_hi:[0,0,1]
v_pk_fma_f32 v[218:219], v[218:219], v[28:29], v[28:29] op_sel:[0,0,1] op_sel_hi:[1,0,1] neg_lo:[0,0,1] neg_hi:[0,0,1]
v_pk_fma_f32 v[220:221], v[220:221], v[28:29], v[28:29] op_sel:[0,0,1] op_sel_hi:[1,0,1] neg_lo:[0,0,1] neg_hi:[0,0,1]
v_pk_fma_f32 v[222:223], v[222:223], v[28:29], v[28:29] op_sel:[0,0,1] op_sel_hi:[1,0,1] neg_lo:[0,0,1] neg_hi:[0,0,1]
v_pk_fma_f32 v[192:193], v[192:193], v[24:25], v[24:25] op_sel:[0,0,1] op_sel_hi:[1,0,1] neg_lo:[0,0,1] neg_hi:[0,0,1]
v_pk_fma_f32 v[194:195], v[194:195], v[24:25], v[24:25] op_sel:[0,0,1] op_sel_hi:[1,0,1] neg_lo:[0,0,1] neg_hi:[0,0,1]
v_pk_fma_f32 v[196:197], v[196:197], v[24:25], v[24:25] op_sel:[0,0,1] op_sel_hi:[1,0,1] neg_lo:[0,0,1] neg_hi:[0,0,1]
v_pk_fma_f32 v[198:199], v[198:199], v[24:25], v[24:25] op_sel:[0,0,1] op_sel_hi:[1,0,1] neg_lo:[0,0,1] neg_hi:[0,0,1]
v_pk_fma_f32 v[200:201], v[200:201], v[28:29], v[28:29] op_sel:[0,0,1] op_sel_hi:[1,0,1] neg_lo:[0,0,1] neg_hi:[0,0,1]
v_pk_fma_f32 v[202:203], v[202:203], v[28:29], v[28:29] op_sel:[0,0,1] op_sel_hi:[1,0,1] neg_lo:[0,0,1] neg_hi:[0,0,1]
v_pk_fma_f32 v[204:205], v[204:205], v[28:29], v[28:29] op_sel:[0,0,1] op_sel_hi:[1,0,1] neg_lo:[0,0,1] neg_hi:[0,0,1]
v_pk_fma_f32 v[206:207], v[206:207], v[28:29], v[28:29] op_sel:[0,0,1] op_sel_hi:[1,0,1] neg_lo:[0,0,1] neg_hi:[0,0,1]
v_pk_fma_f32 v[176:177], v[176:177], v[24:25], v[24:25] op_sel:[0,0,1] op_sel_hi:[1,0,1] neg_lo:[0,0,1] neg_hi:[0,0,1]
v_pk_fma_f32 v[178:179], v[178:179], v[24:25], v[24:25] op_sel:[0,0,1] op_sel_hi:[1,0,1] neg_lo:[0,0,1] neg_hi:[0,0,1]
v_pk_fma_f32 v[180:181], v[180:181], v[24:25], v[24:25] op_sel:[0,0,1] op_sel_hi:[1,0,1] neg_lo:[0,0,1] neg_hi:[0,0,1]
v_pk_fma_f32 v[182:183], v[182:183], v[24:25], v[24:25] op_sel:[0,0,1] op_sel_hi:[1,0,1] neg_lo:[0,0,1] neg_hi:[0,0,1]
v_pk_fma_f32 v[184:185], v[184:185], v[28:29], v[28:29] op_sel:[0,0,1] op_sel_hi:[1,0,1] neg_lo:[0,0,1] neg_hi:[0,0,1]
v_pk_fma_f32 v[186:187], v[186:187], v[28:29], v[28:29] op_sel:[0,0,1] op_sel_hi:[1,0,1] neg_lo:[0,0,1] neg_hi:[0,0,1]
v_pk_fma_f32 v[188:189], v[188:189], v[28:29], v[28:29] op_sel:[0,0,1] op_sel_hi:[1,0,1] neg_lo:[0,0,1] neg_hi:[0,0,1]
v_pk_fma_f32 v[190:191], v[190:191], v[28:29], v[28:29] op_sel:[0,0,1] op_sel_hi:[1,0,1] neg_lo:[0,0,1] neg_hi:[0,0,1]
v_pk_fma_f32 v[160:161], v[160:161], v[24:25], v[24:25] op_sel:[0,0,1] op_sel_hi:[1,0,1] neg_lo:[0,0,1] neg_hi:[0,0,1]
v_pk_fma_f32 v[162:163], v[162:163], v[24:25], v[24:25] op_sel:[0,0,1] op_sel_hi:[1,0,1] neg_lo:[0,0,1] neg_hi:[0,0,1]
v_pk_fma_f32 v[164:165], v[164:165], v[24:25], v[24:25] op_sel:[0,0,1] op_sel_hi:[1,0,1] neg_lo:[0,0,1] neg_hi:[0,0,1]
v_pk_fma_f32 v[166:167], v[166:167], v[24:25], v[24:25] op_sel:[0,0,1] op_sel_hi:[1,0,1] neg_lo:[0,0,1] neg_hi:[0,0,1]
v_pk_fma_f32 v[168:169], v[168:169], v[28:29], v[28:29] op_sel:[0,0,1] op_sel_hi:[1,0,1] neg_lo:[0,0,1] neg_hi:[0,0,1]
v_pk_fma_f32 v[170:171], v[170:171], v[28:29], v[28:29] op_sel:[0,0,1] op_sel_hi:[1,0,1] neg_lo:[0,0,1] neg_hi:[0,0,1]
v_pk_fma_f32 v[172:173], v[172:173], v[28:29], v[28:29] op_sel:[0,0,1] op_sel_hi:[1,0,1] neg_lo:[0,0,1] neg_hi:[0,0,1]
v_pk_fma_f32 v[174:175], v[174:175], v[28:29], v[28:29] op_sel:[0,0,1] op_sel_hi:[1,0,1] neg_lo:[0,0,1] neg_hi:[0,0,1]
v_pk_fma_f32 v[144:145], v[144:145], v[24:25], v[24:25] op_sel:[0,0,1] op_sel_hi:[1,0,1] neg_lo:[0,0,1] neg_hi:[0,0,1]
v_pk_fma_f32 v[146:147], v[146:147], v[24:25], v[24:25] op_sel:[0,0,1] op_sel_hi:[1,0,1] neg_lo:[0,0,1] neg_hi:[0,0,1]
v_pk_fma_f32 v[148:149], v[148:149], v[24:25], v[24:25] op_sel:[0,0,1] op_sel_hi:[1,0,1] neg_lo:[0,0,1] neg_hi:[0,0,1]
v_pk_fma_f32 v[150:151], v[150:151], v[24:25], v[24:25] op_sel:[0,0,1] op_sel_hi:[1,0,1] neg_lo:[0,0,1] neg_hi:[0,0,1]
v_pk_fma_f32 v[152:153], v[152:153], v[28:29], v[28:29] op_sel:[0,0,1] op_sel_hi:[1,0,1] neg_lo:[0,0,1] neg_hi:[0,0,1]
v_pk_fma_f32 v[154:155], v[154:155], v[28:29], v[28:29] op_sel:[0,0,1] op_sel_hi:[1,0,1] neg_lo:[0,0,1] neg_hi:[0,0,1]
v_pk_fma_f32 v[156:157], v[156:157], v[28:29], v[28:29] op_sel:[0,0,1] op_sel_hi:[1,0,1] neg_lo:[0,0,1] neg_hi:[0,0,1]
v_pk_fma_f32 v[158:159], v[158:159], v[28:29], v[28:29] op_sel:[0,0,1] op_sel_hi:[1,0,1] neg_lo:[0,0,1] neg_hi:[0,0,1]
v_pk_fma_f32 v[128:129], v[128:129], v[24:25], v[24:25] op_sel:[0,0,1] op_sel_hi:[1,0,1] neg_lo:[0,0,1] neg_hi:[0,0,1]
v_pk_fma_f32 v[130:131], v[130:131], v[24:25], v[24:25] op_sel:[0,0,1] op_sel_hi:[1,0,1] neg_lo:[0,0,1] neg_hi:[0,0,1]
v_pk_fma_f32 v[132:133], v[132:133], v[24:25], v[24:25] op_sel:[0,0,1] op_sel_hi:[1,0,1] neg_lo:[0,0,1] neg_hi:[0,0,1]
v_pk_fma_f32 v[134:135], v[134:135], v[24:25], v[24:25] op_sel:[0,0,1] op_sel_hi:[1,0,1] neg_lo:[0,0,1] neg_hi:[0,0,1]
v_pk_fma_f32 v[136:137], v[136:137], v[28:29], v[28:29] op_sel:[0,0,1] op_sel_hi:[1,0,1] neg_lo:[0,0,1] neg_hi:[0,0,1]
v_pk_fma_f32 v[138:139], v[138:139], v[28:29], v[28:29] op_sel:[0,0,1] op_sel_hi:[1,0,1] neg_lo:[0,0,1] neg_hi:[0,0,1]
v_pk_fma_f32 v[140:141], v[140:141], v[28:29], v[28:29] op_sel:[0,0,1] op_sel_hi:[1,0,1] neg_lo:[0,0,1] neg_hi:[0,0,1]
v_pk_fma_f32 v[142:143], v[142:143], v[28:29], v[28:29] op_sel:[0,0,1] op_sel_hi:[1,0,1] neg_lo:[0,0,1] neg_hi:[0,0,1]
v_pk_fma_f32 v[112:113], v[112:113], v[24:25], v[24:25] op_sel:[0,0,1] op_sel_hi:[1,0,1] neg_lo:[0,0,1] neg_hi:[0,0,1]
v_pk_fma_f32 v[114:115], v[114:115], v[24:25], v[24:25] op_sel:[0,0,1] op_sel_hi:[1,0,1] neg_lo:[0,0,1] neg_hi:[0,0,1]
v_pk_fma_f32 v[116:117], v[116:117], v[24:25], v[24:25] op_sel:[0,0,1] op_sel_hi:[1,0,1] neg_lo:[0,0,1] neg_hi:[0,0,1]
v_pk_fma_f32 v[118:119], v[118:119], v[24:25], v[24:25] op_sel:[0,0,1] op_sel_hi:[1,0,1] neg_lo:[0,0,1] neg_hi:[0,0,1]
v_pk_fma_f32 v[120:121], v[120:121], v[28:29], v[28:29] op_sel:[0,0,1] op_sel_hi:[1,0,1] neg_lo:[0,0,1] neg_hi:[0,0,1]
v_pk_fma_f32 v[122:123], v[122:123], v[28:29], v[28:29] op_sel:[0,0,1] op_sel_hi:[1,0,1] neg_lo:[0,0,1] neg_hi:[0,0,1]
v_pk_fma_f32 v[124:125], v[124:125], v[28:29], v[28:29] op_sel:[0,0,1] op_sel_hi:[1,0,1] neg_lo:[0,0,1] neg_hi:[0,0,1]
v_pk_fma_f32 v[126:127], v[126:127], v[28:29], v[28:29] op_sel:[0,0,1] op_sel_hi:[1,0,1] neg_lo:[0,0,1] neg_hi:[0,0,1]
v_pk_fma_f32 v[96:97], v[96:97], v[24:25], v[24:25] op_sel:[0,0,1] op_sel_hi:[1,0,1] neg_lo:[0,0,1] neg_hi:[0,0,1]
v_pk_fma_f32 v[98:99], v[98:99], v[24:25], v[24:25] op_sel:[0,0,1] op_sel_hi:[1,0,1] neg_lo:[0,0,1] neg_hi:[0,0,1]
v_pk_fma_f32 v[100:101], v[100:101], v[24:25], v[24:25] op_sel:[0,0,1] op_sel_hi:[1,0,1] neg_lo:[0,0,1] neg_hi:[0,0,1]
v_pk_fma_f32 v[102:103], v[102:103], v[24:25], v[24:25] op_sel:[0,0,1] op_sel_hi:[1,0,1] neg_lo:[0,0,1] neg_hi:[0,0,1]
v_pk_fma_f32 v[104:105], v[104:105], v[28:29], v[28:29] op_sel:[0,0,1] op_sel_hi:[1,0,1] neg_lo:[0,0,1] neg_hi:[0,0,1]
v_pk_fma_f32 v[106:107], v[106:107], v[28:29], v[28:29] op_sel:[0,0,1] op_sel_hi:[1,0,1] neg_lo:[0,0,1] neg_hi:[0,0,1]
v_pk_fma_f32 v[108:109], v[108:109], v[28:29], v[28:29] op_sel:[0,0,1] op_sel_hi:[1,0,1] neg_lo:[0,0,1] neg_hi:[0,0,1]
v_pk_fma_f32 v[110:111], v[110:111], v[28:29], v[28:29] op_sel:[0,0,1] op_sel_hi:[1,0,1] neg_lo:[0,0,1] neg_hi:[0,0,1]
v_pk_fma_f32 v[80:81], v[80:81], v[24:25], v[24:25] op_sel:[0,0,1] op_sel_hi:[1,0,1] neg_lo:[0,0,1] neg_hi:[0,0,1]
v_pk_fma_f32 v[82:83], v[82:83], v[24:25], v[24:25] op_sel:[0,0,1] op_sel_hi:[1,0,1] neg_lo:[0,0,1] neg_hi:[0,0,1]
v_pk_fma_f32 v[84:85], v[84:85], v[24:25], v[24:25] op_sel:[0,0,1] op_sel_hi:[1,0,1] neg_lo:[0,0,1] neg_hi:[0,0,1]
v_pk_fma_f32 v[86:87], v[86:87], v[24:25], v[24:25] op_sel:[0,0,1] op_sel_hi:[1,0,1] neg_lo:[0,0,1] neg_hi:[0,0,1]
v_pk_fma_f32 v[88:89], v[88:89], v[28:29], v[28:29] op_sel:[0,0,1] op_sel_hi:[1,0,1] neg_lo:[0,0,1] neg_hi:[0,0,1]
v_pk_fma_f32 v[90:91], v[90:91], v[28:29], v[28:29] op_sel:[0,0,1] op_sel_hi:[1,0,1] neg_lo:[0,0,1] neg_hi:[0,0,1]
v_pk_fma_f32 v[92:93], v[92:93], v[28:29], v[28:29] op_sel:[0,0,1] op_sel_hi:[1,0,1] neg_lo:[0,0,1] neg_hi:[0,0,1]
v_pk_fma_f32 v[94:95], v[94:95], v[28:29], v[28:29] op_sel:[0,0,1] op_sel_hi:[1,0,1] neg_lo:[0,0,1] neg_hi:[0,0,1]
v_pk_fma_f32 v[64:65], v[64:65], v[24:25], v[24:25] op_sel:[0,0,1] op_sel_hi:[1,0,1] neg_lo:[0,0,1] neg_hi:[0,0,1]
v_pk_fma_f32 v[66:67], v[66:67], v[24:25], v[24:25] op_sel:[0,0,1] op_sel_hi:[1,0,1] neg_lo:[0,0,1] neg_hi:[0,0,1]
v_pk_fma_f32 v[68:69], v[68:69], v[24:25], v[24:25] op_sel:[0,0,1] op_sel_hi:[1,0,1] neg_lo:[0,0,1] neg_hi:[0,0,1]
v_pk_fma_f32 v[70:71], v[70:71], v[24:25], v[24:25] op_sel:[0,0,1] op_sel_hi:[1,0,1] neg_lo:[0,0,1] neg_hi:[0,0,1]
v_pk_fma_f32 v[72:73], v[72:73], v[28:29], v[28:29] op_sel:[0,0,1] op_sel_hi:[1,0,1] neg_lo:[0,0,1] neg_hi:[0,0,1]
v_pk_fma_f32 v[74:75], v[74:75], v[28:29], v[28:29] op_sel:[0,0,1] op_sel_hi:[1,0,1] neg_lo:[0,0,1] neg_hi:[0,0,1]
v_pk_fma_f32 v[76:77], v[76:77], v[28:29], v[28:29] op_sel:[0,0,1] op_sel_hi:[1,0,1] neg_lo:[0,0,1] neg_hi:[0,0,1]
v_pk_fma_f32 v[78:79], v[78:79], v[28:29], v[28:29] op_sel:[0,0,1] op_sel_hi:[1,0,1] neg_lo:[0,0,1] neg_hi:[0,0,1]
v_pk_fma_f32 v[48:49], v[48:49], v[24:25], v[24:25] op_sel:[0,0,1] op_sel_hi:[1,0,1] neg_lo:[0,0,1] neg_hi:[0,0,1]
v_pk_fma_f32 v[50:51], v[50:51], v[24:25], v[24:25] op_sel:[0,0,1] op_sel_hi:[1,0,1] neg_lo:[0,0,1] neg_hi:[0,0,1]
v_pk_fma_f32 v[52:53], v[52:53], v[24:25], v[24:25] op_sel:[0,0,1] op_sel_hi:[1,0,1] neg_lo:[0,0,1] neg_hi:[0,0,1]
v_pk_fma_f32 v[54:55], v[54:55], v[24:25], v[24:25] op_sel:[0,0,1] op_sel_hi:[1,0,1] neg_lo:[0,0,1] neg_hi:[0,0,1]
v_pk_fma_f32 v[56:57], v[56:57], v[28:29], v[28:29] op_sel:[0,0,1] op_sel_hi:[1,0,1] neg_lo:[0,0,1] neg_hi:[0,0,1]
v_pk_fma_f32 v[58:59], v[58:59], v[28:29], v[28:29] op_sel:[0,0,1] op_sel_hi:[1,0,1] neg_lo:[0,0,1] neg_hi:[0,0,1]
v_pk_fma_f32 v[60:61], v[60:61], v[28:29], v[28:29] op_sel:[0,0,1] op_sel_hi:[1,0,1] neg_lo:[0,0,1] neg_hi:[0,0,1]
v_pk_fma_f32 v[62:63], v[62:63], v[28:29], v[28:29] op_sel:[0,0,1] op_sel_hi:[1,0,1] neg_lo:[0,0,1] neg_hi:[0,0,1]
v_pk_fma_f32 v[32:33], v[32:33], v[24:25], v[24:25] op_sel:[0,0,1] op_sel_hi:[1,0,1] neg_lo:[0,0,1] neg_hi:[0,0,1]
v_pk_fma_f32 v[34:35], v[34:35], v[24:25], v[24:25] op_sel:[0,0,1] op_sel_hi:[1,0,1] neg_lo:[0,0,1] neg_hi:[0,0,1]
v_pk_fma_f32 v[36:37], v[36:37], v[24:25], v[24:25] op_sel:[0,0,1] op_sel_hi:[1,0,1] neg_lo:[0,0,1] neg_hi:[0,0,1]
v_pk_fma_f32 v[38:39], v[38:39], v[24:25], v[24:25] op_sel:[0,0,1] op_sel_hi:[1,0,1] neg_lo:[0,0,1] neg_hi:[0,0,1]
v_pk_fma_f32 v[40:41], v[40:41], v[28:29], v[28:29] op_sel:[0,0,1] op_sel_hi:[1,0,1] neg_lo:[0,0,1] neg_hi:[0,0,1]
v_pk_fma_f32 v[42:43], v[42:43], v[28:29], v[28:29] op_sel:[0,0,1] op_sel_hi:[1,0,1] neg_lo:[0,0,1] neg_hi:[0,0,1]
v_pk_fma_f32 v[44:45], v[44:45], v[28:29], v[28:29] op_sel:[0,0,1] op_sel_hi:[1,0,1] neg_lo:[0,0,1] neg_hi:[0,0,1]
v_pk_fma_f32 v[46:47], v[46:47], v[28:29], v[28:29] op_sel:[0,0,1] op_sel_hi:[1,0,1] neg_lo:[0,0,1] neg_hi:[0,0,1]
s_mov_b32 s47, 131072
L_s0%=:
s_waitcnt vmcnt(0)
v_accvgpr_read_b32 v16, a8
v_accvgpr_read_b32 v17, a9
v_accvgpr_read_b32 v18, a10
v_accvgpr_read_b32 v19, a11
v_accvgpr_read_b32 v20, a12
v_accvgpr_read_b32 v21, a13
v_accvgpr_read_b32 v22, a14
v_accvgpr_read_b32 v23, a15
v_xor_b32 v24, s78, v16
v_and_b32 v24, s97, v24
v_xor_b32 v28, s79, v17
v_or_b32 v24, v24, v28
v_xor_b32 v25, s78, v18
v_and_b32 v25, s97, v25
v_xor_b32 v28, s79, v19
v_or_b32 v25, v25, v28
v_xor_b32 v26, s78, v20
v_and_b32 v26, s97, v26
v_xor_b32 v28, s79, v21
v_or_b32 v26, v26, v28
v_xor_b32 v27, s78, v22
v_and_b32 v27, s97, v27
v_xor_b32 v28, s79, v23
v_or_b32 v27, v27, v28
v_or3_b32 v24, v24, v25, v26
v_or_b32 v24, v24, v27
v_cmp_ne_u32 vcc, 0, v24
s_cbranch_vccz L_s0d%=
s_sleep 1
buffer_load_dwordx4 a[8:11], v14, s[60:63], s53 offen offset:0 sc1
buffer_load_dwordx4 a[12:15], v14, s[60:63], s53 offen offset:16 sc1
s_sub_u32 s47, s47, 1
s_cbranch_scc0 L_s0%=
s_waitcnt vmcnt(0)
L_s0d%=:
s_mov_b32 s53, 0x3db8aa3b
v_cmp_le_u32 vcc, 112, v0
v_lshlrev_b32 v24, 16, v16
v_add_f32 v16, 1.0, v24
v_cvt_pk_bf16_f32 v16, v16, v16
v_and_b32 v16, s97, v16
v_cndmask_b32 v24, v24, v16, vcc
v_mul_f32 v24, s53, v24
v_lshlrev_b32 v25, 16, v18
v_add_f32 v18, 1.0, v25
v_cvt_pk_bf16_f32 v18, v18, v18
v_and_b32 v18, s97, v18
v_cndmask_b32 v25, v25, v18, vcc
v_mul_f32 v25, s53, v25
v_lshlrev_b32 v26, 16, v20
v_add_f32 v20, 1.0, v26
v_cvt_pk_bf16_f32 v20, v20, v20
v_and_b32 v20, s97, v20
v_cndmask_b32 v26, v26, v20, vcc
v_mul_f32 v26, s53, v26
v_lshlrev_b32 v27, 16, v22
v_add_f32 v22, 1.0, v27
v_cvt_pk_bf16_f32 v22, v22, v22
v_and_b32 v22, s97, v22
v_cndmask_b32 v27, v27, v22, vcc
v_mul_f32 v27, s53, v27
v_lshlrev_b32 v14, 4, v0
v_add_u32 v14, 157696, v14
ds_write_b128 v14, v[24:27] offset:0
s_mul_i32 s53, s80, 7168
s_waitcnt lgkmcnt(0)
s_barrier
ds_read_b128 v[24:27], v11 offset:3472
ds_read_b128 v[28:31], v11 offset:1680
ds_read_b128 v[16:19], v11 offset:3456
ds_read_b128 v[20:23], v11 offset:1664
s_waitcnt lgkmcnt(2)
v_pk_fma_f32 v[254:255], v[254:255], v[26:27], v[30:31]
v_pk_fma_f32 v[252:253], v[252:253], v[24:25], v[28:29]
v_pk_fma_f32 v[246:247], v[246:247], v[26:27], v[30:31]
v_pk_fma_f32 v[244:245], v[244:245], v[24:25], v[28:29]
ds_read_b128 v[24:27], v11 offset:3344
ds_read_b128 v[28:31], v11 offset:1552
s_add_u32 m0, s96, 0
s_nop 0
buffer_load_dwordx4 v8, s[88:91], s83 offen offset:0 lds
s_waitcnt lgkmcnt(2)
v_pk_fma_f32 v[250:251], v[250:251], v[18:19], v[22:23]
v_pk_fma_f32 v[248:249], v[248:249], v[16:17], v[20:21]
v_pk_fma_f32 v[242:243], v[242:243], v[18:19], v[22:23]
v_pk_fma_f32 v[240:241], v[240:241], v[16:17], v[20:21]
ds_read_b128 v[16:19], v11 offset:3328
ds_read_b128 v[20:23], v11 offset:1536
s_add_u32 s83, s83, 15296
buffer_load_dwordx4 v8, s[88:91], s83 offen offset:1088 lds
v_cvt_pk_bf16_f32 v255, v254, v255
v_cvt_pk_bf16_f32 v254, v252, v253
v_cvt_pk_bf16_f32 v253, v250, v251
v_cvt_pk_bf16_f32 v252, v248, v249
v_cvt_pk_bf16_f32 v251, v246, v247
v_cvt_pk_bf16_f32 v250, v244, v245
v_cvt_pk_bf16_f32 v249, v242, v243
v_cvt_pk_bf16_f32 v248, v240, v241
s_waitcnt lgkmcnt(2)
v_pk_fma_f32 v[238:239], v[238:239], v[26:27], v[30:31]
v_pk_fma_f32 v[236:237], v[236:237], v[24:25], v[28:29]
v_pk_fma_f32 v[230:231], v[230:231], v[26:27], v[30:31]
v_pk_fma_f32 v[228:229], v[228:229], v[24:25], v[28:29]
ds_read_b128 v[24:27], v11 offset:3216
ds_read_b128 v[28:31], v11 offset:1424
s_add_u32 s83, s83, 15360
buffer_load_dwordx4 v8, s[88:91], s83 offen offset:2112 lds
s_waitcnt lgkmcnt(2)
v_pk_fma_f32 v[234:235], v[234:235], v[18:19], v[22:23]
v_pk_fma_f32 v[232:233], v[232:233], v[16:17], v[20:21]
v_pk_fma_f32 v[226:227], v[226:227], v[18:19], v[22:23]
v_pk_fma_f32 v[224:225], v[224:225], v[16:17], v[20:21]
ds_read_b128 v[16:19], v11 offset:3200
ds_read_b128 v[20:23], v11 offset:1408
s_add_u32 s83, s83, 15296
buffer_load_dwordx4 v8, s[88:91], s83 offen offset:3200 lds
s_add_u32 s83, s83, 216192
v_cvt_pk_bf16_f32 v247, v238, v239
v_cvt_pk_bf16_f32 v246, v236, v237
v_cvt_pk_bf16_f32 v245, v234, v235
v_cvt_pk_bf16_f32 v244, v232, v233
v_cvt_pk_bf16_f32 v243, v230, v231
v_cvt_pk_bf16_f32 v242, v228, v229
v_cvt_pk_bf16_f32 v241, v226, v227
v_cvt_pk_bf16_f32 v240, v224, v225
s_waitcnt lgkmcnt(2)
v_pk_fma_f32 v[222:223], v[222:223], v[26:27], v[30:31]
v_pk_fma_f32 v[220:221], v[220:221], v[24:25], v[28:29]
v_pk_fma_f32 v[214:215], v[214:215], v[26:27], v[30:31]
v_pk_fma_f32 v[212:213], v[212:213], v[24:25], v[28:29]
ds_read_b128 v[24:27], v11 offset:3088
ds_read_b128 v[28:31], v11 offset:1296
s_add_u32 m0, s96, 16896
s_nop 0
buffer_load_dwordx4 v8, s[88:91], s83 offen offset:0 lds
s_waitcnt lgkmcnt(2)
v_pk_fma_f32 v[218:219], v[218:219], v[18:19], v[22:23]
v_pk_fma_f32 v[216:217], v[216:217], v[16:17], v[20:21]
v_pk_fma_f32 v[210:211], v[210:211], v[18:19], v[22:23]
v_pk_fma_f32 v[208:209], v[208:209], v[16:17], v[20:21]
ds_read_b128 v[16:19], v11 offset:3072
ds_read_b128 v[20:23], v11 offset:1280
s_add_u32 s83, s83, 15296
buffer_load_dwordx4 v8, s[88:91], s83 offen offset:1088 lds
v_cvt_pk_bf16_f32 v239, v222, v223
v_cvt_pk_bf16_f32 v238, v220, v221
v_cvt_pk_bf16_f32 v237, v218, v219
v_cvt_pk_bf16_f32 v236, v216, v217
v_cvt_pk_bf16_f32 v235, v214, v215
v_cvt_pk_bf16_f32 v234, v212, v213
v_cvt_pk_bf16_f32 v233, v210, v211
v_cvt_pk_bf16_f32 v232, v208, v209
s_waitcnt lgkmcnt(2)
v_pk_fma_f32 v[206:207], v[206:207], v[26:27], v[30:31]
v_pk_fma_f32 v[204:205], v[204:205], v[24:25], v[28:29]
v_pk_fma_f32 v[198:199], v[198:199], v[26:27], v[30:31]
v_pk_fma_f32 v[196:197], v[196:197], v[24:25], v[28:29]
ds_read_b128 v[24:27], v11 offset:2960
ds_read_b128 v[28:31], v11 offset:1168
s_add_u32 s83, s83, 15360
buffer_load_dwordx4 v8, s[88:91], s83 offen offset:2112 lds
s_waitcnt lgkmcnt(2)
v_pk_fma_f32 v[202:203], v[202:203], v[18:19], v[22:23]
v_pk_fma_f32 v[200:201], v[200:201], v[16:17], v[20:21]
v_pk_fma_f32 v[194:195], v[194:195], v[18:19], v[22:23]
v_pk_fma_f32 v[192:193], v[192:193], v[16:17], v[20:21]
ds_read_b128 v[16:19], v11 offset:2944
ds_read_b128 v[20:23], v11 offset:1152
s_add_u32 s83, s83, 15296
buffer_load_dwordx4 v8, s[88:91], s83 offen offset:3200 lds
s_add_u32 s83, s83, 216192
v_cvt_pk_bf16_f32 v231, v206, v207
v_cvt_pk_bf16_f32 v230, v204, v205
v_cvt_pk_bf16_f32 v229, v202, v203
v_cvt_pk_bf16_f32 v228, v200, v201
v_cvt_pk_bf16_f32 v227, v198, v199
v_cvt_pk_bf16_f32 v226, v196, v197
v_cvt_pk_bf16_f32 v225, v194, v195
v_cvt_pk_bf16_f32 v224, v192, v193
ds_read_b128 v[200:203], v11 offset:2704
ds_read_b128 v[204:207], v11 offset:912
ds_read_b128 v[192:195], v11 offset:2688
ds_read_b128 v[196:199], v11 offset:896
s_waitcnt lgkmcnt(6)
v_pk_fma_f32 v[190:191], v[190:191], v[26:27], v[30:31]
v_pk_fma_f32 v[188:189], v[188:189], v[24:25], v[28:29]
v_pk_fma_f32 v[182:183], v[182:183], v[26:27], v[30:31]
v_pk_fma_f32 v[180:181], v[180:181], v[24:25], v[28:29]
ds_read_b128 v[24:27], v11 offset:2832
ds_read_b128 v[28:31], v11 offset:1040
s_add_u32 m0, s96, 107008
s_nop 0
buffer_load_dwordx4 v8, s[88:91], s83 offen offset:0 lds
s_waitcnt lgkmcnt(6)
v_pk_fma_f32 v[186:187], v[186:187], v[18:19], v[22:23]
v_pk_fma_f32 v[184:185], v[184:185], v[16:17], v[20:21]
v_pk_fma_f32 v[178:179], v[178:179], v[18:19], v[22:23]
v_pk_fma_f32 v[176:177], v[176:177], v[16:17], v[20:21]
ds_read_b128 v[16:19], v11 offset:2816
ds_read_b128 v[20:23], v11 offset:1024
s_add_u32 s83, s83, 15296
buffer_load_dwordx4 v8, s[88:91], s83 offen offset:1088 lds
v_cvt_pk_bf16_f32 v223, v190, v191
v_cvt_pk_bf16_f32 v222, v188, v189
v_cvt_pk_bf16_f32 v221, v186, v187
v_cvt_pk_bf16_f32 v220, v184, v185
v_cvt_pk_bf16_f32 v219, v182, v183
v_cvt_pk_bf16_f32 v218, v180, v181
v_cvt_pk_bf16_f32 v217, v178, v179
v_cvt_pk_bf16_f32 v216, v176, v177
ds_read_b128 v[184:187], v11 offset:2576
ds_read_b128 v[188:191], v11 offset:784
ds_read_b128 v[176:179], v11 offset:2560
ds_read_b128 v[180:183], v11 offset:768
s_waitcnt lgkmcnt(6)
v_pk_fma_f32 v[174:175], v[174:175], v[26:27], v[30:31]
v_pk_fma_f32 v[172:173], v[172:173], v[24:25], v[28:29]
v_pk_fma_f32 v[166:167], v[166:167], v[26:27], v[30:31]
v_pk_fma_f32 v[164:165], v[164:165], v[24:25], v[28:29]
s_add_u32 s83, s83, 15360
buffer_load_dwordx4 v8, s[88:91], s83 offen offset:2112 lds
s_waitcnt lgkmcnt(4)
v_pk_fma_f32 v[170:171], v[170:171], v[18:19], v[22:23]
v_pk_fma_f32 v[168:169], v[168:169], v[16:17], v[20:21]
v_pk_fma_f32 v[162:163], v[162:163], v[18:19], v[22:23]
v_pk_fma_f32 v[160:161], v[160:161], v[16:17], v[20:21]
s_add_u32 s83, s83, 15296
buffer_load_dwordx4 v8, s[88:91], s83 offen offset:3200 lds
s_add_u32 s83, s83, 216192
v_cvt_pk_bf16_f32 v215, v174, v175
v_cvt_pk_bf16_f32 v214, v172, v173
v_cvt_pk_bf16_f32 v213, v170, v171
v_cvt_pk_bf16_f32 v212, v168, v169
v_cvt_pk_bf16_f32 v211, v166, v167
v_cvt_pk_bf16_f32 v210, v164, v165
v_cvt_pk_bf16_f32 v209, v162, v163
v_cvt_pk_bf16_f32 v208, v160, v161
ds_read_b128 v[168:171], v11 offset:2448
ds_read_b128 v[172:175], v11 offset:656
ds_read_b128 v[160:163], v11 offset:2432
ds_read_b128 v[164:167], v11 offset:640
v_pk_fma_f32 v[158:159], v[158:159], v[202:203], v[206:207]
v_pk_fma_f32 v[156:157], v[156:157], v[200:201], v[204:205]
v_pk_fma_f32 v[150:151], v[150:151], v[202:203], v[206:207]
v_pk_fma_f32 v[148:149], v[148:149], v[200:201], v[204:205]
s_add_u32 m0, s96, 90112
s_nop 0
buffer_load_dwordx4 v8, s[88:91], s83 offen offset:0 lds
v_pk_fma_f32 v[154:155], v[154:155], v[194:195], v[198:199]
v_pk_fma_f32 v[152:153], v[152:153], v[192:193], v[196:197]
v_pk_fma_f32 v[146:147], v[146:147], v[194:195], v[198:199]
v_pk_fma_f32 v[144:145], v[144:145], v[192:193], v[196:197]
s_add_u32 s83, s83, 15296
buffer_load_dwordx4 v8, s[88:91], s83 offen offset:1088 lds
v_accvgpr_read_b32 v16, a40
v_accvgpr_read_b32 v17, a41
v_accvgpr_read_b32 v18, a42
v_accvgpr_read_b32 v19, a43
v_accvgpr_read_b32 v20, a44
v_accvgpr_read_b32 v21, a45
v_accvgpr_read_b32 v22, a46
v_cvt_pk_bf16_f32 v207, v158, v159
v_cvt_pk_bf16_f32 v206, v156, v157
v_cvt_pk_bf16_f32 v205, v154, v155
v_cvt_pk_bf16_f32 v204, v152, v153
v_cvt_pk_bf16_f32 v203, v150, v151
v_cvt_pk_bf16_f32 v202, v148, v149
v_cvt_pk_bf16_f32 v201, v146, v147
v_cvt_pk_bf16_f32 v200, v144, v145
ds_read_b128 v[152:155], v11 offset:2320
ds_read_b128 v[156:159], v11 offset:528
ds_read_b128 v[144:147], v11 offset:2304
ds_read_b128 v[148:151], v11 offset:512
s_waitcnt lgkmcnt(10)
v_pk_fma_f32 v[142:143], v[142:143], v[186:187], v[190:191]
v_pk_fma_f32 v[140:141], v[140:141], v[184:185], v[188:189]
v_pk_fma_f32 v[134:135], v[134:135], v[186:187], v[190:191]
v_pk_fma_f32 v[132:133], v[132:133], v[184:185], v[188:189]
s_add_u32 s83, s83, 15360
buffer_load_dwordx4 v8, s[88:91], s83 offen offset:2112 lds
s_waitcnt lgkmcnt(8)
v_pk_fma_f32 v[138:139], v[138:139], v[178:179], v[182:183]
v_pk_fma_f32 v[136:137], v[136:137], v[176:177], v[180:181]
v_pk_fma_f32 v[130:131], v[130:131], v[178:179], v[182:183]
v_pk_fma_f32 v[128:129], v[128:129], v[176:177], v[180:181]
s_add_u32 s83, s83, 15296
buffer_load_dwordx4 v8, s[88:91], s83 offen offset:3200 lds
s_add_u32 s83, s83, 216192
v_cvt_pk_bf16_f32 v199, v142, v143
v_cvt_pk_bf16_f32 v198, v140, v141
v_cvt_pk_bf16_f32 v197, v138, v139
v_cvt_pk_bf16_f32 v196, v136, v137
v_cvt_pk_bf16_f32 v195, v134, v135
v_cvt_pk_bf16_f32 v194, v132, v133
v_cvt_pk_bf16_f32 v193, v130, v131
v_cvt_pk_bf16_f32 v192, v128, v129
ds_read_b128 v[136:139], v11 offset:2192
ds_read_b128 v[140:143], v11 offset:400
ds_read_b128 v[128:131], v11 offset:2176
ds_read_b128 v[132:135], v11 offset:384
s_waitcnt lgkmcnt(10)
v_pk_fma_f32 v[126:127], v[126:127], v[170:171], v[174:175]
v_pk_fma_f32 v[124:125], v[124:125], v[168:169], v[172:173]
v_pk_fma_f32 v[118:119], v[118:119], v[170:171], v[174:175]
v_pk_fma_f32 v[116:117], v[116:117], v[168:169], v[172:173]
s_add_u32 m0, s53, 34816
s_nop 0
buffer_load_dwordx4 v16, s[92:95], 0 offen offset:0 lds
s_waitcnt lgkmcnt(8)
v_pk_fma_f32 v[122:123], v[122:123], v[162:163], v[166:167]
v_pk_fma_f32 v[120:121], v[120:121], v[160:161], v[164:165]
v_pk_fma_f32 v[114:115], v[114:115], v[162:163], v[166:167]
v_pk_fma_f32 v[112:113], v[112:113], v[160:161], v[164:165]
buffer_load_dwordx4 v17, s[92:95], 0 offen offset:1024 lds
v_cvt_pk_bf16_f32 v191, v126, v127
v_cvt_pk_bf16_f32 v190, v124, v125
v_cvt_pk_bf16_f32 v189, v122, v123
v_cvt_pk_bf16_f32 v188, v120, v121
v_cvt_pk_bf16_f32 v187, v118, v119
v_cvt_pk_bf16_f32 v186, v116, v117
v_cvt_pk_bf16_f32 v185, v114, v115
v_cvt_pk_bf16_f32 v184, v112, v113
ds_read_b128 v[120:123], v11 offset:2064
ds_read_b128 v[124:127], v11 offset:272
ds_read_b128 v[112:115], v11 offset:2048
ds_read_b128 v[116:119], v11 offset:256
s_waitcnt lgkmcnt(10)
v_pk_fma_f32 v[110:111], v[110:111], v[154:155], v[158:159]
v_pk_fma_f32 v[108:109], v[108:109], v[152:153], v[156:157]
v_pk_fma_f32 v[102:103], v[102:103], v[154:155], v[158:159]
v_pk_fma_f32 v[100:101], v[100:101], v[152:153], v[156:157]
buffer_load_dwordx4 v18, s[92:95], 0 offen offset:2048 lds
s_waitcnt lgkmcnt(8)
v_pk_fma_f32 v[106:107], v[106:107], v[146:147], v[150:151]
v_pk_fma_f32 v[104:105], v[104:105], v[144:145], v[148:149]
v_pk_fma_f32 v[98:99], v[98:99], v[146:147], v[150:151]
v_pk_fma_f32 v[96:97], v[96:97], v[144:145], v[148:149]
buffer_load_dwordx4 v19, s[92:95], 0 offen offset:3072 lds
v_cvt_pk_bf16_f32 v183, v110, v111
v_cvt_pk_bf16_f32 v182, v108, v109
v_cvt_pk_bf16_f32 v181, v106, v107
v_cvt_pk_bf16_f32 v180, v104, v105
v_cvt_pk_bf16_f32 v179, v102, v103
v_cvt_pk_bf16_f32 v178, v100, v101
v_cvt_pk_bf16_f32 v177, v98, v99
v_cvt_pk_bf16_f32 v176, v96, v97
ds_read_b128 v[104:107], v11 offset:1936
ds_read_b128 v[108:111], v11 offset:144
ds_read_b128 v[96:99], v11 offset:1920
ds_read_b128 v[100:103], v11 offset:128
s_waitcnt lgkmcnt(10)
v_pk_fma_f32 v[94:95], v[94:95], v[138:139], v[142:143]
v_pk_fma_f32 v[92:93], v[92:93], v[136:137], v[140:141]
v_pk_fma_f32 v[86:87], v[86:87], v[138:139], v[142:143]
v_pk_fma_f32 v[84:85], v[84:85], v[136:137], v[140:141]
s_add_u32 m0, s53, 38912
s_nop 0
buffer_load_dwordx4 v20, s[92:95], s51 offen offset:0 lds
s_waitcnt lgkmcnt(8)
v_pk_fma_f32 v[90:91], v[90:91], v[130:131], v[134:135]
v_pk_fma_f32 v[88:89], v[88:89], v[128:129], v[132:133]
v_pk_fma_f32 v[82:83], v[82:83], v[130:131], v[134:135]
v_pk_fma_f32 v[80:81], v[80:81], v[128:129], v[132:133]
buffer_load_dwordx4 v21, s[92:95], s51 offen offset:1024 lds
v_cvt_pk_bf16_f32 v175, v94, v95
v_cvt_pk_bf16_f32 v174, v92, v93
v_cvt_pk_bf16_f32 v173, v90, v91
v_cvt_pk_bf16_f32 v172, v88, v89
v_cvt_pk_bf16_f32 v171, v86, v87
v_cvt_pk_bf16_f32 v170, v84, v85
v_cvt_pk_bf16_f32 v169, v82, v83
v_cvt_pk_bf16_f32 v168, v80, v81
ds_read_b128 v[88:91], v11 offset:1808
ds_read_b128 v[92:95], v11 offset:16
ds_read_b128 v[80:83], v11 offset:1792
ds_read_b128 v[84:87], v11 offset:0
s_waitcnt lgkmcnt(10)
v_pk_fma_f32 v[78:79], v[78:79], v[122:123], v[126:127]
v_pk_fma_f32 v[76:77], v[76:77], v[120:121], v[124:125]
v_pk_fma_f32 v[70:71], v[70:71], v[122:123], v[126:127]
v_pk_fma_f32 v[68:69], v[68:69], v[120:121], v[124:125]
buffer_load_dwordx4 v22, s[92:95], s51 offen offset:2048 lds
s_waitcnt lgkmcnt(8)
v_pk_fma_f32 v[74:75], v[74:75], v[114:115], v[118:119]
v_pk_fma_f32 v[72:73], v[72:73], v[112:113], v[116:117]
v_pk_fma_f32 v[66:67], v[66:67], v[114:115], v[118:119]
v_pk_fma_f32 v[64:65], v[64:65], v[112:113], v[116:117]
v_cvt_pk_bf16_f32 v167, v78, v79
v_cvt_pk_bf16_f32 v166, v76, v77
v_cvt_pk_bf16_f32 v165, v74, v75
v_cvt_pk_bf16_f32 v164, v72, v73
v_cvt_pk_bf16_f32 v163, v70, v71
v_cvt_pk_bf16_f32 v162, v68, v69
v_cvt_pk_bf16_f32 v161, v66, v67
v_cvt_pk_bf16_f32 v160, v64, v65
s_waitcnt lgkmcnt(6)
v_pk_fma_f32 v[62:63], v[62:63], v[106:107], v[110:111]
v_pk_fma_f32 v[60:61], v[60:61], v[104:105], v[108:109]
v_pk_fma_f32 v[54:55], v[54:55], v[106:107], v[110:111]
v_pk_fma_f32 v[52:53], v[52:53], v[104:105], v[108:109]
s_waitcnt lgkmcnt(4)
v_pk_fma_f32 v[58:59], v[58:59], v[98:99], v[102:103]
v_pk_fma_f32 v[56:57], v[56:57], v[96:97], v[100:101]
v_pk_fma_f32 v[50:51], v[50:51], v[98:99], v[102:103]
v_pk_fma_f32 v[48:49], v[48:49], v[96:97], v[100:101]
v_cvt_pk_bf16_f32 v159, v62, v63
v_cvt_pk_bf16_f32 v158, v60, v61
v_cvt_pk_bf16_f32 v157, v58, v59
v_cvt_pk_bf16_f32 v156, v56, v57
v_cvt_pk_bf16_f32 v155, v54, v55
v_cvt_pk_bf16_f32 v154, v52, v53
v_cvt_pk_bf16_f32 v153, v50, v51
v_cvt_pk_bf16_f32 v152, v48, v49
s_waitcnt lgkmcnt(2)
v_pk_fma_f32 v[46:47], v[46:47], v[90:91], v[94:95]
v_pk_fma_f32 v[44:45], v[44:45], v[88:89], v[92:93]
v_pk_fma_f32 v[38:39], v[38:39], v[90:91], v[94:95]
v_pk_fma_f32 v[36:37], v[36:37], v[88:89], v[92:93]
s_waitcnt lgkmcnt(0)
v_pk_fma_f32 v[42:43], v[42:43], v[82:83], v[86:87]
v_pk_fma_f32 v[40:41], v[40:41], v[80:81], v[84:85]
v_pk_fma_f32 v[34:35], v[34:35], v[82:83], v[86:87]
v_pk_fma_f32 v[32:33], v[32:33], v[80:81], v[84:85]
v_cvt_pk_bf16_f32 v151, v46, v47
v_cvt_pk_bf16_f32 v150, v44, v45
v_cvt_pk_bf16_f32 v149, v42, v43
v_cvt_pk_bf16_f32 v148, v40, v41
v_cvt_pk_bf16_f32 v147, v38, v39
v_cvt_pk_bf16_f32 v146, v36, v37
v_cvt_pk_bf16_f32 v145, v34, v35
v_cvt_pk_bf16_f32 v144, v32, v33
v_subrev_u32 v0, 90112, v9
v_subrev_u32 v11, 90112, v10
s_nop 0
ds_read_b64_tr_b16 a[128:129], v9 offset:50688
ds_read_b64_tr_b16 a[130:131], v9 offset:52800
ds_read_b64_tr_b16 a[132:133], v10 offset:50688
ds_read_b64_tr_b16 a[134:135], v10 offset:52800
ds_read_b64_tr_b16 a[136:137], v9 offset:50752
ds_read_b64_tr_b16 a[138:139], v9 offset:52864
ds_read_b64_tr_b16 a[140:141], v10 offset:50752
ds_read_b64_tr_b16 a[142:143], v10 offset:52864
s_waitcnt lgkmcnt(0)
v_mfma_f32_16x16x32_bf16 v[16:19], a[128:131], v[144:147], 0
ds_read_b64_tr_b16 a[144:145], v9 offset:50816
ds_read_b64_tr_b16 a[146:147], v9 offset:52928
v_mfma_f32_16x16x32_bf16 v[20:23], a[128:131], v[148:151], 0
ds_read_b64_tr_b16 a[148:149], v10 offset:50816
ds_read_b64_tr_b16 a[150:151], v10 offset:52928
v_mfma_f32_16x16x32_bf16 v[24:27], a[132:135], v[144:147], 0
ds_read_b64_tr_b16 a[152:153], v9 offset:50880
ds_read_b64_tr_b16 a[154:155], v9 offset:52992
v_mfma_f32_16x16x32_bf16 v[28:31], a[132:135], v[148:151], 0
ds_read_b64_tr_b16 a[156:157], v10 offset:50880
ds_read_b64_tr_b16 a[158:159], v10 offset:52992
v_mfma_f32_16x16x32_bf16 v[32:35], a[136:139], v[144:147], 0
ds_read_b64_tr_b16 a[160:161], v9 offset:50944
ds_read_b64_tr_b16 a[162:163], v9 offset:53056
v_mfma_f32_16x16x32_bf16 v[36:39], a[136:139], v[148:151], 0
ds_read_b64_tr_b16 a[164:165], v10 offset:50944
ds_read_b64_tr_b16 a[166:167], v10 offset:53056
v_mfma_f32_16x16x32_bf16 v[40:43], a[140:143], v[144:147], 0
ds_read_b64_tr_b16 a[168:169], v9 offset:51008
ds_read_b64_tr_b16 a[170:171], v9 offset:53120
v_mfma_f32_16x16x32_bf16 v[44:47], a[140:143], v[148:151], 0
ds_read_b64_tr_b16 a[172:173], v10 offset:51008
s_waitcnt lgkmcnt(7)
v_mfma_f32_16x16x32_bf16 v[48:51], a[144:147], v[144:147], 0
s_waitcnt vmcnt(23)
s_barrier
ds_read_b64_tr_b16 a[174:175], v10 offset:53120
v_mfma_f32_16x16x32_bf16 v[52:55], a[144:147], v[148:151], 0
ds_read_b64_tr_b16 a[176:177], v9 offset:51072
ds_read_b64_tr_b16 a[178:179], v9 offset:53184
v_mfma_f32_16x16x32_bf16 v[56:59], a[148:151], v[144:147], 0
ds_read_b64_tr_b16 a[180:181], v10 offset:51072
ds_read_b64_tr_b16 a[182:183], v10 offset:53184
v_mfma_f32_16x16x32_bf16 v[60:63], a[148:151], v[148:151], 0
ds_read_b64_tr_b16 a[184:185], v9 offset:51136
ds_read_b64_tr_b16 a[186:187], v9 offset:53248
v_mfma_f32_16x16x32_bf16 v[64:67], a[152:155], v[144:147], 0
ds_read_b64_tr_b16 a[188:189], v10 offset:51136
ds_read_b64_tr_b16 a[190:191], v10 offset:53248
v_mfma_f32_16x16x32_bf16 v[68:71], a[152:155], v[148:151], 0
ds_read_b64_tr_b16 a[128:129], v9 offset:33792
ds_read_b64_tr_b16 a[130:131], v9 offset:35904
v_mfma_f32_16x16x32_bf16 v[72:75], a[156:159], v[144:147], 0
ds_read_b64_tr_b16 a[132:133], v10 offset:33792
ds_read_b64_tr_b16 a[134:135], v10 offset:35904
v_mfma_f32_16x16x32_bf16 v[76:79], a[156:159], v[148:151], 0
ds_read_b64_tr_b16 a[136:137], v9 offset:33856
s_waitcnt lgkmcnt(13)
v_mfma_f32_16x16x32_bf16 v[80:83], a[160:163], v[144:147], 0
ds_read_b64_tr_b16 a[138:139], v9 offset:35968
ds_read_b64_tr_b16 a[140:141], v10 offset:33856
v_mfma_f32_16x16x32_bf16 v[84:87], a[160:163], v[148:151], 0
ds_read_b64_tr_b16 a[142:143], v10 offset:35968
ds_read_b64_tr_b16 a[144:145], v9 offset:33920
v_mfma_f32_16x16x32_bf16 v[88:91], a[164:167], v[144:147], 0
ds_read_b64_tr_b16 a[146:147], v9 offset:36032
ds_read_b64_tr_b16 a[148:149], v10 offset:33920
v_mfma_f32_16x16x32_bf16 v[92:95], a[164:167], v[148:151], 0
ds_read_b64_tr_b16 a[150:151], v10 offset:36032
ds_read_b64_tr_b16 a[152:153], v9 offset:33984
v_mfma_f32_16x16x32_bf16 v[96:99], a[168:171], v[144:147], 0
ds_read_b64_tr_b16 a[154:155], v9 offset:36096
ds_read_b64_tr_b16 a[156:157], v10 offset:33984
v_mfma_f32_16x16x32_bf16 v[100:103], a[168:171], v[148:151], 0
ds_read_b64_tr_b16 a[158:159], v10 offset:36096
ds_read_b64_tr_b16 a[160:161], v9 offset:34048
v_mfma_f32_16x16x32_bf16 v[104:107], a[172:175], v[144:147], 0
ds_read_b64_tr_b16 a[162:163], v9 offset:36160
v_mfma_f32_16x16x32_bf16 v[108:111], a[172:175], v[148:151], 0
ds_read_b64_tr_b16 a[164:165], v10 offset:34048
s_waitcnt lgkmcnt(14)
v_mfma_f32_16x16x32_bf16 v[112:115], a[176:179], v[144:147], 0
ds_read_b64_tr_b16 a[166:167], v10 offset:36160
ds_read_b64_tr_b16 a[168:169], v9 offset:34112
v_mfma_f32_16x16x32_bf16 v[116:119], a[176:179], v[148:151], 0
ds_read_b64_tr_b16 a[170:171], v9 offset:36224
ds_read_b64_tr_b16 a[172:173], v10 offset:34112
v_mfma_f32_16x16x32_bf16 v[120:123], a[180:183], v[144:147], 0
ds_read_b64_tr_b16 a[174:175], v10 offset:36224
v_mfma_f32_16x16x32_bf16 v[124:127], a[180:183], v[148:151], 0
v_mfma_f32_16x16x32_bf16 v[128:131], a[184:187], v[144:147], 0
v_mfma_f32_16x16x32_bf16 v[132:135], a[184:187], v[148:151], 0
v_mfma_f32_16x16x32_bf16 v[136:139], a[188:191], v[144:147], 0
v_mfma_f32_16x16x32_bf16 v[140:143], a[188:191], v[148:151], 0
s_waitcnt lgkmcnt(14)
v_mfma_f32_16x16x32_bf16 v[16:19], a[128:131], v[152:155], v[16:19]
ds_read_b64_tr_b16 a[176:177], v9 offset:34176
ds_read_b64_tr_b16 a[178:179], v9 offset:36288
v_mfma_f32_16x16x32_bf16 v[20:23], a[128:131], v[156:159], v[20:23]
ds_read_b64_tr_b16 a[180:181], v10 offset:34176
ds_read_b64_tr_b16 a[182:183], v10 offset:36288
v_mfma_f32_16x16x32_bf16 v[24:27], a[132:135], v[152:155], v[24:27]
s_add_u32 s84, s8, 0
s_addc_u32 s85, s9, 0
v_mfma_f32_16x16x32_bf16 v[28:31], a[132:135], v[156:159], v[28:31]
ds_read_b64_tr_b16 a[184:185], v9 offset:34240
ds_read_b64_tr_b16 a[186:187], v9 offset:36352
v_mfma_f32_16x16x32_bf16 v[32:35], a[136:139], v[152:155], v[32:35]
ds_read_b64_tr_b16 a[188:189], v10 offset:34240
s_and_b32 s85, s85, 0xffff
v_mfma_f32_16x16x32_bf16 v[36:39], a[136:139], v[156:159], v[36:39]
ds_read_b64_tr_b16 a[190:191], v10 offset:36352
s_mov_b32 s86, 1835008
v_mfma_f32_16x16x32_bf16 v[40:43], a[140:143], v[152:155], v[40:43]
s_mov_b32 s87, 131072
v_mfma_f32_16x16x32_bf16 v[44:47], a[140:143], v[156:159], v[44:47]
s_waitcnt lgkmcnt(14)
v_mfma_f32_16x16x32_bf16 v[48:51], a[144:147], v[152:155], v[48:51]
s_waitcnt vmcnt(19)
s_barrier
v_mfma_f32_16x16x32_bf16 v[52:55], a[144:147], v[156:159], v[52:55]
ds_read_b64_tr_b16 a[128:129], v0 offset:0
s_add_u32 m0, s96, 140800
v_mfma_f32_16x16x32_bf16 v[56:59], a[148:151], v[152:155], v[56:59]
ds_read_b64_tr_b16 a[130:131], v0 offset:2112
ds_read_b64_tr_b16 a[132:133], v11 offset:0
v_mfma_f32_16x16x32_bf16 v[60:63], a[148:151], v[156:159], v[60:63]
buffer_load_dwordx4 v8, s[88:91], s83 offen offset:0 lds
v_mfma_f32_16x16x32_bf16 v[64:67], a[152:155], v[152:155], v[64:67]
ds_read_b64_tr_b16 a[134:135], v11 offset:2112
ds_read_b64_tr_b16 a[136:137], v0 offset:64
v_mfma_f32_16x16x32_bf16 v[68:71], a[152:155], v[156:159], v[68:71]
ds_read_b64_tr_b16 a[138:139], v0 offset:2176
ds_read_b64_tr_b16 a[140:141], v11 offset:64
v_mfma_f32_16x16x32_bf16 v[72:75], a[156:159], v[152:155], v[72:75]
ds_read_b64_tr_b16 a[142:143], v11 offset:2176
s_add_u32 s83, s83, 15296
v_mfma_f32_16x16x32_bf16 v[76:79], a[156:159], v[156:159], v[76:79]
ds_read_b64_tr_b16 a[144:145], v0 offset:128
s_waitcnt lgkmcnt(14)
v_mfma_f32_16x16x32_bf16 v[80:83], a[160:163], v[152:155], v[80:83]
ds_read_b64_tr_b16 a[146:147], v0 offset:2240
ds_read_b64_tr_b16 a[148:149], v11 offset:128
v_mfma_f32_16x16x32_bf16 v[84:87], a[160:163], v[156:159], v[84:87]
buffer_load_dwordx4 v8, s[88:91], s83 offen offset:1088 lds
v_mfma_f32_16x16x32_bf16 v[88:91], a[164:167], v[152:155], v[88:91]
ds_read_b64_tr_b16 a[150:151], v11 offset:2240
ds_read_b64_tr_b16 a[152:153], v0 offset:192
v_mfma_f32_16x16x32_bf16 v[92:95], a[164:167], v[156:159], v[92:95]
ds_read_b64_tr_b16 a[154:155], v0 offset:2304
s_add_u32 s83, s83, 15360
v_mfma_f32_16x16x32_bf16 v[96:99], a[168:171], v[152:155], v[96:99]
ds_read_b64_tr_b16 a[156:157], v11 offset:192
ds_read_b64_tr_b16 a[158:159], v11 offset:2304
v_mfma_f32_16x16x32_bf16 v[100:103], a[168:171], v[156:159], v[100:103]
ds_read_b64_tr_b16 a[160:161], v0 offset:256
v_mfma_f32_16x16x32_bf16 v[104:107], a[172:175], v[152:155], v[104:107]
buffer_load_dwordx4 v8, s[88:91], s83 offen offset:2112 lds
v_mfma_f32_16x16x32_bf16 v[108:111], a[172:175], v[156:159], v[108:111]
ds_read_b64_tr_b16 a[162:163], v0 offset:2368
s_waitcnt lgkmcnt(14)
v_mfma_f32_16x16x32_bf16 v[112:115], a[176:179], v[152:155], v[112:115]
ds_read_b64_tr_b16 a[164:165], v11 offset:256
ds_read_b64_tr_b16 a[166:167], v11 offset:2368
v_mfma_f32_16x16x32_bf16 v[116:119], a[176:179], v[156:159], v[116:119]
ds_read_b64_tr_b16 a[168:169], v0 offset:320
s_add_u32 s83, s83, 15296
v_mfma_f32_16x16x32_bf16 v[120:123], a[180:183], v[152:155], v[120:123]
ds_read_b64_tr_b16 a[170:171], v0 offset:2432
ds_read_b64_tr_b16 a[172:173], v11 offset:320
v_mfma_f32_16x16x32_bf16 v[124:127], a[180:183], v[156:159], v[124:127]
ds_read_b64_tr_b16 a[174:175], v11 offset:2432
v_mfma_f32_16x16x32_bf16 v[128:131], a[184:187], v[152:155], v[128:131]
buffer_load_dwordx4 v8, s[88:91], s83 offen offset:3200 lds
v_mfma_f32_16x16x32_bf16 v[132:135], a[184:187], v[156:159], v[132:135]
s_add_u32 s83, s83, 216192
v_mfma_f32_16x16x32_bf16 v[136:139], a[188:191], v[152:155], v[136:139]
v_mfma_f32_16x16x32_bf16 v[140:143], a[188:191], v[156:159], v[140:143]
s_waitcnt lgkmcnt(14)
v_mfma_f32_16x16x32_bf16 v[16:19], a[128:131], v[160:163], v[16:19]
ds_read_b64_tr_b16 a[176:177], v0 offset:384
ds_read_b64_tr_b16 a[178:179], v0 offset:2496
v_mfma_f32_16x16x32_bf16 v[20:23], a[128:131], v[164:167], v[20:23]
ds_read_b64_tr_b16 a[180:181], v11 offset:384
ds_read_b64_tr_b16 a[182:183], v11 offset:2496
v_mfma_f32_16x16x32_bf16 v[24:27], a[132:135], v[160:163], v[24:27]
s_add_u32 s92, s26, 0
s_addc_u32 s93, s27, 0
v_mfma_f32_16x16x32_bf16 v[28:31], a[132:135], v[164:167], v[28:31]
ds_read_b64_tr_b16 a[184:185], v0 offset:448
ds_read_b64_tr_b16 a[186:187], v0 offset:2560
v_mfma_f32_16x16x32_bf16 v[32:35], a[136:139], v[160:163], v[32:35]
ds_read_b64_tr_b16 a[188:189], v11 offset:448
s_and_b32 s93, s93, 0xffff
v_mfma_f32_16x16x32_bf16 v[36:39], a[136:139], v[164:167], v[36:39]
ds_read_b64_tr_b16 a[190:191], v11 offset:2560
s_mov_b32 s94, 1835008
v_mfma_f32_16x16x32_bf16 v[40:43], a[140:143], v[160:163], v[40:43]
s_mov_b32 s95, 131072
v_mfma_f32_16x16x32_bf16 v[44:47], a[140:143], v[164:167], v[44:47]
s_waitcnt lgkmcnt(14)
v_mfma_f32_16x16x32_bf16 v[48:51], a[144:147], v[160:163], v[48:51]
s_waitcnt vmcnt(19)
s_barrier
v_mfma_f32_16x16x32_bf16 v[52:55], a[144:147], v[164:167], v[52:55]
ds_read_b64_tr_b16 a[128:129], v0 offset:16896
s_add_u32 m0, s96, 123904
v_mfma_f32_16x16x32_bf16 v[56:59], a[148:151], v[160:163], v[56:59]
ds_read_b64_tr_b16 a[130:131], v0 offset:19008
ds_read_b64_tr_b16 a[132:133], v11 offset:16896
v_mfma_f32_16x16x32_bf16 v[60:63], a[148:151], v[164:167], v[60:63]
buffer_load_dwordx4 v8, s[88:91], s83 offen offset:0 lds
v_mfma_f32_16x16x32_bf16 v[64:67], a[152:155], v[160:163], v[64:67]
ds_read_b64_tr_b16 a[134:135], v11 offset:19008
ds_read_b64_tr_b16 a[136:137], v0 offset:16960
v_mfma_f32_16x16x32_bf16 v[68:71], a[152:155], v[164:167], v[68:71]
ds_read_b64_tr_b16 a[138:139], v0 offset:19072
ds_read_b64_tr_b16 a[140:141], v11 offset:16960
v_mfma_f32_16x16x32_bf16 v[72:75], a[156:159], v[160:163], v[72:75]
ds_read_b64_tr_b16 a[142:143], v11 offset:19072
s_add_u32 s83, s83, 15296
v_mfma_f32_16x16x32_bf16 v[76:79], a[156:159], v[164:167], v[76:79]
ds_read_b64_tr_b16 a[144:145], v0 offset:17024
s_waitcnt lgkmcnt(14)
v_mfma_f32_16x16x32_bf16 v[80:83], a[160:163], v[160:163], v[80:83]
ds_read_b64_tr_b16 a[146:147], v0 offset:19136
ds_read_b64_tr_b16 a[148:149], v11 offset:17024
v_mfma_f32_16x16x32_bf16 v[84:87], a[160:163], v[164:167], v[84:87]
buffer_load_dwordx4 v8, s[88:91], s83 offen offset:1088 lds
v_mfma_f32_16x16x32_bf16 v[88:91], a[164:167], v[160:163], v[88:91]
ds_read_b64_tr_b16 a[150:151], v11 offset:19136
ds_read_b64_tr_b16 a[152:153], v0 offset:17088
v_mfma_f32_16x16x32_bf16 v[92:95], a[164:167], v[164:167], v[92:95]
ds_read_b64_tr_b16 a[154:155], v0 offset:19200
s_add_u32 s83, s83, 15360
v_mfma_f32_16x16x32_bf16 v[96:99], a[168:171], v[160:163], v[96:99]
ds_read_b64_tr_b16 a[156:157], v11 offset:17088
ds_read_b64_tr_b16 a[158:159], v11 offset:19200
v_mfma_f32_16x16x32_bf16 v[100:103], a[168:171], v[164:167], v[100:103]
ds_read_b64_tr_b16 a[160:161], v0 offset:17152
v_mfma_f32_16x16x32_bf16 v[104:107], a[172:175], v[160:163], v[104:107]
buffer_load_dwordx4 v8, s[88:91], s83 offen offset:2112 lds
v_mfma_f32_16x16x32_bf16 v[108:111], a[172:175], v[164:167], v[108:111]
ds_read_b64_tr_b16 a[162:163], v0 offset:19264
s_waitcnt lgkmcnt(14)
v_mfma_f32_16x16x32_bf16 v[112:115], a[176:179], v[160:163], v[112:115]
ds_read_b64_tr_b16 a[164:165], v11 offset:17152
ds_read_b64_tr_b16 a[166:167], v11 offset:19264
v_mfma_f32_16x16x32_bf16 v[116:119], a[176:179], v[164:167], v[116:119]
ds_read_b64_tr_b16 a[168:169], v0 offset:17216
s_add_u32 s83, s83, 15296
v_mfma_f32_16x16x32_bf16 v[120:123], a[180:183], v[160:163], v[120:123]
ds_read_b64_tr_b16 a[170:171], v0 offset:19328
ds_read_b64_tr_b16 a[172:173], v11 offset:17216
v_mfma_f32_16x16x32_bf16 v[124:127], a[180:183], v[164:167], v[124:127]
ds_read_b64_tr_b16 a[174:175], v11 offset:19328
v_mfma_f32_16x16x32_bf16 v[128:131], a[184:187], v[160:163], v[128:131]
buffer_load_dwordx4 v8, s[88:91], s83 offen offset:3200 lds
v_mfma_f32_16x16x32_bf16 v[132:135], a[184:187], v[164:167], v[132:135]
s_add_u32 s83, s83, 216192
v_mfma_f32_16x16x32_bf16 v[136:139], a[188:191], v[160:163], v[136:139]
v_mfma_f32_16x16x32_bf16 v[140:143], a[188:191], v[164:167], v[140:143]
s_waitcnt lgkmcnt(14)
v_mfma_f32_16x16x32_bf16 v[16:19], a[128:131], v[168:171], v[16:19]
ds_read_b64_tr_b16 a[176:177], v0 offset:17280
ds_read_b64_tr_b16 a[178:179], v0 offset:19392
v_mfma_f32_16x16x32_bf16 v[20:23], a[128:131], v[172:175], v[20:23]
ds_read_b64_tr_b16 a[180:181], v11 offset:17280
ds_read_b64_tr_b16 a[182:183], v11 offset:19392
v_mfma_f32_16x16x32_bf16 v[24:27], a[132:135], v[168:171], v[24:27]
ds_read_b64_tr_b16 a[184:185], v0 offset:17344
s_lshl_b32 s53, s35, 1
v_mfma_f32_16x16x32_bf16 v[28:31], a[132:135], v[172:175], v[28:31]
ds_read_b64_tr_b16 a[186:187], v0 offset:19456
ds_read_b64_tr_b16 a[188:189], v11 offset:17344
v_mfma_f32_16x16x32_bf16 v[32:35], a[136:139], v[168:171], v[32:35]
ds_read_b64_tr_b16 a[190:191], v11 offset:19456
s_and_b32 s54, s80, 1
v_mfma_f32_16x16x32_bf16 v[36:39], a[136:139], v[172:175], v[36:39]
s_add_u32 s53, s53, s54
s_min_u32 s53, s53, 48
v_mfma_f32_16x16x32_bf16 v[40:43], a[140:143], v[168:171], v[40:43]
v_mfma_f32_16x16x32_bf16 v[44:47], a[140:143], v[172:175], v[44:47]
s_waitcnt lgkmcnt(14)
v_mfma_f32_16x16x32_bf16 v[48:51], a[144:147], v[168:171], v[48:51]
s_waitcnt vmcnt(19)
s_barrier
v_mfma_f32_16x16x32_bf16 v[52:55], a[144:147], v[172:175], v[52:55]
ds_read_b64_tr_b16 a[128:129], v9 offset:16896
s_add_u32 m0, s96, 0
v_mfma_f32_16x16x32_bf16 v[56:59], a[148:151], v[168:171], v[56:59]
ds_read_b64_tr_b16 a[130:131], v9 offset:19008
ds_read_b64_tr_b16 a[132:133], v10 offset:16896
v_mfma_f32_16x16x32_bf16 v[60:63], a[148:151], v[172:175], v[60:63]
buffer_load_dwordx4 v8, s[88:91], s83 offen offset:0 lds
v_mfma_f32_16x16x32_bf16 v[64:67], a[152:155], v[168:171], v[64:67]
ds_read_b64_tr_b16 a[134:135], v10 offset:19008
ds_read_b64_tr_b16 a[136:137], v9 offset:16960
v_mfma_f32_16x16x32_bf16 v[68:71], a[152:155], v[172:175], v[68:71]
ds_read_b64_tr_b16 a[138:139], v9 offset:19072
ds_read_b64_tr_b16 a[140:141], v10 offset:16960
v_mfma_f32_16x16x32_bf16 v[72:75], a[156:159], v[168:171], v[72:75]
ds_read_b64_tr_b16 a[142:143], v10 offset:19072
s_add_u32 s83, s83, 15296
v_mfma_f32_16x16x32_bf16 v[76:79], a[156:159], v[172:175], v[76:79]
ds_read_b64_tr_b16 a[144:145], v9 offset:17024
s_waitcnt lgkmcnt(14)
v_mfma_f32_16x16x32_bf16 v[80:83], a[160:163], v[168:171], v[80:83]
ds_read_b64_tr_b16 a[146:147], v9 offset:19136
ds_read_b64_tr_b16 a[148:149], v10 offset:17024
v_mfma_f32_16x16x32_bf16 v[84:87], a[160:163], v[172:175], v[84:87]
buffer_load_dwordx4 v8, s[88:91], s83 offen offset:1088 lds
v_mfma_f32_16x16x32_bf16 v[88:91], a[164:167], v[168:171], v[88:91]
ds_read_b64_tr_b16 a[150:151], v10 offset:19136
ds_read_b64_tr_b16 a[152:153], v9 offset:17088
v_mfma_f32_16x16x32_bf16 v[92:95], a[164:167], v[172:175], v[92:95]
ds_read_b64_tr_b16 a[154:155], v9 offset:19200
s_add_u32 s83, s83, 15360
v_mfma_f32_16x16x32_bf16 v[96:99], a[168:171], v[168:171], v[96:99]
ds_read_b64_tr_b16 a[156:157], v10 offset:17088
ds_read_b64_tr_b16 a[158:159], v10 offset:19200
v_mfma_f32_16x16x32_bf16 v[100:103], a[168:171], v[172:175], v[100:103]
ds_read_b64_tr_b16 a[160:161], v9 offset:17152
v_mfma_f32_16x16x32_bf16 v[104:107], a[172:175], v[168:171], v[104:107]
buffer_load_dwordx4 v8, s[88:91], s83 offen offset:2112 lds
v_mfma_f32_16x16x32_bf16 v[108:111], a[172:175], v[172:175], v[108:111]
ds_read_b64_tr_b16 a[162:163], v9 offset:19264
s_waitcnt lgkmcnt(14)
v_mfma_f32_16x16x32_bf16 v[112:115], a[176:179], v[168:171], v[112:115]
ds_read_b64_tr_b16 a[164:165], v10 offset:17152
ds_read_b64_tr_b16 a[166:167], v10 offset:19264
v_mfma_f32_16x16x32_bf16 v[116:119], a[176:179], v[172:175], v[116:119]
ds_read_b64_tr_b16 a[168:169], v9 offset:17216
s_add_u32 s83, s83, 15296
v_mfma_f32_16x16x32_bf16 v[120:123], a[180:183], v[168:171], v[120:123]
ds_read_b64_tr_b16 a[170:171], v9 offset:19328
ds_read_b64_tr_b16 a[172:173], v10 offset:17216
v_mfma_f32_16x16x32_bf16 v[124:127], a[180:183], v[172:175], v[124:127]
ds_read_b64_tr_b16 a[174:175], v10 offset:19328
v_mfma_f32_16x16x32_bf16 v[128:131], a[184:187], v[168:171], v[128:131]
buffer_load_dwordx4 v8, s[88:91], s83 offen offset:3200 lds
v_mfma_f32_16x16x32_bf16 v[132:135], a[184:187], v[172:175], v[132:135]
s_add_u32 s83, s83, 216192
v_mfma_f32_16x16x32_bf16 v[136:139], a[188:191], v[168:171], v[136:139]
v_mfma_f32_16x16x32_bf16 v[140:143], a[188:191], v[172:175], v[140:143]
s_waitcnt lgkmcnt(14)
v_mfma_f32_16x16x32_bf16 v[16:19], a[128:131], v[176:179], v[16:19]
ds_read_b64_tr_b16 a[176:177], v9 offset:17280
ds_read_b64_tr_b16 a[178:179], v9 offset:19392
v_mfma_f32_16x16x32_bf16 v[20:23], a[128:131], v[180:183], v[20:23]
ds_read_b64_tr_b16 a[180:181], v10 offset:17280
ds_read_b64_tr_b16 a[182:183], v10 offset:19392
v_mfma_f32_16x16x32_bf16 v[24:27], a[132:135], v[176:179], v[24:27]
ds_read_b64_tr_b16 a[184:185], v9 offset:17344
s_lshl_b32 s53, s53, 15
v_mfma_f32_16x16x32_bf16 v[28:31], a[132:135], v[180:183], v[28:31]
ds_read_b64_tr_b16 a[186:187], v9 offset:19456
ds_read_b64_tr_b16 a[188:189], v10 offset:17344
v_mfma_f32_16x16x32_bf16 v[32:35], a[136:139], v[176:179], v[32:35]
ds_read_b64_tr_b16 a[190:191], v10 offset:19456
s_lshl_b32 s54, s82, 21
v_mfma_f32_16x16x32_bf16 v[36:39], a[136:139], v[180:183], v[36:39]
s_add_u32 s53, s53, s54
s_add_u32 s1, s53, 20971520
v_mfma_f32_16x16x32_bf16 v[40:43], a[140:143], v[176:179], v[40:43]
v_mfma_f32_16x16x32_bf16 v[44:47], a[140:143], v[180:183], v[44:47]
s_waitcnt lgkmcnt(14)
v_mfma_f32_16x16x32_bf16 v[48:51], a[144:147], v[176:179], v[48:51]
s_waitcnt vmcnt(19)
s_barrier
v_mfma_f32_16x16x32_bf16 v[52:55], a[144:147], v[180:183], v[52:55]
ds_read_b64_tr_b16 a[128:129], v9 offset:0
s_add_u32 m0, s96, 16896
v_mfma_f32_16x16x32_bf16 v[56:59], a[148:151], v[176:179], v[56:59]
ds_read_b64_tr_b16 a[130:131], v9 offset:2112
ds_read_b64_tr_b16 a[132:133], v10 offset:0
v_mfma_f32_16x16x32_bf16 v[60:63], a[148:151], v[180:183], v[60:63]
buffer_load_dwordx4 v8, s[88:91], s83 offen offset:0 lds
v_mfma_f32_16x16x32_bf16 v[64:67], a[152:155], v[176:179], v[64:67]
ds_read_b64_tr_b16 a[134:135], v10 offset:2112
ds_read_b64_tr_b16 a[136:137], v9 offset:64
v_mfma_f32_16x16x32_bf16 v[68:71], a[152:155], v[180:183], v[68:71]
ds_read_b64_tr_b16 a[138:139], v9 offset:2176
ds_read_b64_tr_b16 a[140:141], v10 offset:64
v_mfma_f32_16x16x32_bf16 v[72:75], a[156:159], v[176:179], v[72:75]
ds_read_b64_tr_b16 a[142:143], v10 offset:2176
s_add_u32 s83, s83, 15296
v_mfma_f32_16x16x32_bf16 v[76:79], a[156:159], v[180:183], v[76:79]
ds_read_b64_tr_b16 a[144:145], v9 offset:128
s_waitcnt lgkmcnt(14)
v_mfma_f32_16x16x32_bf16 v[80:83], a[160:163], v[176:179], v[80:83]
ds_read_b64_tr_b16 a[146:147], v9 offset:2240
ds_read_b64_tr_b16 a[148:149], v10 offset:128
v_mfma_f32_16x16x32_bf16 v[84:87], a[160:163], v[180:183], v[84:87]
buffer_load_dwordx4 v8, s[88:91], s83 offen offset:1088 lds
v_mfma_f32_16x16x32_bf16 v[88:91], a[164:167], v[176:179], v[88:91]
ds_read_b64_tr_b16 a[150:151], v10 offset:2240
ds_read_b64_tr_b16 a[152:153], v9 offset:192
v_mfma_f32_16x16x32_bf16 v[92:95], a[164:167], v[180:183], v[92:95]
ds_read_b64_tr_b16 a[154:155], v9 offset:2304
s_add_u32 s83, s83, 15360
v_mfma_f32_16x16x32_bf16 v[96:99], a[168:171], v[176:179], v[96:99]
ds_read_b64_tr_b16 a[156:157], v10 offset:192
ds_read_b64_tr_b16 a[158:159], v10 offset:2304
v_mfma_f32_16x16x32_bf16 v[100:103], a[168:171], v[180:183], v[100:103]
ds_read_b64_tr_b16 a[160:161], v9 offset:256
v_mfma_f32_16x16x32_bf16 v[104:107], a[172:175], v[176:179], v[104:107]
buffer_load_dwordx4 v8, s[88:91], s83 offen offset:2112 lds
v_mfma_f32_16x16x32_bf16 v[108:111], a[172:175], v[180:183], v[108:111]
ds_read_b64_tr_b16 a[162:163], v9 offset:2368
s_waitcnt lgkmcnt(14)
v_mfma_f32_16x16x32_bf16 v[112:115], a[176:179], v[176:179], v[112:115]
ds_read_b64_tr_b16 a[164:165], v10 offset:256
ds_read_b64_tr_b16 a[166:167], v10 offset:2368
v_mfma_f32_16x16x32_bf16 v[116:119], a[176:179], v[180:183], v[116:119]
ds_read_b64_tr_b16 a[168:169], v9 offset:320
s_add_u32 s83, s83, 15296
v_mfma_f32_16x16x32_bf16 v[120:123], a[180:183], v[176:179], v[120:123]
ds_read_b64_tr_b16 a[170:171], v9 offset:2432
ds_read_b64_tr_b16 a[172:173], v10 offset:320
v_mfma_f32_16x16x32_bf16 v[124:127], a[180:183], v[180:183], v[124:127]
ds_read_b64_tr_b16 a[174:175], v10 offset:2432
v_mfma_f32_16x16x32_bf16 v[128:131], a[184:187], v[176:179], v[128:131]
buffer_load_dwordx4 v8, s[88:91], s83 offen offset:3200 lds
v_mfma_f32_16x16x32_bf16 v[132:135], a[184:187], v[180:183], v[132:135]
s_add_u32 s83, s83, 216192
v_mfma_f32_16x16x32_bf16 v[136:139], a[188:191], v[176:179], v[136:139]
v_mfma_f32_16x16x32_bf16 v[140:143], a[188:191], v[180:183], v[140:143]
s_waitcnt lgkmcnt(14)
v_mfma_f32_16x16x32_bf16 v[16:19], a[128:131], v[184:187], v[16:19]
ds_read_b64_tr_b16 a[176:177], v9 offset:384
ds_read_b64_tr_b16 a[178:179], v9 offset:2496
v_mfma_f32_16x16x32_bf16 v[20:23], a[128:131], v[188:191], v[20:23]
ds_read_b64_tr_b16 a[180:181], v10 offset:384
ds_read_b64_tr_b16 a[182:183], v10 offset:2496
v_mfma_f32_16x16x32_bf16 v[24:27], a[132:135], v[184:187], v[24:27]
ds_read_b64_tr_b16 a[184:185], v9 offset:448
s_mov_b32 s0, s1
v_mfma_f32_16x16x32_bf16 v[28:31], a[132:135], v[188:191], v[28:31]
ds_read_b64_tr_b16 a[186:187], v9 offset:2560
ds_read_b64_tr_b16 a[188:189], v10 offset:448
v_mfma_f32_16x16x32_bf16 v[32:35], a[136:139], v[184:187], v[32:35]
ds_read_b64_tr_b16 a[190:191], v10 offset:2560
s_lshr_b32 s53, s35, 1
v_mfma_f32_16x16x32_bf16 v[36:39], a[136:139], v[188:191], v[36:39]
s_lshl_b32 s53, s53, 2
s_add_u32 s53, s53, s80
v_mfma_f32_16x16x32_bf16 v[40:43], a[140:143], v[184:187], v[40:43]
v_mfma_f32_16x16x32_bf16 v[44:47], a[140:143], v[188:191], v[44:47]
s_waitcnt lgkmcnt(14)
v_mfma_f32_16x16x32_bf16 v[48:51], a[144:147], v[184:187], v[48:51]
s_waitcnt vmcnt(12)
s_barrier
v_mfma_f32_16x16x32_bf16 v[52:55], a[144:147], v[188:191], v[52:55]
ds_read_b64_tr_b16 a[128:129], v9 offset:50688
s_add_u32 m0, s96, 107008
v_mfma_f32_16x16x32_bf16 v[56:59], a[148:151], v[184:187], v[56:59]
ds_read_b64_tr_b16 a[130:131], v9 offset:52800
ds_read_b64_tr_b16 a[132:133], v10 offset:50688
v_mfma_f32_16x16x32_bf16 v[60:63], a[148:151], v[188:191], v[60:63]
buffer_load_dwordx4 v8, s[88:91], s83 offen offset:0 lds
v_mfma_f32_16x16x32_bf16 v[64:67], a[152:155], v[184:187], v[64:67]
ds_read_b64_tr_b16 a[134:135], v10 offset:52800
ds_read_b64_tr_b16 a[136:137], v9 offset:50752
v_mfma_f32_16x16x32_bf16 v[68:71], a[152:155], v[188:191], v[68:71]
ds_read_b64_tr_b16 a[138:139], v9 offset:52864
ds_read_b64_tr_b16 a[140:141], v10 offset:50752
v_mfma_f32_16x16x32_bf16 v[72:75], a[156:159], v[184:187], v[72:75]
ds_read_b64_tr_b16 a[142:143], v10 offset:52864
s_add_u32 s83, s83, 15296
v_mfma_f32_16x16x32_bf16 v[76:79], a[156:159], v[188:191], v[76:79]
ds_read_b64_tr_b16 a[144:145], v9 offset:50816
s_waitcnt lgkmcnt(14)
v_mfma_f32_16x16x32_bf16 v[80:83], a[160:163], v[184:187], v[80:83]
ds_read_b64_tr_b16 a[146:147], v9 offset:52928
ds_read_b64_tr_b16 a[148:149], v10 offset:50816
v_mfma_f32_16x16x32_bf16 v[84:87], a[160:163], v[188:191], v[84:87]
buffer_load_dwordx4 v8, s[88:91], s83 offen offset:1088 lds
v_mfma_f32_16x16x32_bf16 v[88:91], a[164:167], v[184:187], v[88:91]
ds_read_b64_tr_b16 a[150:151], v10 offset:52928
ds_read_b64_tr_b16 a[152:153], v9 offset:50880
v_mfma_f32_16x16x32_bf16 v[92:95], a[164:167], v[188:191], v[92:95]
ds_read_b64_tr_b16 a[154:155], v9 offset:52992
s_add_u32 s83, s83, 15360
v_mfma_f32_16x16x32_bf16 v[96:99], a[168:171], v[184:187], v[96:99]
ds_read_b64_tr_b16 a[156:157], v10 offset:50880
ds_read_b64_tr_b16 a[158:159], v10 offset:52992
v_mfma_f32_16x16x32_bf16 v[100:103], a[168:171], v[188:191], v[100:103]
ds_read_b64_tr_b16 a[160:161], v9 offset:50944
v_mfma_f32_16x16x32_bf16 v[104:107], a[172:175], v[184:187], v[104:107]
buffer_load_dwordx4 v8, s[88:91], s83 offen offset:2112 lds
v_mfma_f32_16x16x32_bf16 v[108:111], a[172:175], v[188:191], v[108:111]
ds_read_b64_tr_b16 a[162:163], v9 offset:53056
s_waitcnt lgkmcnt(14)
v_mfma_f32_16x16x32_bf16 v[112:115], a[176:179], v[184:187], v[112:115]
ds_read_b64_tr_b16 a[164:165], v10 offset:50944
ds_read_b64_tr_b16 a[166:167], v10 offset:53056
v_mfma_f32_16x16x32_bf16 v[116:119], a[176:179], v[188:191], v[116:119]
ds_read_b64_tr_b16 a[168:169], v9 offset:51008
s_add_u32 s83, s83, 15296
v_mfma_f32_16x16x32_bf16 v[120:123], a[180:183], v[184:187], v[120:123]
ds_read_b64_tr_b16 a[170:171], v9 offset:53120
ds_read_b64_tr_b16 a[172:173], v10 offset:51008
v_mfma_f32_16x16x32_bf16 v[124:127], a[180:183], v[188:191], v[124:127]
ds_read_b64_tr_b16 a[174:175], v10 offset:53120
v_mfma_f32_16x16x32_bf16 v[128:131], a[184:187], v[184:187], v[128:131]
buffer_load_dwordx4 v8, s[88:91], s83 offen offset:3200 lds
v_mfma_f32_16x16x32_bf16 v[132:135], a[184:187], v[188:191], v[132:135]
s_add_u32 s83, s83, 216192
v_mfma_f32_16x16x32_bf16 v[136:139], a[188:191], v[184:187], v[136:139]
v_mfma_f32_16x16x32_bf16 v[140:143], a[188:191], v[188:191], v[140:143]
s_waitcnt lgkmcnt(14)
v_mfma_f32_16x16x32_bf16 v[16:19], a[128:131], v[192:195], v[16:19]
ds_read_b64_tr_b16 a[176:177], v9 offset:51072
ds_read_b64_tr_b16 a[178:179], v9 offset:53184
v_mfma_f32_16x16x32_bf16 v[20:23], a[128:131], v[196:199], v[20:23]
ds_read_b64_tr_b16 a[180:181], v10 offset:51072
ds_read_b64_tr_b16 a[182:183], v10 offset:53184
v_mfma_f32_16x16x32_bf16 v[24:27], a[132:135], v[192:195], v[24:27]
ds_read_b64_tr_b16 a[184:185], v9 offset:51136
s_mul_i32 s53, s53, 3584
v_mfma_f32_16x16x32_bf16 v[28:31], a[132:135], v[196:199], v[28:31]
ds_read_b64_tr_b16 a[186:187], v9 offset:53248
ds_read_b64_tr_b16 a[188:189], v10 offset:51136
v_mfma_f32_16x16x32_bf16 v[32:35], a[136:139], v[192:195], v[32:35]
ds_read_b64_tr_b16 a[190:191], v10 offset:53248
s_mul_i32 s54, s32, 229376
v_mfma_f32_16x16x32_bf16 v[36:39], a[136:139], v[196:199], v[36:39]
s_add_u32 s53, s53, s54
s_add_u32 s2, s53, 16777216
v_mfma_f32_16x16x32_bf16 v[40:43], a[140:143], v[192:195], v[40:43]
v_mfma_f32_16x16x32_bf16 v[44:47], a[140:143], v[196:199], v[44:47]
s_waitcnt lgkmcnt(14)
v_mfma_f32_16x16x32_bf16 v[48:51], a[144:147], v[192:195], v[48:51]
s_waitcnt vmcnt(12)
s_barrier
v_mfma_f32_16x16x32_bf16 v[52:55], a[144:147], v[196:199], v[52:55]
ds_read_b64_tr_b16 a[128:129], v9 offset:33792
s_add_u32 m0, s96, 90112
v_mfma_f32_16x16x32_bf16 v[56:59], a[148:151], v[192:195], v[56:59]
ds_read_b64_tr_b16 a[130:131], v9 offset:35904
ds_read_b64_tr_b16 a[132:133], v10 offset:33792
v_mfma_f32_16x16x32_bf16 v[60:63], a[148:151], v[196:199], v[60:63]
buffer_load_dwordx4 v8, s[88:91], s83 offen offset:0 lds
v_mfma_f32_16x16x32_bf16 v[64:67], a[152:155], v[192:195], v[64:67]
ds_read_b64_tr_b16 a[134:135], v10 offset:35904
ds_read_b64_tr_b16 a[136:137], v9 offset:33856
v_mfma_f32_16x16x32_bf16 v[68:71], a[152:155], v[196:199], v[68:71]
ds_read_b64_tr_b16 a[138:139], v9 offset:35968
ds_read_b64_tr_b16 a[140:141], v10 offset:33856
v_mfma_f32_16x16x32_bf16 v[72:75], a[156:159], v[192:195], v[72:75]
ds_read_b64_tr_b16 a[142:143], v10 offset:35968
s_add_u32 s83, s83, 15296
v_mfma_f32_16x16x32_bf16 v[76:79], a[156:159], v[196:199], v[76:79]
ds_read_b64_tr_b16 a[144:145], v9 offset:33920
s_waitcnt lgkmcnt(14)
v_mfma_f32_16x16x32_bf16 v[80:83], a[160:163], v[192:195], v[80:83]
ds_read_b64_tr_b16 a[146:147], v9 offset:36032
ds_read_b64_tr_b16 a[148:149], v10 offset:33920
v_mfma_f32_16x16x32_bf16 v[84:87], a[160:163], v[196:199], v[84:87]
buffer_load_dwordx4 v8, s[88:91], s83 offen offset:1088 lds
v_mfma_f32_16x16x32_bf16 v[88:91], a[164:167], v[192:195], v[88:91]
ds_read_b64_tr_b16 a[150:151], v10 offset:36032
ds_read_b64_tr_b16 a[152:153], v9 offset:33984
v_mfma_f32_16x16x32_bf16 v[92:95], a[164:167], v[196:199], v[92:95]
ds_read_b64_tr_b16 a[154:155], v9 offset:36096
s_add_u32 s83, s83, 15360
v_mfma_f32_16x16x32_bf16 v[96:99], a[168:171], v[192:195], v[96:99]
ds_read_b64_tr_b16 a[156:157], v10 offset:33984
ds_read_b64_tr_b16 a[158:159], v10 offset:36096
v_mfma_f32_16x16x32_bf16 v[100:103], a[168:171], v[196:199], v[100:103]
ds_read_b64_tr_b16 a[160:161], v9 offset:34048
v_mfma_f32_16x16x32_bf16 v[104:107], a[172:175], v[192:195], v[104:107]
buffer_load_dwordx4 v8, s[88:91], s83 offen offset:2112 lds
v_mfma_f32_16x16x32_bf16 v[108:111], a[172:175], v[196:199], v[108:111]
ds_read_b64_tr_b16 a[162:163], v9 offset:36160
s_waitcnt lgkmcnt(14)
v_mfma_f32_16x16x32_bf16 v[112:115], a[176:179], v[192:195], v[112:115]
ds_read_b64_tr_b16 a[164:165], v10 offset:34048
ds_read_b64_tr_b16 a[166:167], v10 offset:36160
v_mfma_f32_16x16x32_bf16 v[116:119], a[176:179], v[196:199], v[116:119]
ds_read_b64_tr_b16 a[168:169], v9 offset:34112
s_add_u32 s83, s83, 15296
v_mfma_f32_16x16x32_bf16 v[120:123], a[180:183], v[192:195], v[120:123]
ds_read_b64_tr_b16 a[170:171], v9 offset:36224
ds_read_b64_tr_b16 a[172:173], v10 offset:34112
v_mfma_f32_16x16x32_bf16 v[124:127], a[180:183], v[196:199], v[124:127]
ds_read_b64_tr_b16 a[174:175], v10 offset:36224
v_mfma_f32_16x16x32_bf16 v[128:131], a[184:187], v[192:195], v[128:131]
buffer_load_dwordx4 v8, s[88:91], s83 offen offset:3200 lds
v_mfma_f32_16x16x32_bf16 v[132:135], a[184:187], v[196:199], v[132:135]
s_add_u32 s83, s83, 216192
v_mfma_f32_16x16x32_bf16 v[136:139], a[188:191], v[192:195], v[136:139]
v_mfma_f32_16x16x32_bf16 v[140:143], a[188:191], v[196:199], v[140:143]
s_waitcnt lgkmcnt(14)
v_mfma_f32_16x16x32_bf16 v[16:19], a[128:131], v[200:203], v[16:19]
ds_read_b64_tr_b16 a[176:177], v9 offset:34176
ds_read_b64_tr_b16 a[178:179], v9 offset:36288
v_mfma_f32_16x16x32_bf16 v[20:23], a[128:131], v[204:207], v[20:23]
ds_read_b64_tr_b16 a[180:181], v10 offset:34176
ds_read_b64_tr_b16 a[182:183], v10 offset:36288
v_mfma_f32_16x16x32_bf16 v[24:27], a[132:135], v[200:203], v[24:27]
ds_read_b64_tr_b16 a[184:185], v9 offset:34240
v_mfma_f32_16x16x32_bf16 v[28:31], a[132:135], v[204:207], v[28:31]
ds_read_b64_tr_b16 a[186:187], v9 offset:36352
ds_read_b64_tr_b16 a[188:189], v10 offset:34240
v_mfma_f32_16x16x32_bf16 v[32:35], a[136:139], v[200:203], v[32:35]
ds_read_b64_tr_b16 a[190:191], v10 offset:36352
v_mfma_f32_16x16x32_bf16 v[36:39], a[136:139], v[204:207], v[36:39]
v_mfma_f32_16x16x32_bf16 v[40:43], a[140:143], v[200:203], v[40:43]
v_mfma_f32_16x16x32_bf16 v[44:47], a[140:143], v[204:207], v[44:47]
s_waitcnt lgkmcnt(14)
v_mfma_f32_16x16x32_bf16 v[48:51], a[144:147], v[200:203], v[48:51]
s_waitcnt vmcnt(12)
s_barrier
v_mfma_f32_16x16x32_bf16 v[52:55], a[144:147], v[204:207], v[52:55]
ds_read_b64_tr_b16 a[128:129], v0 offset:0
s_add_u32 m0, s96, 140800
v_mfma_f32_16x16x32_bf16 v[56:59], a[148:151], v[200:203], v[56:59]
ds_read_b64_tr_b16 a[130:131], v0 offset:2112
ds_read_b64_tr_b16 a[132:133], v11 offset:0
v_mfma_f32_16x16x32_bf16 v[60:63], a[148:151], v[204:207], v[60:63]
buffer_load_dwordx4 v8, s[88:91], s83 offen offset:0 lds
v_mfma_f32_16x16x32_bf16 v[64:67], a[152:155], v[200:203], v[64:67]
ds_read_b64_tr_b16 a[134:135], v11 offset:2112
ds_read_b64_tr_b16 a[136:137], v0 offset:64
v_mfma_f32_16x16x32_bf16 v[68:71], a[152:155], v[204:207], v[68:71]
ds_read_b64_tr_b16 a[138:139], v0 offset:2176
ds_read_b64_tr_b16 a[140:141], v11 offset:64
v_mfma_f32_16x16x32_bf16 v[72:75], a[156:159], v[200:203], v[72:75]
ds_read_b64_tr_b16 a[142:143], v11 offset:2176
s_add_u32 s83, s83, 15296
v_mfma_f32_16x16x32_bf16 v[76:79], a[156:159], v[204:207], v[76:79]
ds_read_b64_tr_b16 a[144:145], v0 offset:128
s_waitcnt lgkmcnt(14)
v_mfma_f32_16x16x32_bf16 v[80:83], a[160:163], v[200:203], v[80:83]
ds_read_b64_tr_b16 a[146:147], v0 offset:2240
ds_read_b64_tr_b16 a[148:149], v11 offset:128
v_mfma_f32_16x16x32_bf16 v[84:87], a[160:163], v[204:207], v[84:87]
buffer_load_dwordx4 v8, s[88:91], s83 offen offset:1088 lds
v_mfma_f32_16x16x32_bf16 v[88:91], a[164:167], v[200:203], v[88:91]
ds_read_b64_tr_b16 a[150:151], v11 offset:2240
ds_read_b64_tr_b16 a[152:153], v0 offset:192
v_mfma_f32_16x16x32_bf16 v[92:95], a[164:167], v[204:207], v[92:95]
ds_read_b64_tr_b16 a[154:155], v0 offset:2304
s_add_u32 s83, s83, 15360
v_mfma_f32_16x16x32_bf16 v[96:99], a[168:171], v[200:203], v[96:99]
ds_read_b64_tr_b16 a[156:157], v11 offset:192
ds_read_b64_tr_b16 a[158:159], v11 offset:2304
v_mfma_f32_16x16x32_bf16 v[100:103], a[168:171], v[204:207], v[100:103]
ds_read_b64_tr_b16 a[160:161], v0 offset:256
v_mfma_f32_16x16x32_bf16 v[104:107], a[172:175], v[200:203], v[104:107]
buffer_load_dwordx4 v8, s[88:91], s83 offen offset:2112 lds
v_mfma_f32_16x16x32_bf16 v[108:111], a[172:175], v[204:207], v[108:111]
ds_read_b64_tr_b16 a[162:163], v0 offset:2368
s_waitcnt lgkmcnt(14)
v_mfma_f32_16x16x32_bf16 v[112:115], a[176:179], v[200:203], v[112:115]
ds_read_b64_tr_b16 a[164:165], v11 offset:256
ds_read_b64_tr_b16 a[166:167], v11 offset:2368
v_mfma_f32_16x16x32_bf16 v[116:119], a[176:179], v[204:207], v[116:119]
ds_read_b64_tr_b16 a[168:169], v0 offset:320
v_accvgpr_read_b32 v13, a48
v_mfma_f32_16x16x32_bf16 v[120:123], a[180:183], v[200:203], v[120:123]
ds_read_b64_tr_b16 a[170:171], v0 offset:2432
v_accvgpr_read_b32 v14, a49
v_mfma_f32_16x16x32_bf16 v[124:127], a[180:183], v[204:207], v[124:127]
ds_read_b64_tr_b16 a[172:173], v11 offset:320
ds_read_b64_tr_b16 a[174:175], v11 offset:2432
v_mfma_f32_16x16x32_bf16 v[128:131], a[184:187], v[200:203], v[128:131]
s_add_u32 s83, s83, 15296
v_mfma_f32_16x16x32_bf16 v[132:135], a[184:187], v[204:207], v[132:135]
buffer_load_dwordx4 v8, s[88:91], s83 offen offset:3200 lds
v_mfma_f32_16x16x32_bf16 v[136:139], a[188:191], v[200:203], v[136:139]
s_add_u32 s83, s83, 216192
v_mfma_f32_16x16x32_bf16 v[140:143], a[188:191], v[204:207], v[140:143]
s_waitcnt lgkmcnt(14)
v_mfma_f32_16x16x32_bf16 v[16:19], a[128:131], v[208:211], v[16:19]
ds_read_b64_tr_b16 a[176:177], v0 offset:384
ds_read_b64_tr_b16 a[178:179], v0 offset:2496
v_mfma_f32_16x16x32_bf16 v[20:23], a[128:131], v[212:215], v[20:23]
ds_read_b64_tr_b16 a[180:181], v11 offset:384
ds_read_b64_tr_b16 a[182:183], v11 offset:2496
v_mfma_f32_16x16x32_bf16 v[24:27], a[132:135], v[208:211], v[24:27]
ds_read_b64_tr_b16 a[184:185], v0 offset:448
v_mfma_f32_16x16x32_bf16 v[28:31], a[132:135], v[212:215], v[28:31]
ds_read_b64_tr_b16 a[186:187], v0 offset:2560
ds_read_b64_tr_b16 a[188:189], v11 offset:448
v_mfma_f32_16x16x32_bf16 v[32:35], a[136:139], v[208:211], v[32:35]
ds_read_b64_tr_b16 a[190:191], v11 offset:2560
v_mfma_f32_16x16x32_bf16 v[36:39], a[136:139], v[212:215], v[36:39]
v_mfma_f32_16x16x32_bf16 v[40:43], a[140:143], v[208:211], v[40:43]
v_mfma_f32_16x16x32_bf16 v[44:47], a[140:143], v[212:215], v[44:47]
s_waitcnt lgkmcnt(14)
v_mfma_f32_16x16x32_bf16 v[48:51], a[144:147], v[208:211], v[48:51]
s_waitcnt vmcnt(12)
s_barrier
v_mfma_f32_16x16x32_bf16 v[52:55], a[144:147], v[212:215], v[52:55]
ds_read_b64_tr_b16 a[128:129], v0 offset:16896
s_add_u32 m0, s96, 123904
v_mfma_f32_16x16x32_bf16 v[56:59], a[148:151], v[208:211], v[56:59]
ds_read_b64_tr_b16 a[130:131], v0 offset:19008
ds_read_b64_tr_b16 a[132:133], v11 offset:16896
v_mfma_f32_16x16x32_bf16 v[60:63], a[148:151], v[212:215], v[60:63]
buffer_load_dwordx4 v8, s[88:91], s83 offen offset:0 lds
v_mfma_f32_16x16x32_bf16 v[64:67], a[152:155], v[208:211], v[64:67]
ds_read_b64_tr_b16 a[64:65], v13 offset:0
ds_read_b64_tr_b16 a[134:135], v11 offset:19008
v_mfma_f32_16x16x32_bf16 v[68:71], a[152:155], v[212:215], v[68:71]
ds_read_b64_tr_b16 a[136:137], v0 offset:16960
ds_read_b64_tr_b16 a[138:139], v0 offset:19072
v_mfma_f32_16x16x32_bf16 v[72:75], a[156:159], v[208:211], v[72:75]
ds_read_b64_tr_b16 a[66:67], v14 offset:0
ds_read_b64_tr_b16 a[140:141], v11 offset:16960
v_mfma_f32_16x16x32_bf16 v[76:79], a[156:159], v[212:215], v[76:79]
ds_read_b64_tr_b16 a[142:143], v11 offset:19072
s_waitcnt lgkmcnt(14)
v_mfma_f32_16x16x32_bf16 v[80:83], a[160:163], v[208:211], v[80:83]
ds_read_b64_tr_b16 a[144:145], v0 offset:17024
ds_read_b64_tr_b16 a[68:69], v13 offset:32
v_mfma_f32_16x16x32_bf16 v[84:87], a[160:163], v[212:215], v[84:87]
ds_read_b64_tr_b16 a[146:147], v0 offset:19136
ds_read_b64_tr_b16 a[148:149], v11 offset:17024
v_mfma_f32_16x16x32_bf16 v[88:91], a[164:167], v[208:211], v[88:91]
ds_read_b64_tr_b16 a[150:151], v11 offset:19136
ds_read_b64_tr_b16 a[70:71], v14 offset:32
v_mfma_f32_16x16x32_bf16 v[92:95], a[164:167], v[212:215], v[92:95]
ds_read_b64_tr_b16 a[152:153], v0 offset:17088
s_add_u32 s83, s83, 15296
v_mfma_f32_16x16x32_bf16 v[96:99], a[168:171], v[208:211], v[96:99]
buffer_load_dwordx4 v8, s[88:91], s83 offen offset:1088 lds
v_mfma_f32_16x16x32_bf16 v[100:103], a[168:171], v[212:215], v[100:103]
ds_read_b64_tr_b16 a[154:155], v0 offset:19200
ds_read_b64_tr_b16 a[156:157], v11 offset:17088
v_mfma_f32_16x16x32_bf16 v[104:107], a[172:175], v[208:211], v[104:107]
ds_read_b64_tr_b16 a[72:73], v13 offset:64
ds_read_b64_tr_b16 a[158:159], v11 offset:19200
v_mfma_f32_16x16x32_bf16 v[108:111], a[172:175], v[212:215], v[108:111]
ds_read_b64_tr_b16 a[160:161], v0 offset:17152
s_waitcnt lgkmcnt(14)
v_mfma_f32_16x16x32_bf16 v[112:115], a[176:179], v[208:211], v[112:115]
ds_read_b64_tr_b16 a[162:163], v0 offset:19264
ds_read_b64_tr_b16 a[74:75], v14 offset:64
v_mfma_f32_16x16x32_bf16 v[116:119], a[176:179], v[212:215], v[116:119]
ds_read_b64_tr_b16 a[164:165], v11 offset:17152
s_add_u32 s83, s83, 15360
v_mfma_f32_16x16x32_bf16 v[120:123], a[180:183], v[208:211], v[120:123]
buffer_load_dwordx4 v8, s[88:91], s83 offen offset:2112 lds
v_mfma_f32_16x16x32_bf16 v[124:127], a[180:183], v[212:215], v[124:127]
ds_read_b64_tr_b16 a[166:167], v11 offset:19264
ds_read_b64_tr_b16 a[168:169], v0 offset:17216
v_mfma_f32_16x16x32_bf16 v[128:131], a[184:187], v[208:211], v[128:131]
ds_read_b64_tr_b16 a[76:77], v13 offset:96
ds_read_b64_tr_b16 a[170:171], v0 offset:19328
v_mfma_f32_16x16x32_bf16 v[132:135], a[184:187], v[212:215], v[132:135]
ds_read_b64_tr_b16 a[172:173], v11 offset:17216
ds_read_b64_tr_b16 a[174:175], v11 offset:19328
v_mfma_f32_16x16x32_bf16 v[136:139], a[188:191], v[208:211], v[136:139]
s_add_u32 s83, s83, 15296
v_mfma_f32_16x16x32_bf16 v[140:143], a[188:191], v[212:215], v[140:143]
buffer_load_dwordx4 v8, s[88:91], s83 offen offset:3200 lds
s_add_u32 s83, s83, 216192
s_waitcnt lgkmcnt(14)
v_mfma_f32_16x16x32_bf16 v[16:19], a[128:131], v[216:219], v[16:19]
ds_read_b64_tr_b16 a[176:177], v0 offset:17280
ds_read_b64_tr_b16 a[178:179], v0 offset:19392
v_mfma_f32_16x16x32_bf16 v[20:23], a[128:131], v[220:223], v[20:23]
ds_read_b64_tr_b16 a[180:181], v11 offset:17280
ds_read_b64_tr_b16 a[182:183], v11 offset:19392
v_mfma_f32_16x16x32_bf16 v[24:27], a[132:135], v[216:219], v[24:27]
ds_read_b64_tr_b16 a[184:185], v0 offset:17344
v_mfma_f32_16x16x32_bf16 v[28:31], a[132:135], v[220:223], v[28:31]
ds_read_b64_tr_b16 a[186:187], v0 offset:19456
ds_read_b64_tr_b16 a[188:189], v11 offset:17344
v_mfma_f32_16x16x32_bf16 v[32:35], a[136:139], v[216:219], v[32:35]
ds_read_b64_tr_b16 a[190:191], v11 offset:19456
v_mfma_f32_16x16x32_bf16 v[36:39], a[136:139], v[220:223], v[36:39]
v_mfma_f32_16x16x32_bf16 v[40:43], a[140:143], v[216:219], v[40:43]
v_mfma_f32_16x16x32_bf16 v[44:47], a[140:143], v[220:223], v[44:47]
s_waitcnt lgkmcnt(14)
v_mfma_f32_16x16x32_bf16 v[48:51], a[144:147], v[216:219], v[48:51]
s_waitcnt vmcnt(12)
s_barrier
v_mfma_f32_16x16x32_bf16 v[52:55], a[144:147], v[220:223], v[52:55]
ds_read_b64_tr_b16 a[128:129], v9 offset:16896
ds_read_b64_tr_b16 a[130:131], v9 offset:19008
v_mfma_f32_16x16x32_bf16 v[56:59], a[148:151], v[216:219], v[56:59]
ds_read_b64_tr_b16 a[132:133], v10 offset:16896
ds_read_b64_tr_b16 a[78:79], v14 offset:96
v_mfma_f32_16x16x32_bf16 v[60:63], a[148:151], v[220:223], v[60:63]
ds_read_b64_tr_b16 a[134:135], v10 offset:19008
ds_read_b64_tr_b16 a[136:137], v9 offset:16960
v_mfma_f32_16x16x32_bf16 v[64:67], a[152:155], v[216:219], v[64:67]
ds_read_b64_tr_b16 a[138:139], v9 offset:19072
ds_read_b64_tr_b16 a[80:81], v13 offset:128
v_mfma_f32_16x16x32_bf16 v[68:71], a[152:155], v[220:223], v[68:71]
ds_read_b64_tr_b16 a[140:141], v10 offset:16960
v_mfma_f32_16x16x32_bf16 v[72:75], a[156:159], v[216:219], v[72:75]
ds_read_b64_tr_b16 a[142:143], v10 offset:19072
ds_read_b64_tr_b16 a[144:145], v9 offset:17024
v_mfma_f32_16x16x32_bf16 v[76:79], a[156:159], v[220:223], v[76:79]
ds_read_b64_tr_b16 a[82:83], v14 offset:128
s_waitcnt lgkmcnt(14)
v_mfma_f32_16x16x32_bf16 v[80:83], a[160:163], v[216:219], v[80:83]
ds_read_b64_tr_b16 a[146:147], v9 offset:19136
ds_read_b64_tr_b16 a[148:149], v10 offset:17024
v_mfma_f32_16x16x32_bf16 v[84:87], a[160:163], v[220:223], v[84:87]
ds_read_b64_tr_b16 a[150:151], v10 offset:19136
v_mfma_f32_16x16x32_bf16 v[88:91], a[164:167], v[216:219], v[88:91]
ds_read_b64_tr_b16 a[84:85], v13 offset:160
ds_read_b64_tr_b16 a[152:153], v9 offset:17088
v_mfma_f32_16x16x32_bf16 v[92:95], a[164:167], v[220:223], v[92:95]
ds_read_b64_tr_b16 a[154:155], v9 offset:19200
v_mfma_f32_16x16x32_bf16 v[96:99], a[168:171], v[216:219], v[96:99]
ds_read_b64_tr_b16 a[156:157], v10 offset:17088
ds_read_b64_tr_b16 a[86:87], v14 offset:160
v_mfma_f32_16x16x32_bf16 v[100:103], a[168:171], v[220:223], v[100:103]
ds_read_b64_tr_b16 a[158:159], v10 offset:19200
v_mfma_f32_16x16x32_bf16 v[104:107], a[172:175], v[216:219], v[104:107]
ds_read_b64_tr_b16 a[160:161], v9 offset:17152
ds_read_b64_tr_b16 a[162:163], v9 offset:19264
v_mfma_f32_16x16x32_bf16 v[108:111], a[172:175], v[220:223], v[108:111]
ds_read_b64_tr_b16 a[88:89], v13 offset:192
s_waitcnt lgkmcnt(14)
v_mfma_f32_16x16x32_bf16 v[112:115], a[176:179], v[216:219], v[112:115]
ds_read_b64_tr_b16 a[164:165], v10 offset:17152
ds_read_b64_tr_b16 a[166:167], v10 offset:19264
v_mfma_f32_16x16x32_bf16 v[116:119], a[176:179], v[220:223], v[116:119]
ds_read_b64_tr_b16 a[168:169], v9 offset:17216
v_mfma_f32_16x16x32_bf16 v[120:123], a[180:183], v[216:219], v[120:123]
ds_read_b64_tr_b16 a[90:91], v14 offset:192
ds_read_b64_tr_b16 a[170:171], v9 offset:19328
v_mfma_f32_16x16x32_bf16 v[124:127], a[180:183], v[220:223], v[124:127]
ds_read_b64_tr_b16 a[172:173], v10 offset:17216
v_mfma_f32_16x16x32_bf16 v[128:131], a[184:187], v[216:219], v[128:131]
ds_read_b64_tr_b16 a[174:175], v10 offset:19328
v_mfma_f32_16x16x32_bf16 v[132:135], a[184:187], v[220:223], v[132:135]
v_mfma_f32_16x16x32_bf16 v[136:139], a[188:191], v[216:219], v[136:139]
v_mfma_f32_16x16x32_bf16 v[140:143], a[188:191], v[220:223], v[140:143]
v_mfma_f32_16x16x32_bf16 v[16:19], a[128:131], v[224:227], v[16:19]
ds_read_b64_tr_b16 a[176:177], v9 offset:17280
ds_read_b64_tr_b16 a[178:179], v9 offset:19392
v_mfma_f32_16x16x32_bf16 v[20:23], a[128:131], v[228:231], v[20:23]
ds_read_b64_tr_b16 a[180:181], v10 offset:17280
ds_read_b64_tr_b16 a[182:183], v10 offset:19392
v_mfma_f32_16x16x32_bf16 v[24:27], a[132:135], v[224:227], v[24:27]
ds_read_b64_tr_b16 a[184:185], v9 offset:17344
v_mfma_f32_16x16x32_bf16 v[28:31], a[132:135], v[228:231], v[28:31]
buffer_store_dwordx4 a[64:67], v1, s[60:63], s48 offen offset:0 sc1
v_mfma_f32_16x16x32_bf16 v[32:35], a[136:139], v[224:227], v[32:35]
ds_read_b64_tr_b16 a[186:187], v9 offset:19456
ds_read_b64_tr_b16 a[188:189], v10 offset:17344
v_mfma_f32_16x16x32_bf16 v[36:39], a[136:139], v[228:231], v[36:39]
ds_read_b64_tr_b16 a[190:191], v10 offset:19456
v_mfma_f32_16x16x32_bf16 v[40:43], a[140:143], v[224:227], v[40:43]
v_mfma_f32_16x16x32_bf16 v[44:47], a[140:143], v[228:231], v[44:47]
s_add_u32 s48, s48, s50
s_waitcnt lgkmcnt(14)
v_mfma_f32_16x16x32_bf16 v[48:51], a[144:147], v[224:227], v[48:51]
s_waitcnt vmcnt(9)
s_barrier
v_mfma_f32_16x16x32_bf16 v[52:55], a[144:147], v[228:231], v[52:55]
ds_read_b64_tr_b16 a[128:129], v9 offset:0
ds_read_b64_tr_b16 a[130:131], v9 offset:2112
v_mfma_f32_16x16x32_bf16 v[56:59], a[148:151], v[224:227], v[56:59]
buffer_store_dwordx4 a[68:71], v1, s[60:63], s48 offen offset:0 sc1
v_mfma_f32_16x16x32_bf16 v[60:63], a[148:151], v[228:231], v[60:63]
ds_read_b64_tr_b16 a[132:133], v10 offset:0
ds_read_b64_tr_b16 a[134:135], v10 offset:2112
v_mfma_f32_16x16x32_bf16 v[64:67], a[152:155], v[224:227], v[64:67]
ds_read_b64_tr_b16 a[136:137], v9 offset:64
ds_read_b64_tr_b16 a[138:139], v9 offset:2176
v_mfma_f32_16x16x32_bf16 v[68:71], a[152:155], v[228:231], v[68:71]
ds_read_b64_tr_b16 a[140:141], v10 offset:64
v_mfma_f32_16x16x32_bf16 v[72:75], a[156:159], v[224:227], v[72:75]
ds_read_b64_tr_b16 a[142:143], v10 offset:2176
s_add_u32 s48, s48, s50
v_mfma_f32_16x16x32_bf16 v[76:79], a[156:159], v[228:231], v[76:79]
ds_read_b64_tr_b16 a[144:145], v9 offset:128
s_waitcnt lgkmcnt(14)
v_mfma_f32_16x16x32_bf16 v[80:83], a[160:163], v[224:227], v[80:83]
ds_read_b64_tr_b16 a[146:147], v9 offset:2240
ds_read_b64_tr_b16 a[148:149], v10 offset:128
v_mfma_f32_16x16x32_bf16 v[84:87], a[160:163], v[228:231], v[84:87]
ds_read_b64_tr_b16 a[150:151], v10 offset:2240
v_mfma_f32_16x16x32_bf16 v[88:91], a[164:167], v[224:227], v[88:91]
buffer_store_dwordx4 a[72:75], v1, s[60:63], s48 offen offset:0 sc1
v_mfma_f32_16x16x32_bf16 v[92:95], a[164:167], v[228:231], v[92:95]
ds_read_b64_tr_b16 a[152:153], v9 offset:192
ds_read_b64_tr_b16 a[154:155], v9 offset:2304
v_mfma_f32_16x16x32_bf16 v[96:99], a[168:171], v[224:227], v[96:99]
ds_read_b64_tr_b16 a[156:157], v10 offset:192
v_mfma_f32_16x16x32_bf16 v[100:103], a[168:171], v[228:231], v[100:103]
ds_read_b64_tr_b16 a[158:159], v10 offset:2304
ds_read_b64_tr_b16 a[160:161], v9 offset:256
v_mfma_f32_16x16x32_bf16 v[104:107], a[172:175], v[224:227], v[104:107]
ds_read_b64_tr_b16 a[162:163], v9 offset:2368
s_add_u32 s48, s48, s50
v_mfma_f32_16x16x32_bf16 v[108:111], a[172:175], v[228:231], v[108:111]
ds_read_b64_tr_b16 a[164:165], v10 offset:256
s_waitcnt lgkmcnt(14)
v_mfma_f32_16x16x32_bf16 v[112:115], a[176:179], v[224:227], v[112:115]
ds_read_b64_tr_b16 a[166:167], v10 offset:2368
ds_read_b64_tr_b16 a[168:169], v9 offset:320
v_mfma_f32_16x16x32_bf16 v[116:119], a[176:179], v[228:231], v[116:119]
buffer_store_dwordx4 a[76:79], v1, s[60:63], s48 offen offset:0 sc1
v_mfma_f32_16x16x32_bf16 v[120:123], a[180:183], v[224:227], v[120:123]
ds_read_b64_tr_b16 a[170:171], v9 offset:2432
ds_read_b64_tr_b16 a[172:173], v10 offset:320
v_mfma_f32_16x16x32_bf16 v[124:127], a[180:183], v[228:231], v[124:127]
ds_read_b64_tr_b16 a[174:175], v10 offset:2432
v_mfma_f32_16x16x32_bf16 v[128:131], a[184:187], v[224:227], v[128:131]
v_mfma_f32_16x16x32_bf16 v[132:135], a[184:187], v[228:231], v[132:135]
v_mfma_f32_16x16x32_bf16 v[136:139], a[188:191], v[224:227], v[136:139]
v_mfma_f32_16x16x32_bf16 v[140:143], a[188:191], v[228:231], v[140:143]
s_waitcnt lgkmcnt(14)
v_mfma_f32_16x16x32_bf16 v[16:19], a[128:131], v[232:235], v[16:19]
ds_read_b64_tr_b16 a[176:177], v9 offset:384
ds_read_b64_tr_b16 a[178:179], v9 offset:2496
v_mfma_f32_16x16x32_bf16 v[20:23], a[128:131], v[236:239], v[20:23]
ds_read_b64_tr_b16 a[180:181], v10 offset:384
s_add_u32 s48, s48, s50
v_mfma_f32_16x16x32_bf16 v[24:27], a[132:135], v[232:235], v[24:27]
ds_read_b64_tr_b16 a[182:183], v10 offset:2496
ds_read_b64_tr_b16 a[184:185], v9 offset:448
v_mfma_f32_16x16x32_bf16 v[28:31], a[132:135], v[236:239], v[28:31]
buffer_store_dwordx4 a[80:83], v1, s[60:63], s48 offen offset:0 sc1
v_mfma_f32_16x16x32_bf16 v[32:35], a[136:139], v[232:235], v[32:35]
ds_read_b64_tr_b16 a[186:187], v9 offset:2560
ds_read_b64_tr_b16 a[188:189], v10 offset:448
v_mfma_f32_16x16x32_bf16 v[36:39], a[136:139], v[236:239], v[36:39]
ds_read_b64_tr_b16 a[190:191], v10 offset:2560
s_lshl_b32 s53, s81, 14
v_mfma_f32_16x16x32_bf16 v[40:43], a[140:143], v[232:235], v[40:43]
s_mul_i32 s54, s82, 55296
s_add_u32 s54, s54, s53
v_mfma_f32_16x16x32_bf16 v[44:47], a[140:143], v[236:239], v[44:47]
s_add_u32 s48, s48, s50
s_waitcnt lgkmcnt(14)
v_mfma_f32_16x16x32_bf16 v[48:51], a[144:147], v[232:235], v[48:51]
s_waitcnt vmcnt(9)
s_barrier
v_mfma_f32_16x16x32_bf16 v[52:55], a[144:147], v[236:239], v[52:55]
ds_read_b64_tr_b16 a[128:129], v9 offset:50688
s_add_u32 m0, s45, 0
v_mfma_f32_16x16x32_bf16 v[56:59], a[148:151], v[232:235], v[56:59]
ds_read_b64_tr_b16 a[130:131], v9 offset:52800
ds_read_b64_tr_b16 a[132:133], v10 offset:50688
v_mfma_f32_16x16x32_bf16 v[60:63], a[148:151], v[236:239], v[60:63]
buffer_load_dwordx4 v4, s[36:39], s44 offen offset:0 lds
v_mfma_f32_16x16x32_bf16 v[64:67], a[152:155], v[232:235], v[64:67]
buffer_store_dwordx4 a[84:87], v1, s[60:63], s48 offen offset:0 sc1
v_mfma_f32_16x16x32_bf16 v[68:71], a[152:155], v[236:239], v[68:71]
ds_read_b64_tr_b16 a[134:135], v10 offset:52800
ds_read_b64_tr_b16 a[136:137], v9 offset:50752
v_mfma_f32_16x16x32_bf16 v[72:75], a[156:159], v[232:235], v[72:75]
ds_read_b64_tr_b16 a[138:139], v9 offset:52864
ds_read_b64_tr_b16 a[140:141], v10 offset:50752
v_mfma_f32_16x16x32_bf16 v[76:79], a[156:159], v[236:239], v[76:79]
s_add_u32 s48, s48, s50
s_waitcnt lgkmcnt(14)
v_mfma_f32_16x16x32_bf16 v[80:83], a[160:163], v[232:235], v[80:83]
ds_read_b64_tr_b16 a[142:143], v10 offset:52864
ds_read_b64_tr_b16 a[144:145], v9 offset:50816
v_mfma_f32_16x16x32_bf16 v[84:87], a[160:163], v[236:239], v[84:87]
buffer_load_dwordx4 v5, s[36:39], s44 offen offset:1088 lds
v_mfma_f32_16x16x32_bf16 v[88:91], a[164:167], v[232:235], v[88:91]
ds_read_b64_tr_b16 a[146:147], v9 offset:52928
ds_read_b64_tr_b16 a[148:149], v10 offset:50816
v_mfma_f32_16x16x32_bf16 v[92:95], a[164:167], v[236:239], v[92:95]
buffer_store_dwordx4 a[88:91], v1, s[60:63], s48 offen offset:0 sc1
v_mfma_f32_16x16x32_bf16 v[96:99], a[168:171], v[232:235], v[96:99]
ds_read_b64_tr_b16 a[150:151], v10 offset:52928
ds_read_b64_tr_b16 a[152:153], v9 offset:50880
v_mfma_f32_16x16x32_bf16 v[100:103], a[168:171], v[236:239], v[100:103]
ds_read_b64_tr_b16 a[154:155], v9 offset:52992
ds_read_b64_tr_b16 a[156:157], v10 offset:50880
v_mfma_f32_16x16x32_bf16 v[104:107], a[172:175], v[232:235], v[104:107]
buffer_load_dwordx4 v6, s[36:39], s44 offen offset:2176 lds
v_mfma_f32_16x16x32_bf16 v[108:111], a[172:175], v[236:239], v[108:111]
s_add_u32 s54, s54, 34816
s_waitcnt lgkmcnt(14)
v_mfma_f32_16x16x32_bf16 v[112:115], a[176:179], v[232:235], v[112:115]
ds_read_b64_tr_b16 a[158:159], v10 offset:52992
ds_read_b64_tr_b16 a[160:161], v9 offset:50944
v_mfma_f32_16x16x32_bf16 v[116:119], a[176:179], v[236:239], v[116:119]
ds_read_b64_tr_b16 a[162:163], v9 offset:53056
ds_read_b64_tr_b16 a[164:165], v10 offset:50944
v_mfma_f32_16x16x32_bf16 v[120:123], a[180:183], v[232:235], v[120:123]
ds_read_b64_tr_b16 a[166:167], v10 offset:53056
v_add_u32 v13, s54, v1
v_mfma_f32_16x16x32_bf16 v[124:127], a[180:183], v[236:239], v[124:127]
ds_read_b64_tr_b16 a[168:169], v9 offset:51008
ds_read_b64_tr_b16 a[170:171], v9 offset:53120
v_mfma_f32_16x16x32_bf16 v[128:131], a[184:187], v[232:235], v[128:131]
buffer_load_dwordx4 v7, s[36:39], s44 offen offset:3264 lds
v_mfma_f32_16x16x32_bf16 v[132:135], a[184:187], v[236:239], v[132:135]
ds_read_b64_tr_b16 a[172:173], v10 offset:51008
ds_read_b64_tr_b16 a[174:175], v10 offset:53120
v_mfma_f32_16x16x32_bf16 v[136:139], a[188:191], v[232:235], v[136:139]
v_mfma_f32_16x16x32_bf16 v[140:143], a[188:191], v[236:239], v[140:143]
s_waitcnt lgkmcnt(14)
v_mfma_f32_16x16x32_bf16 v[16:19], a[128:131], v[240:243], v[16:19]
ds_read_b64_tr_b16 a[176:177], v9 offset:51072
ds_read_b64_tr_b16 a[178:179], v9 offset:53184
v_mfma_f32_16x16x32_bf16 v[20:23], a[128:131], v[244:247], v[20:23]
ds_read_b64_tr_b16 a[180:181], v10 offset:51072
ds_read_b64_tr_b16 a[182:183], v10 offset:53184
v_mfma_f32_16x16x32_bf16 v[24:27], a[132:135], v[240:243], v[24:27]
ds_read_b64_tr_b16 a[184:185], v9 offset:51136
s_xor_b32 s55, s82, 1
v_mfma_f32_16x16x32_bf16 v[28:31], a[132:135], v[244:247], v[28:31]
ds_read_b64_tr_b16 a[186:187], v9 offset:53248
ds_read_b64_tr_b16 a[188:189], v10 offset:51136
v_mfma_f32_16x16x32_bf16 v[32:35], a[136:139], v[240:243], v[32:35]
ds_read_b64_tr_b16 a[190:191], v10 offset:53248
s_mul_i32 s55, s55, 55296
v_mfma_f32_16x16x32_bf16 v[36:39], a[136:139], v[244:247], v[36:39]
s_add_u32 s55, s55, s53
s_add_u32 s55, s55, 34816
v_mfma_f32_16x16x32_bf16 v[40:43], a[140:143], v[240:243], v[40:43]
v_add_u32 v14, s55, v1
v_mfma_f32_16x16x32_bf16 v[44:47], a[140:143], v[244:247], v[44:47]
s_waitcnt lgkmcnt(14)
v_mfma_f32_16x16x32_bf16 v[48:51], a[144:147], v[240:243], v[48:51]
s_waitcnt vmcnt(11)
s_barrier
v_mfma_f32_16x16x32_bf16 v[52:55], a[144:147], v[244:247], v[52:55]
ds_read_b64_tr_b16 a[128:129], v9 offset:33792
s_add_u32 m0, s45, 4352
v_mfma_f32_16x16x32_bf16 v[56:59], a[148:151], v[240:243], v[56:59]
ds_read_b64_tr_b16 a[130:131], v9 offset:35904
ds_read_b64_tr_b16 a[132:133], v10 offset:33792
v_mfma_f32_16x16x32_bf16 v[60:63], a[148:151], v[244:247], v[60:63]
buffer_load_dwordx4 v4, s[36:39], s49 offen offset:0 lds
v_mfma_f32_16x16x32_bf16 v[64:67], a[152:155], v[240:243], v[64:67]
ds_read_b64_tr_b16 a[134:135], v10 offset:35904
ds_read_b64_tr_b16 a[136:137], v9 offset:33856
v_mfma_f32_16x16x32_bf16 v[68:71], a[152:155], v[244:247], v[68:71]
ds_read_b64_tr_b16 a[138:139], v9 offset:35968
ds_read_b64_tr_b16 a[140:141], v10 offset:33856
v_mfma_f32_16x16x32_bf16 v[72:75], a[156:159], v[240:243], v[72:75]
ds_read_b64_tr_b16 a[142:143], v10 offset:35968
v_mfma_f32_16x16x32_bf16 v[76:79], a[156:159], v[244:247], v[76:79]
ds_read_b64_tr_b16 a[144:145], v9 offset:33920
s_waitcnt lgkmcnt(14)
v_mfma_f32_16x16x32_bf16 v[80:83], a[160:163], v[240:243], v[80:83]
ds_read_b64_tr_b16 a[146:147], v9 offset:36032
ds_read_b64_tr_b16 a[148:149], v10 offset:33920
v_mfma_f32_16x16x32_bf16 v[84:87], a[160:163], v[244:247], v[84:87]
buffer_load_dwordx4 v5, s[36:39], s49 offen offset:1088 lds
v_mfma_f32_16x16x32_bf16 v[88:91], a[164:167], v[240:243], v[88:91]
ds_read_b64_tr_b16 a[150:151], v10 offset:36032
ds_read_b64_tr_b16 a[152:153], v9 offset:33984
v_mfma_f32_16x16x32_bf16 v[92:95], a[164:167], v[244:247], v[92:95]
ds_read_b64_tr_b16 a[154:155], v9 offset:36096
v_mfma_f32_16x16x32_bf16 v[96:99], a[168:171], v[240:243], v[96:99]
ds_read_b64_tr_b16 a[156:157], v10 offset:33984
ds_read_b64_tr_b16 a[158:159], v10 offset:36096
v_mfma_f32_16x16x32_bf16 v[100:103], a[168:171], v[244:247], v[100:103]
ds_read_b64_tr_b16 a[160:161], v9 offset:34048
v_mfma_f32_16x16x32_bf16 v[104:107], a[172:175], v[240:243], v[104:107]
buffer_load_dwordx4 v6, s[36:39], s49 offen offset:2176 lds
v_mfma_f32_16x16x32_bf16 v[108:111], a[172:175], v[244:247], v[108:111]
ds_read_b64_tr_b16 a[162:163], v9 offset:36160
s_waitcnt lgkmcnt(14)
v_mfma_f32_16x16x32_bf16 v[112:115], a[176:179], v[240:243], v[112:115]
ds_read_b64_tr_b16 a[164:165], v10 offset:34048
ds_read_b64_tr_b16 a[166:167], v10 offset:36160
v_mfma_f32_16x16x32_bf16 v[116:119], a[176:179], v[244:247], v[116:119]
ds_read_b64_tr_b16 a[168:169], v9 offset:34112
v_mfma_f32_16x16x32_bf16 v[120:123], a[180:183], v[240:243], v[120:123]
ds_read_b64_tr_b16 a[170:171], v9 offset:36224
ds_read_b64_tr_b16 a[172:173], v10 offset:34112
v_mfma_f32_16x16x32_bf16 v[124:127], a[180:183], v[244:247], v[124:127]
ds_read_b64_tr_b16 a[174:175], v10 offset:36224
v_mfma_f32_16x16x32_bf16 v[128:131], a[184:187], v[240:243], v[128:131]
buffer_load_dwordx4 v7, s[36:39], s49 offen offset:3264 lds
v_mfma_f32_16x16x32_bf16 v[132:135], a[184:187], v[244:247], v[132:135]
v_mfma_f32_16x16x32_bf16 v[136:139], a[188:191], v[240:243], v[136:139]
v_mfma_f32_16x16x32_bf16 v[140:143], a[188:191], v[244:247], v[140:143]
s_waitcnt lgkmcnt(14)
v_mfma_f32_16x16x32_bf16 v[16:19], a[128:131], v[248:251], v[16:19]
ds_read_b64_tr_b16 a[176:177], v9 offset:34176
ds_read_b64_tr_b16 a[178:179], v9 offset:36288
v_mfma_f32_16x16x32_bf16 v[20:23], a[128:131], v[252:255], v[20:23]
ds_read_b64_tr_b16 a[180:181], v10 offset:34176
ds_read_b64_tr_b16 a[182:183], v10 offset:36288
v_mfma_f32_16x16x32_bf16 v[24:27], a[132:135], v[248:251], v[24:27]
ds_read_b64_tr_b16 a[184:185], v9 offset:34240
v_mfma_f32_16x16x32_bf16 v[28:31], a[132:135], v[252:255], v[28:31]
ds_read_b64_tr_b16 a[186:187], v9 offset:36352
ds_read_b64_tr_b16 a[188:189], v10 offset:34240
v_mfma_f32_16x16x32_bf16 v[32:35], a[136:139], v[248:251], v[32:35]
ds_read_b64_tr_b16 a[190:191], v10 offset:36352
v_mfma_f32_16x16x32_bf16 v[36:39], a[136:139], v[252:255], v[36:39]
v_mfma_f32_16x16x32_bf16 v[40:43], a[140:143], v[248:251], v[40:43]
v_cvt_pk_bf16_f32 v192, v16, v17
v_cvt_pk_bf16_f32 v193, v18, v19
v_mfma_f32_16x16x32_bf16 v[44:47], a[140:143], v[252:255], v[44:47]
v_cvt_pk_bf16_f32 v194, v24, v25
v_cvt_pk_bf16_f32 v195, v26, v27
s_waitcnt lgkmcnt(14)
v_mfma_f32_16x16x32_bf16 v[48:51], a[144:147], v[248:251], v[48:51]
v_permlane32_swap_b32 v192, v194
v_permlane32_swap_b32 v193, v195
v_mfma_f32_16x16x32_bf16 v[52:55], a[144:147], v[252:255], v[52:55]
v_permlane16_swap_b32 v192, v194
v_permlane16_swap_b32 v193, v195
v_cvt_pk_bf16_f32 v224, v20, v21
v_cvt_pk_bf16_f32 v225, v22, v23
v_cvt_pk_bf16_f32 v226, v28, v29
v_cvt_pk_bf16_f32 v227, v30, v31
v_mfma_f32_16x16x32_bf16 v[56:59], a[148:151], v[248:251], v[56:59]
v_permlane32_swap_b32 v224, v226
v_permlane32_swap_b32 v225, v227
v_mfma_f32_16x16x32_bf16 v[60:63], a[148:151], v[252:255], v[60:63]
v_permlane16_swap_b32 v224, v226
v_permlane16_swap_b32 v225, v227
v_cvt_pk_bf16_f32 v196, v32, v33
v_cvt_pk_bf16_f32 v197, v34, v35
v_cvt_pk_bf16_f32 v198, v40, v41
v_cvt_pk_bf16_f32 v199, v42, v43
v_mfma_f32_16x16x32_bf16 v[64:67], a[152:155], v[248:251], v[64:67]
ds_write_b128 v13, v[192:195] offset:0
ds_write_b128 v13, v[224:227] offset:8192
v_permlane32_swap_b32 v196, v198
v_permlane32_swap_b32 v197, v199
v_mfma_f32_16x16x32_bf16 v[68:71], a[152:155], v[252:255], v[68:71]
v_permlane16_swap_b32 v196, v198
v_permlane16_swap_b32 v197, v199
v_cvt_pk_bf16_f32 v228, v36, v37
v_cvt_pk_bf16_f32 v229, v38, v39
v_cvt_pk_bf16_f32 v230, v44, v45
v_cvt_pk_bf16_f32 v231, v46, v47
v_mfma_f32_16x16x32_bf16 v[72:75], a[156:159], v[248:251], v[72:75]
v_permlane32_swap_b32 v228, v230
v_permlane32_swap_b32 v229, v231
v_mfma_f32_16x16x32_bf16 v[76:79], a[156:159], v[252:255], v[76:79]
v_permlane16_swap_b32 v228, v230
v_permlane16_swap_b32 v229, v231
v_cvt_pk_bf16_f32 v200, v48, v49
v_cvt_pk_bf16_f32 v201, v50, v51
v_cvt_pk_bf16_f32 v202, v56, v57
v_cvt_pk_bf16_f32 v203, v58, v59
s_waitcnt lgkmcnt(10)
v_mfma_f32_16x16x32_bf16 v[80:83], a[160:163], v[248:251], v[80:83]
ds_write_b128 v13, v[196:199] offset:1024
ds_write_b128 v13, v[228:231] offset:9216
v_permlane32_swap_b32 v200, v202
v_permlane32_swap_b32 v201, v203
v_mfma_f32_16x16x32_bf16 v[84:87], a[160:163], v[252:255], v[84:87]
v_permlane16_swap_b32 v200, v202
v_permlane16_swap_b32 v201, v203
v_cvt_pk_bf16_f32 v232, v52, v53
v_cvt_pk_bf16_f32 v233, v54, v55
v_cvt_pk_bf16_f32 v234, v60, v61
v_cvt_pk_bf16_f32 v235, v62, v63
v_mfma_f32_16x16x32_bf16 v[88:91], a[164:167], v[248:251], v[88:91]
v_permlane32_swap_b32 v232, v234
v_permlane32_swap_b32 v233, v235
v_mfma_f32_16x16x32_bf16 v[92:95], a[164:167], v[252:255], v[92:95]
v_permlane16_swap_b32 v232, v234
v_permlane16_swap_b32 v233, v235
v_cvt_pk_bf16_f32 v204, v64, v65
v_cvt_pk_bf16_f32 v205, v66, v67
v_cvt_pk_bf16_f32 v206, v72, v73
v_cvt_pk_bf16_f32 v207, v74, v75
v_mfma_f32_16x16x32_bf16 v[96:99], a[168:171], v[248:251], v[96:99]
ds_write_b128 v13, v[200:203] offset:2048
ds_write_b128 v13, v[232:235] offset:10240
v_permlane32_swap_b32 v204, v206
v_permlane32_swap_b32 v205, v207
v_mfma_f32_16x16x32_bf16 v[100:103], a[168:171], v[252:255], v[100:103]
v_permlane16_swap_b32 v204, v206
v_permlane16_swap_b32 v205, v207
v_cvt_pk_bf16_f32 v236, v68, v69
v_cvt_pk_bf16_f32 v237, v70, v71
v_cvt_pk_bf16_f32 v238, v76, v77
v_cvt_pk_bf16_f32 v239, v78, v79
v_mfma_f32_16x16x32_bf16 v[104:107], a[172:175], v[248:251], v[104:107]
v_permlane32_swap_b32 v236, v238
v_permlane32_swap_b32 v237, v239
v_mfma_f32_16x16x32_bf16 v[108:111], a[172:175], v[252:255], v[108:111]
v_permlane16_swap_b32 v236, v238
v_permlane16_swap_b32 v237, v239
v_cvt_pk_bf16_f32 v208, v80, v81
v_cvt_pk_bf16_f32 v209, v82, v83
v_cvt_pk_bf16_f32 v210, v88, v89
v_cvt_pk_bf16_f32 v211, v90, v91
s_waitcnt lgkmcnt(6)
v_mfma_f32_16x16x32_bf16 v[112:115], a[176:179], v[248:251], v[112:115]
ds_write_b128 v13, v[204:207] offset:3072
ds_write_b128 v13, v[236:239] offset:11264
v_permlane32_swap_b32 v208, v210
v_permlane32_swap_b32 v209, v211
v_mfma_f32_16x16x32_bf16 v[116:119], a[176:179], v[252:255], v[116:119]
v_permlane16_swap_b32 v208, v210
v_permlane16_swap_b32 v209, v211
v_cvt_pk_bf16_f32 v240, v84, v85
v_cvt_pk_bf16_f32 v241, v86, v87
v_cvt_pk_bf16_f32 v242, v92, v93
v_cvt_pk_bf16_f32 v243, v94, v95
v_mfma_f32_16x16x32_bf16 v[120:123], a[180:183], v[248:251], v[120:123]
v_permlane32_swap_b32 v240, v242
v_permlane32_swap_b32 v241, v243
v_mfma_f32_16x16x32_bf16 v[124:127], a[180:183], v[252:255], v[124:127]
v_permlane16_swap_b32 v240, v242
v_permlane16_swap_b32 v241, v243
v_mfma_f32_16x16x32_bf16 v[128:131], a[184:187], v[248:251], v[128:131]
ds_write_b128 v13, v[208:211] offset:4096
ds_write_b128 v13, v[240:243] offset:12288
v_mfma_f32_16x16x32_bf16 v[132:135], a[184:187], v[252:255], v[132:135]
v_mfma_f32_16x16x32_bf16 v[136:139], a[188:191], v[248:251], v[136:139]
v_mfma_f32_16x16x32_bf16 v[140:143], a[188:191], v[252:255], v[140:143]
s_add_u32 s44, s44, 32768
s_add_u32 s49, s49, 32768
s_mov_b32 s48, 0x41000000
s_nop 1
v_cvt_pk_bf16_f32 v212, v96, v97
v_cvt_pk_bf16_f32 v213, v98, v99
v_cvt_pk_bf16_f32 v214, v104, v105
v_cvt_pk_bf16_f32 v215, v106, v107
v_cvt_pk_bf16_f32 v244, v100, v101
v_cvt_pk_bf16_f32 v245, v102, v103
v_cvt_pk_bf16_f32 v246, v108, v109
v_cvt_pk_bf16_f32 v247, v110, v111
v_cvt_pk_bf16_f32 v216, v112, v113
v_cvt_pk_bf16_f32 v217, v114, v115
v_cvt_pk_bf16_f32 v218, v120, v121
v_cvt_pk_bf16_f32 v219, v122, v123
v_cvt_pk_bf16_f32 v248, v116, v117
v_cvt_pk_bf16_f32 v249, v118, v119
v_cvt_pk_bf16_f32 v250, v124, v125
v_cvt_pk_bf16_f32 v251, v126, v127
v_cvt_pk_bf16_f32 v220, v128, v129
v_cvt_pk_bf16_f32 v221, v130, v131
v_cvt_pk_bf16_f32 v222, v136, v137
v_cvt_pk_bf16_f32 v223, v138, v139
v_cvt_pk_bf16_f32 v252, v132, v133
v_cvt_pk_bf16_f32 v253, v134, v135
v_cvt_pk_bf16_f32 v254, v140, v141
v_cvt_pk_bf16_f32 v255, v142, v143
v_permlane32_swap_b32 v212, v214
v_permlane32_swap_b32 v244, v246
v_permlane32_swap_b32 v216, v218
v_permlane32_swap_b32 v248, v250
v_permlane32_swap_b32 v220, v222
v_permlane32_swap_b32 v252, v254
v_permlane32_swap_b32 v213, v215
v_permlane32_swap_b32 v245, v247
v_permlane32_swap_b32 v217, v219
v_permlane32_swap_b32 v249, v251
v_permlane32_swap_b32 v221, v223
v_permlane32_swap_b32 v253, v255
v_permlane16_swap_b32 v212, v214
v_permlane16_swap_b32 v244, v246
v_permlane16_swap_b32 v216, v218
v_permlane16_swap_b32 v248, v250
v_permlane16_swap_b32 v220, v222
v_permlane16_swap_b32 v252, v254
v_permlane16_swap_b32 v213, v215
v_permlane16_swap_b32 v245, v247
v_permlane16_swap_b32 v217, v219
v_permlane16_swap_b32 v249, v251
v_permlane16_swap_b32 v221, v223
v_permlane16_swap_b32 v253, v255
ds_write_b128 v13, v[212:215] offset:5120
ds_write_b128 v13, v[244:247] offset:13312
ds_write_b128 v13, v[216:219] offset:6144
ds_write_b128 v13, v[248:251] offset:14336
ds_write_b128 v13, v[220:223] offset:7168
ds_write_b128 v13, v[252:255] offset:15360
v_mov_b32 v16, s76
v_mov_b32 v17, s77
v_min_u32 v18, 1, v15
v_lshlrev_b32 v18, 31, v18
s_waitcnt vmcnt(0) lgkmcnt(0)
s_barrier
ds_read_b128 v[128:131], v14 offset:0
ds_read_b128 v[132:135], v14 offset:1024
ds_read_b128 v[136:139], v14 offset:2048
ds_read_b128 v[140:143], v14 offset:3072
ds_read_b128 v[144:147], v14 offset:4096
ds_read_b128 v[148:151], v14 offset:5120
ds_read_b128 v[152:155], v14 offset:6144
ds_read_b128 v[156:159], v14 offset:7168
ds_read_b128 v[160:163], v14 offset:8192
ds_read_b128 v[164:167], v14 offset:9216
ds_read_b128 v[168:171], v14 offset:10240
ds_read_b128 v[172:175], v14 offset:11264
ds_read_b128 v[176:179], v14 offset:12288
ds_read_b128 v[180:183], v14 offset:13312
ds_read_b128 v[184:187], v14 offset:14336
ds_read_b128 v[188:191], v14 offset:15360
ds_read_b128 v[96:99], v12 offset:0
ds_read_b128 v[100:103], v12 offset:64
ds_read_b128 v[104:107], v12 offset:128
ds_read_b128 v[108:111], v12 offset:192
s_cmp_eq_u32 s80, 0
s_cbranch_scc0 L_noal%=
s_lshl_b32 s54, s99, 3
s_add_u32 s54, s54, 131072
buffer_store_dwordx2 v[16:17], v18, s[60:63], s54 offen offset:0 sc1
L_noal%=:
v_lshlrev_b32 v0, 7, v15
s_waitcnt lgkmcnt(4)
s_barrier
s_waitcnt lgkmcnt(2)
v_mfma_f32_16x16x32_bf16 v[32:35], v[96:99], v[192:195], 0
ds_read_b128 v[112:115], v12 offset:256
s_add_u32 m0, s45, 34816
v_mfma_f32_16x16x32_bf16 v[32:35], v[100:103], v[196:199], v[32:35]
buffer_load_dwordx4 v4, s[36:39], s44 offen offset:0 lds
v_mfma_f32_16x16x32_bf16 v[36:39], v[96:99], v[224:227], 0
ds_read_b128 v[116:119], v12 offset:320
ds_read_b128 v[120:123], v12 offset:384
v_mfma_f32_16x16x32_bf16 v[36:39], v[100:103], v[228:231], v[36:39]
buffer_load_dwordx4 v5, s[36:39], s44 offen offset:1088 lds
v_mfma_f32_16x16x32_bf16 v[40:43], v[96:99], v[128:131], 0
ds_read_b128 v[124:127], v12 offset:448
v_mfma_f32_16x16x32_bf16 v[40:43], v[100:103], v[132:135], v[40:43]
buffer_load_dwordx4 v6, s[36:39], s44 offen offset:2176 lds
v_mfma_f32_16x16x32_bf16 v[44:47], v[96:99], v[160:163], 0
v_mfma_f32_16x16x32_bf16 v[44:47], v[100:103], v[164:167], v[44:47]
buffer_load_dwordx4 v7, s[36:39], s44 offen offset:3264 lds
s_waitcnt lgkmcnt(4)
v_mfma_f32_16x16x32_bf16 v[32:35], v[104:107], v[200:203], v[32:35]
s_add_u32 m0, s45, 39168
s_add_u32 s44, s44, 32768
v_mfma_f32_16x16x32_bf16 v[32:35], v[108:111], v[204:207], v[32:35]
buffer_load_dwordx4 v4, s[36:39], s49 offen offset:0 lds
v_mfma_f32_16x16x32_bf16 v[36:39], v[104:107], v[232:235], v[36:39]
ds_read_b128 v[96:99], v12 offset:8704
ds_read_b128 v[100:103], v12 offset:8768
v_mfma_f32_16x16x32_bf16 v[36:39], v[108:111], v[236:239], v[36:39]
buffer_load_dwordx4 v5, s[36:39], s49 offen offset:1088 lds
v_mfma_f32_16x16x32_bf16 v[40:43], v[104:107], v[136:139], v[40:43]
v_mfma_f32_16x16x32_bf16 v[40:43], v[108:111], v[140:143], v[40:43]
buffer_load_dwordx4 v6, s[36:39], s49 offen offset:2176 lds
v_mfma_f32_16x16x32_bf16 v[44:47], v[104:107], v[168:171], v[44:47]
v_mfma_f32_16x16x32_bf16 v[44:47], v[108:111], v[172:175], v[44:47]
buffer_load_dwordx4 v7, s[36:39], s49 offen offset:3264 lds
s_waitcnt lgkmcnt(4)
v_mfma_f32_16x16x32_bf16 v[32:35], v[112:115], v[208:211], v[32:35]
s_add_u32 s49, s49, 32768
s_lshl_b32 s53, s81, 1
v_mfma_f32_16x16x32_bf16 v[32:35], v[116:119], v[212:215], v[32:35]
s_add_u32 m0, s46, 0
s_add_u32 s53, s53, s82
v_mfma_f32_16x16x32_bf16 v[36:39], v[112:115], v[240:243], v[36:39]
buffer_load_dwordx4 v4, s[40:43], s44 offen offset:0 lds
v_mfma_f32_16x16x32_bf16 v[36:39], v[116:119], v[244:247], v[36:39]
s_lshl_b32 s54, s33, 2
s_add_u32 s53, s53, s54
v_mfma_f32_16x16x32_bf16 v[40:43], v[112:115], v[144:147], v[40:43]
s_lshl_b32 s54, s53, 8
s_lshl_b32 s55, s32, 3
v_mfma_f32_16x16x32_bf16 v[40:43], v[116:119], v[148:151], v[40:43]
buffer_load_dwordx4 v5, s[40:43], s44 offen offset:1088 lds
v_mfma_f32_16x16x32_bf16 v[44:47], v[112:115], v[176:179], v[44:47]
s_add_u32 s54, s54, s55
s_add_u32 s57, s54, 196608
v_mfma_f32_16x16x32_bf16 v[44:47], v[116:119], v[180:183], v[44:47]
s_lshl_b32 s53, s53, 1
s_waitcnt lgkmcnt(2)
v_mfma_f32_16x16x32_bf16 v[32:35], v[120:123], v[216:219], v[32:35]
s_lshl_b32 s54, s32, 7
s_add_u32 s53, s53, s54
v_mfma_f32_16x16x32_bf16 v[32:35], v[124:127], v[220:223], v[32:35]
buffer_load_dwordx4 v6, s[40:43], s44 offen offset:2176 lds
s_mul_i32 s53, s53, 14336
v_mfma_f32_16x16x32_bf16 v[36:39], v[120:123], v[248:251], v[36:39]
s_add_u32 s56, s53, 33554432
s_mul_i32 s53, s32, 229376
v_mfma_f32_16x16x32_bf16 v[36:39], v[124:127], v[252:255], v[36:39]
s_add_u32 s3, s53, 16777216
s_lshl_b32 s10, s80, 12
v_mfma_f32_16x16x32_bf16 v[40:43], v[120:123], v[152:155], v[40:43]
buffer_load_dwordx4 v7, s[40:43], s44 offen offset:3264 lds
s_lshl_b32 s11, s80, 11
v_mfma_f32_16x16x32_bf16 v[40:43], v[124:127], v[156:159], v[40:43]
s_add_u32 s16, s3, s11
s_add_u32 s3, s3, s10
v_mfma_f32_16x16x32_bf16 v[44:47], v[120:123], v[184:187], v[44:47]
ds_read_b128 v[104:107], v12 offset:8832
ds_read_b128 v[108:111], v12 offset:8896
s_lshr_b32 s54, s35, 4
s_lshl_b32 s8, s34, 1
v_mfma_f32_16x16x32_bf16 v[44:47], v[124:127], v[188:191], v[44:47]
s_add_u32 s8, s8, s54
s_and_b32 s9, s35, 15
s_waitcnt lgkmcnt(2)
v_mfma_f32_16x16x32_bf16 v[48:51], v[96:99], v[192:195], 0
s_add_u32 m0, s46, 4352
s_lshl_b32 s54, s8, 3
s_lshr_b32 s55, s9, 1
v_mfma_f32_16x16x32_bf16 v[48:51], v[100:103], v[196:199], v[48:51]
buffer_load_dwordx4 v4, s[40:43], s49 offen offset:0 lds
s_add_u32 s54, s54, s55
s_mul_i32 s59, s54, 14336
v_mfma_f32_16x16x32_bf16 v[52:55], v[96:99], v[224:227], 0
s_lshr_b32 s54, s54, 1
s_lshl_b32 s54, s54, 8
v_mfma_f32_16x16x32_bf16 v[52:55], v[100:103], v[228:231], v[52:55]
s_add_u32 s58, s54, 196608
s_and_b32 s52, s9, 1
v_mfma_f32_16x16x32_bf16 v[56:59], v[96:99], v[128:131], 0
buffer_load_dwordx4 v5, s[40:43], s49 offen offset:1088 lds
s_mul_i32 s54, s52, 7168
v_mfma_f32_16x16x32_bf16 v[56:59], v[100:103], v[132:135], v[56:59]
s_add_u32 s78, s59, s54
s_lshl_b32 s52, s8, 9
v_mfma_f32_16x16x32_bf16 v[60:63], v[96:99], v[160:163], 0
ds_read_b128 v[112:115], v12 offset:8960
ds_read_b128 v[116:119], v12 offset:9024
s_lshl_b32 s54, s9, 5
s_lshl_b32 s55, s80, 3
v_mfma_f32_16x16x32_bf16 v[60:63], v[100:103], v[164:167], v[60:63]
ds_read_b128 v[120:123], v12 offset:9088
ds_read_b128 v[124:127], v12 offset:9152
s_add_u32 s54, s54, s55
s_add_u32 s54, s54, s52
s_waitcnt lgkmcnt(4)
v_mfma_f32_16x16x32_bf16 v[48:51], v[104:107], v[200:203], v[48:51]
s_add_u32 s79, s54, 262144
s_lshl_b32 s54, s80, 7
v_mfma_f32_16x16x32_bf16 v[48:51], v[108:111], v[204:207], v[48:51]
buffer_load_dwordx4 v6, s[40:43], s49 offen offset:2176 lds
s_add_u32 s54, s54, s52
s_add_u32 s88, s54, 262144
v_mfma_f32_16x16x32_bf16 v[52:55], v[104:107], v[232:235], v[52:55]
s_lshl_b32 s55, s9, 3
s_add_u32 s54, s54, s55
v_mfma_f32_16x16x32_bf16 v[52:55], v[108:111], v[236:239], v[52:55]
s_add_u32 s89, s54, 327680
s_lshr_b32 s55, s9, 2
v_mfma_f32_16x16x32_bf16 v[56:59], v[104:107], v[136:139], v[56:59]
buffer_load_dwordx4 v7, s[40:43], s49 offen offset:3264 lds
s_lshl_b32 s54, s55, 7
s_add_u32 s54, s54, s52
v_mfma_f32_16x16x32_bf16 v[56:59], v[108:111], v[140:143], v[56:59]
s_lshl_b32 s52, s80, 5
s_add_u32 s54, s54, s52
v_mfma_f32_16x16x32_bf16 v[60:63], v[104:107], v[168:171], v[60:63]
s_add_u32 s90, s54, 327680
s_lshl_b32 s54, s8, 2
v_mfma_f32_16x16x32_bf16 v[60:63], v[108:111], v[172:175], v[60:63]
s_waitcnt vmcnt(63) lgkmcnt(0)
s_barrier
s_add_u32 s55, s55, s54
v_mfma_f32_16x16x32_bf16 v[48:51], v[112:115], v[208:211], v[48:51]
s_and_b32 s52, s35, 3
s_mul_i32 s99, s52, 224
v_mfma_f32_16x16x32_bf16 v[48:51], v[116:119], v[212:215], v[48:51]
s_mul_i32 s53, s55, 28672
s_add_u32 s98, s53, s99
v_mfma_f32_16x16x32_bf16 v[52:55], v[112:115], v[240:243], v[52:55]
s_add_u32 s99, s99, 139648
s_lshl_b32 s52, s52, 2
v_mfma_f32_16x16x32_bf16 v[52:55], v[116:119], v[244:247], v[52:55]
s_add_u32 s52, s52, s80
s_mul_i32 s52, s52, 100352
v_mfma_f32_16x16x32_bf16 v[56:59], v[112:115], v[144:147], v[56:59]
s_add_u32 m0, s45, 0
s_add_u32 s5, s52, 22577152
s_lshl_b32 s55, s55, 2
v_mfma_f32_16x16x32_bf16 v[56:59], v[116:119], v[148:151], v[56:59]
buffer_load_dwordx4 v4, s[36:39], s44 offen offset:0 lds
s_add_u32 s55, s55, s80
v_mfma_f32_16x16x32_bf16 v[60:63], v[112:115], v[176:179], v[60:63]
s_mul_i32 s55, s55, 28672
s_add_u32 s96, s55, 8388608
v_mfma_f32_16x16x32_bf16 v[60:63], v[116:119], v[180:183], v[60:63]
s_add_u32 s54, s54, s80
s_mul_i32 s55, s54, 28672
v_mfma_f32_16x16x32_bf16 v[48:51], v[120:123], v[216:219], v[48:51]
s_add_u32 s91, s55, 1048576
s_lshl_b32 s54, s54, 4
v_mfma_f32_16x16x32_bf16 v[48:51], v[124:127], v[220:223], v[48:51]
s_add_u32 s54, s54, s9
s_mul_i32 s54, s54, 7168
v_mfma_f32_16x16x32_bf16 v[52:55], v[120:123], v[248:251], v[52:55]
s_add_u32 s83, s54, 8388608
s_mul_i32 s6, s80, 7168
v_mfma_f32_16x16x32_bf16 v[52:55], v[124:127], v[252:255], v[52:55]
s_mul_i32 s53, s9, 100352
s_add_u32 s53, s53, s6
v_mfma_f32_16x16x32_bf16 v[56:59], v[120:123], v[152:155], v[56:59]
buffer_load_dwordx4 v5, s[36:39], s44 offen offset:1088 lds
s_add_u32 s4, s53, 20971520
s_lshl_b32 s7, s6, 1
v_mfma_f32_16x16x32_bf16 v[56:59], v[124:127], v[156:159], v[56:59]
s_add_u32 s7, s7, 100352
v_mfma_f32_16x16x32_bf16 v[60:63], v[120:123], v[184:187], v[60:63]
v_mfma_f32_16x16x32_bf16 v[60:63], v[124:127], v[188:191], v[60:63]
v_max3_f32 v80, v32, v33, v34
v_max3_f32 v81, v36, v37, v38
v_max3_f32 v82, v40, v41, v42
v_max3_f32 v83, v44, v45, v46
v_max3_f32 v80, v80, v35, v48
v_max3_f32 v81, v81, v39, v52
v_max3_f32 v82, v82, v43, v56
s_nop 0
v_max3_f32 v83, v83, v47, v60
v_max3_f32 v80, v80, v49, v50
v_max3_f32 v81, v81, v53, v54
v_max3_f32 v82, v82, v57, v58
v_max3_f32 v83, v83, v61, v62
v_max_f32 v80, v80, v51
v_max_f32 v81, v81, v55
v_max_f32 v82, v82, v59
v_max_f32 v83, v83, v63
v_mov_b32 v84, v80
v_mov_b32 v85, v81
v_mov_b32 v86, v82
v_mov_b32 v87, v83
v_permlane32_swap_b32 v80, v84
v_permlane32_swap_b32 v81, v85
v_permlane32_swap_b32 v82, v86
v_permlane32_swap_b32 v83, v87
v_max_f32 v80, v80, v84
v_max_f32 v81, v81, v85
v_max_f32 v82, v82, v86
v_max_f32 v83, v83, v87
v_mov_b32 v84, v80
v_mov_b32 v85, v81
v_mov_b32 v86, v82
v_mov_b32 v87, v83
v_permlane16_swap_b32 v80, v84
v_permlane16_swap_b32 v81, v85
v_permlane16_swap_b32 v82, v86
v_permlane16_swap_b32 v83, v87
v_max_f32 v80, v80, v84
v_max_f32 v81, v81, v85
v_max_f32 v82, v82, v86
v_max_f32 v83, v83, v87
v_add_f32 v80, 2.0, v80
v_add_f32 v81, 2.0, v81
v_add_f32 v82, 2.0, v82
v_add_f32 v83, 2.0, v83
v_sub_f32 v32, v32, v80
v_sub_f32 v33, v33, v80
v_sub_f32 v34, v34, v80
v_sub_f32 v35, v35, v80
v_sub_f32 v36, v36, v81
v_sub_f32 v37, v37, v81
v_sub_f32 v38, v38, v81
v_sub_f32 v39, v39, v81
v_sub_f32 v40, v40, v82
v_sub_f32 v41, v41, v82
v_sub_f32 v42, v42, v82
v_sub_f32 v43, v43, v82
v_sub_f32 v44, v44, v83
v_sub_f32 v45, v45, v83
v_sub_f32 v46, v46, v83
v_sub_f32 v47, v47, v83
v_sub_f32 v48, v48, v80
v_sub_f32 v49, v49, v80
v_sub_f32 v50, v50, v80
v_sub_f32 v51, v51, v80
v_sub_f32 v52, v52, v81
v_sub_f32 v53, v53, v81
v_sub_f32 v54, v54, v81
v_sub_f32 v55, v55, v81
v_sub_f32 v56, v56, v82
v_sub_f32 v57, v57, v82
v_sub_f32 v58, v58, v82
v_sub_f32 v59, v59, v82
v_sub_f32 v60, v60, v83
v_sub_f32 v61, v61, v83
v_sub_f32 v62, v62, v83
v_sub_f32 v63, v63, v83
v_sub_f32 v16, 0, v80
v_sub_f32 v17, 0, v80
v_sub_f32 v18, 0, v80
v_sub_f32 v19, 0, v80
v_mov_b32 v8, 0
v_sub_f32 v20, 0, v81
v_sub_f32 v21, 0, v81
v_sub_f32 v22, 0, v81
v_sub_f32 v23, 0, v81
v_mov_b32 v9, 0
v_sub_f32 v24, 0, v82
v_sub_f32 v25, 0, v82
v_sub_f32 v26, 0, v82
v_sub_f32 v27, 0, v82
v_mov_b32 v10, 0
v_sub_f32 v28, 0, v83
v_sub_f32 v29, 0, v83
v_sub_f32 v30, 0, v83
v_sub_f32 v31, 0, v83
v_mov_b32 v11, 0
v_exp_f32 v32, v32
v_exp_f32 v33, v33
buffer_load_dwordx4 v6, s[36:39], s44 offen offset:2176 lds
v_exp_f32 v34, v34
v_add_f32 v8, v8, v32
v_add_f32 v8, v8, v33
v_add_f32 v8, v8, v34
v_exp_f32 v35, v35
v_exp_f32 v36, v36
v_exp_f32 v37, v37
v_add_f32 v8, v8, v35
v_add_f32 v9, v9, v36
buffer_load_dwordx4 v7, s[36:39], s44 offen offset:3264 lds
v_exp_f32 v38, v38
v_exp_f32 v39, v39
v_add_f32 v9, v9, v37
v_exp_f32 v40, v40
s_add_u32 m0, s45, 4352
v_add_f32 v9, v9, v38
v_exp_f32 v41, v41
v_add_f32 v9, v9, v39
v_add_f32 v10, v10, v40
buffer_load_dwordx4 v4, s[36:39], s49 offen offset:0 lds
v_exp_f32 v42, v42
v_exp_f32 v43, v43
v_add_f32 v10, v10, v41
v_exp_f32 v44, v44
v_add_f32 v10, v10, v42
v_add_f32 v10, v10, v43
v_exp_f32 v45, v45
v_exp_f32 v46, v46
buffer_load_dwordx4 v5, s[36:39], s49 offen offset:1088 lds
v_add_f32 v11, v11, v44
v_add_f32 v11, v11, v45
v_exp_f32 v47, v47
v_add_f32 v11, v11, v46
v_exp_f32 v48, v48
v_exp_f32 v49, v49
v_add_f32 v11, v11, v47
v_add_f32 v8, v8, v48
v_add_f32 v8, v8, v49
buffer_load_dwordx4 v6, s[36:39], s49 offen offset:2176 lds
buffer_load_dwordx4 v7, s[36:39], s49 offen offset:3264 lds
s_waitcnt vmcnt(16) lgkmcnt(0)
s_barrier
ds_read_b128 v[96:99], v12 offset:34816
ds_read_b128 v[100:103], v12 offset:34880
s_add_u32 s44, s44, 32768
s_add_u32 s49, s49, 32768
ds_read_b128 v[104:107], v12 offset:34944
ds_read_b128 v[108:111], v12 offset:35008
L_p0%=_next:
s_waitcnt lgkmcnt(2)
v_mfma_f32_16x16x32_bf16 v[64:67], v[96:99], v[192:195], v[16:19]
v_exp_f32 v50, v50
v_mfma_f32_16x16x32_bf16 v[64:67], v[100:103], v[196:199], v[64:67]
ds_read_b128 v[112:115], v12 offset:35072
s_add_u32 m0, s46, 34816
v_mfma_f32_16x16x32_bf16 v[68:71], v[96:99], v[224:227], v[20:23]
buffer_load_dwordx4 v4, s[40:43], s44 offen offset:0 lds
v_mfma_f32_16x16x32_bf16 v[68:71], v[100:103], v[228:231], v[68:71]
ds_read_b128 v[116:119], v12 offset:35136
v_add_f32 v8, v8, v50
v_mfma_f32_16x16x32_bf16 v[72:75], v[96:99], v[128:131], v[24:27]
v_exp_f32 v51, v51
v_mfma_f32_16x16x32_bf16 v[72:75], v[100:103], v[132:135], v[72:75]
v_exp_f32 v52, v52
v_mfma_f32_16x16x32_bf16 v[76:79], v[96:99], v[160:163], v[28:31]
buffer_load_dwordx4 v5, s[40:43], s44 offen offset:1088 lds
v_mfma_f32_16x16x32_bf16 v[76:79], v[100:103], v[164:167], v[76:79]
ds_read_b128 v[120:123], v12 offset:35200
s_waitcnt lgkmcnt(3)
v_mfma_f32_16x16x32_bf16 v[64:67], v[104:107], v[200:203], v[64:67]
ds_read_b128 v[124:127], v12 offset:35264
v_add_f32 v8, v8, v51
v_mfma_f32_16x16x32_bf16 v[64:67], v[108:111], v[204:207], v[64:67]
v_exp_f32 v53, v53
v_mfma_f32_16x16x32_bf16 v[68:71], v[104:107], v[232:235], v[68:71]
v_add_f32 v9, v9, v52
v_add_f32 v9, v9, v53
v_mfma_f32_16x16x32_bf16 v[68:71], v[108:111], v[236:239], v[68:71]
buffer_load_dwordx4 v6, s[40:43], s44 offen offset:2176 lds
v_mfma_f32_16x16x32_bf16 v[72:75], v[104:107], v[136:139], v[72:75]
v_exp_f32 v54, v54
v_mfma_f32_16x16x32_bf16 v[72:75], v[108:111], v[140:143], v[72:75]
v_exp_f32 v55, v55
v_mfma_f32_16x16x32_bf16 v[76:79], v[104:107], v[168:171], v[76:79]
ds_read_b128 v[96:99], v12 offset:43520
ds_read_b128 v[100:103], v12 offset:43584
v_mfma_f32_16x16x32_bf16 v[76:79], v[108:111], v[172:175], v[76:79]
v_add_f32 v9, v9, v54
s_waitcnt lgkmcnt(4)
v_mfma_f32_16x16x32_bf16 v[64:67], v[112:115], v[208:211], v[64:67]
v_exp_f32 v56, v56
v_mfma_f32_16x16x32_bf16 v[64:67], v[116:119], v[212:215], v[64:67]
buffer_load_dwordx4 v7, s[40:43], s44 offen offset:3264 lds
v_mfma_f32_16x16x32_bf16 v[68:71], v[112:115], v[240:243], v[68:71]
ds_read_b128 v[104:107], v12 offset:43648
v_add_f32 v9, v9, v55
v_mfma_f32_16x16x32_bf16 v[68:71], v[116:119], v[244:247], v[68:71]
ds_read_b128 v[108:111], v12 offset:43712
s_add_u32 m0, s46, 39168
v_mfma_f32_16x16x32_bf16 v[72:75], v[112:115], v[144:147], v[72:75]
v_exp_f32 v57, v57
v_mfma_f32_16x16x32_bf16 v[72:75], v[116:119], v[148:151], v[72:75]
v_add_f32 v10, v10, v56
v_add_f32 v10, v10, v57
v_mfma_f32_16x16x32_bf16 v[76:79], v[112:115], v[176:179], v[76:79]
buffer_load_dwordx4 v4, s[40:43], s49 offen offset:0 lds
v_mfma_f32_16x16x32_bf16 v[76:79], v[116:119], v[180:183], v[76:79]
ds_read_b128 v[112:115], v12 offset:43776
s_waitcnt lgkmcnt(5)
v_mfma_f32_16x16x32_bf16 v[64:67], v[120:123], v[216:219], v[64:67]
v_exp_f32 v58, v58
v_mfma_f32_16x16x32_bf16 v[64:67], v[124:127], v[220:223], v[64:67]
v_exp_f32 v59, v59
v_mfma_f32_16x16x32_bf16 v[68:71], v[120:123], v[248:251], v[68:71]
v_exp_f32 v60, v60
v_mfma_f32_16x16x32_bf16 v[68:71], v[124:127], v[252:255], v[68:71]
ds_read_b128 v[116:119], v12 offset:43840
v_add_f32 v10, v10, v58
v_mfma_f32_16x16x32_bf16 v[72:75], v[120:123], v[152:155], v[72:75]
buffer_load_dwordx4 v5, s[40:43], s49 offen offset:1088 lds
v_mfma_f32_16x16x32_bf16 v[72:75], v[124:127], v[156:159], v[72:75]
v_add_f32 v10, v10, v59
v_add_f32 v11, v11, v60
v_mfma_f32_16x16x32_bf16 v[76:79], v[120:123], v[184:187], v[76:79]
v_exp_f32 v61, v61
v_mfma_f32_16x16x32_bf16 v[76:79], v[124:127], v[188:191], v[76:79]
ds_read_b128 v[120:123], v12 offset:43904
s_waitcnt lgkmcnt(5)
v_mfma_f32_16x16x32_bf16 v[80:83], v[96:99], v[192:195], v[16:19]
ds_read_b128 v[124:127], v12 offset:43968
v_add_f32 v11, v11, v61
v_mfma_f32_16x16x32_bf16 v[80:83], v[100:103], v[196:199], v[80:83]
v_exp_f32 v62, v62
v_mfma_f32_16x16x32_bf16 v[84:87], v[96:99], v[224:227], v[20:23]
buffer_load_dwordx4 v6, s[40:43], s49 offen offset:2176 lds
v_mfma_f32_16x16x32_bf16 v[84:87], v[100:103], v[228:231], v[84:87]
v_exp_f32 v63, v63
v_mfma_f32_16x16x32_bf16 v[88:91], v[96:99], v[128:131], v[24:27]
v_add_f32 v11, v11, v62
v_add_f32 v11, v11, v63
v_mfma_f32_16x16x32_bf16 v[88:91], v[100:103], v[132:135], v[88:91]
v_cvt_pk_bf16_f32 v32, v32, v33
v_cvt_pk_bf16_f32 v33, v34, v35
v_mfma_f32_16x16x32_bf16 v[92:95], v[96:99], v[160:163], v[28:31]
v_cvt_pk_bf16_f32 v34, v48, v49
v_cvt_pk_bf16_f32 v35, v50, v51
v_mfma_f32_16x16x32_bf16 v[92:95], v[100:103], v[164:167], v[92:95]
v_cvt_pk_bf16_f32 v36, v36, v37
s_waitcnt lgkmcnt(4)
v_mfma_f32_16x16x32_bf16 v[80:83], v[104:107], v[200:203], v[80:83]
v_cvt_pk_bf16_f32 v37, v38, v39
v_cvt_pk_bf16_f32 v38, v52, v53
v_mfma_f32_16x16x32_bf16 v[80:83], v[108:111], v[204:207], v[80:83]
buffer_load_dwordx4 v7, s[40:43], s49 offen offset:3264 lds
v_mfma_f32_16x16x32_bf16 v[84:87], v[104:107], v[232:235], v[84:87]
v_cvt_pk_bf16_f32 v39, v54, v55
v_cvt_pk_bf16_f32 v40, v40, v41
v_mfma_f32_16x16x32_bf16 v[84:87], v[108:111], v[236:239], v[84:87]
v_cvt_pk_bf16_f32 v41, v42, v43
v_cvt_pk_bf16_f32 v42, v56, v57
v_mfma_f32_16x16x32_bf16 v[88:91], v[104:107], v[136:139], v[88:91]
v_cvt_pk_bf16_f32 v43, v58, v59
v_cvt_pk_bf16_f32 v44, v44, v45
v_mfma_f32_16x16x32_bf16 v[88:91], v[108:111], v[140:143], v[88:91]
v_cvt_pk_bf16_f32 v45, v46, v47
v_cvt_pk_bf16_f32 v46, v60, v61
v_mfma_f32_16x16x32_bf16 v[92:95], v[104:107], v[168:171], v[92:95]
v_cvt_pk_bf16_f32 v47, v62, v63
v_max3_f32 v13, v64, v65, v66
v_mfma_f32_16x16x32_bf16 v[92:95], v[108:111], v[172:175], v[92:95]
s_waitcnt vmcnt(16) lgkmcnt(0)
s_barrier
v_mfma_f32_16x16x32_bf16 v[80:83], v[112:115], v[208:211], v[80:83]
ds_read_b64_tr_b16 v[96:97], v2 offset:0
ds_read_b64_tr_b16 v[98:99], v2 offset:8704
v_mfma_f32_16x16x32_bf16 v[80:83], v[116:119], v[212:215], v[80:83]
ds_read_b64_tr_b16 v[100:101], v3 offset:0
v_max3_f32 v14, v67, v68, v69
v_mfma_f32_16x16x32_bf16 v[84:87], v[112:115], v[240:243], v[84:87]
ds_read_b64_tr_b16 v[102:103], v3 offset:8704
ds_read_b64_tr_b16 v[104:105], v2 offset:64
v_mfma_f32_16x16x32_bf16 v[84:87], v[116:119], v[244:247], v[84:87]
ds_read_b64_tr_b16 v[106:107], v2 offset:8768
v_max3_f32 v13, v13, v70, v71
v_mfma_f32_16x16x32_bf16 v[88:91], v[112:115], v[144:147], v[88:91]
ds_read_b64_tr_b16 v[108:109], v3 offset:64
s_add_u32 m0, s45, 34816
v_mfma_f32_16x16x32_bf16 v[88:91], v[116:119], v[148:151], v[88:91]
buffer_load_dwordx4 v4, s[36:39], s44 offen offset:0 lds
v_mfma_f32_16x16x32_bf16 v[92:95], v[112:115], v[176:179], v[92:95]
ds_read_b64_tr_b16 v[110:111], v3 offset:8768
v_max3_f32 v14, v14, v72, v73
v_mfma_f32_16x16x32_bf16 v[92:95], v[116:119], v[180:183], v[92:95]
v_max3_f32 v13, v13, v74, v75
v_max3_f32 v14, v14, v76, v77
v_mfma_f32_16x16x32_bf16 v[80:83], v[120:123], v[216:219], v[80:83]
ds_read_b64_tr_b16 v[112:113], v2 offset:128
v_max3_f32 v13, v13, v78, v79
v_mfma_f32_16x16x32_bf16 v[80:83], v[124:127], v[220:223], v[80:83]
ds_read_b64_tr_b16 v[114:115], v2 offset:8832
ds_read_b64_tr_b16 v[116:117], v3 offset:128
v_mfma_f32_16x16x32_bf16 v[84:87], v[120:123], v[248:251], v[84:87]
ds_read_b64_tr_b16 v[118:119], v3 offset:8832
v_mfma_f32_16x16x32_bf16 v[84:87], v[124:127], v[252:255], v[84:87]
v_mfma_f32_16x16x32_bf16 v[88:91], v[120:123], v[152:155], v[88:91]
buffer_load_dwordx4 v5, s[36:39], s44 offen offset:1088 lds
v_mfma_f32_16x16x32_bf16 v[88:91], v[124:127], v[156:159], v[88:91]
v_mfma_f32_16x16x32_bf16 v[92:95], v[120:123], v[184:187], v[92:95]
v_mfma_f32_16x16x32_bf16 v[92:95], v[124:127], v[188:191], v[92:95]
ds_read_b64_tr_b16 v[120:121], v2 offset:192
s_waitcnt lgkmcnt(9)
v_mfma_f32_16x16x32_bf16 a[0:3], v[32:35], v[96:99], 0
ds_read_b64_tr_b16 v[122:123], v2 offset:8896
ds_read_b64_tr_b16 v[124:125], v3 offset:192
v_mfma_f32_16x16x32_bf16 a[16:19], v[32:35], v[100:103], 0
ds_read_b64_tr_b16 v[126:127], v3 offset:8896
v_mfma_f32_16x16x32_bf16 a[4:7], v[36:39], v[96:99], 0
v_max3_f32 v14, v14, v80, v81
v_max3_f32 v13, v13, v82, v83
v_mfma_f32_16x16x32_bf16 a[20:23], v[36:39], v[100:103], 0
v_max3_f32 v14, v14, v84, v85
v_max3_f32 v13, v13, v86, v87
v_mfma_f32_16x16x32_bf16 a[8:11], v[40:43], v[96:99], 0
buffer_load_dwordx4 v6, s[36:39], s44 offen offset:2176 lds
v_mfma_f32_16x16x32_bf16 a[24:27], v[40:43], v[100:103], 0
v_max3_f32 v14, v14, v88, v89
v_max3_f32 v13, v13, v90, v91
v_mfma_f32_16x16x32_bf16 a[12:15], v[44:47], v[96:99], 0
v_max3_f32 v14, v14, v92, v93
v_max3_f32 v13, v13, v94, v95
v_mfma_f32_16x16x32_bf16 a[28:31], v[44:47], v[100:103], 0
v_max_f32 v13, v13, v14
s_waitcnt lgkmcnt(8)
v_mfma_f32_16x16x32_bf16 a[44:47], v[44:47], v[104:107], 0
ds_read_b64_tr_b16 v[96:97], v2 offset:256
v_cmp_lt_f32 vcc, s48, v13
v_mfma_f32_16x16x32_bf16 a[60:63], v[44:47], v[108:111], 0
ds_read_b64_tr_b16 v[98:99], v2 offset:8960
ds_read_b64_tr_b16 v[100:101], v3 offset:256
v_mfma_f32_16x16x32_bf16 a[40:43], v[40:43], v[104:107], 0
s_cbranch_vccnz L_p1%=_rare
ds_read_b64_tr_b16 v[102:103], v3 offset:8960
v_mfma_f32_16x16x32_bf16 a[56:59], v[40:43], v[108:111], 0
v_exp_f32 v64, v64
v_mfma_f32_16x16x32_bf16 a[36:39], v[36:39], v[104:107], 0
buffer_load_dwordx4 v7, s[36:39], s44 offen offset:3264 lds
v_mfma_f32_16x16x32_bf16 a[52:55], v[36:39], v[108:111], 0
v_exp_f32 v65, v65
v_mfma_f32_16x16x32_bf16 a[32:35], v[32:35], v[104:107], 0
v_exp_f32 v66, v66
v_mfma_f32_16x16x32_bf16 a[48:51], v[32:35], v[108:111], 0
v_add_f32 v8, v8, v64
s_waitcnt lgkmcnt(8)
v_mfma_f32_16x16x32_bf16 a[64:67], v[32:35], v[112:115], 0
v_exp_f32 v67, v67
v_mfma_f32_16x16x32_bf16 a[80:83], v[32:35], v[116:119], 0
s_add_u32 m0, s45, 39168
v_add_f32 v8, v8, v65
v_mfma_f32_16x16x32_bf16 a[68:71], v[36:39], v[112:115], 0
v_exp_f32 v68, v68
v_mfma_f32_16x16x32_bf16 a[84:87], v[36:39], v[116:119], 0
v_add_f32 v8, v8, v66
v_add_f32 v8, v8, v67
v_mfma_f32_16x16x32_bf16 a[72:75], v[40:43], v[112:115], 0
buffer_load_dwordx4 v4, s[36:39], s49 offen offset:0 lds
v_mfma_f32_16x16x32_bf16 a[88:91], v[40:43], v[116:119], 0
v_exp_f32 v69, v69
v_mfma_f32_16x16x32_bf16 a[76:79], v[44:47], v[112:115], 0
v_exp_f32 v70, v70
v_mfma_f32_16x16x32_bf16 a[92:95], v[44:47], v[116:119], 0
ds_read_b64_tr_b16 v[104:105], v2 offset:320
s_waitcnt lgkmcnt(5)
v_mfma_f32_16x16x32_bf16 a[108:111], v[44:47], v[120:123], 0
ds_read_b64_tr_b16 v[106:107], v2 offset:9024
ds_read_b64_tr_b16 v[108:109], v3 offset:320
v_mfma_f32_16x16x32_bf16 a[124:127], v[44:47], v[124:127], 0
ds_read_b64_tr_b16 v[110:111], v3 offset:9024
v_add_f32 v9, v9, v68
v_mfma_f32_16x16x32_bf16 a[104:107], v[40:43], v[120:123], 0
v_exp_f32 v71, v71
v_mfma_f32_16x16x32_bf16 a[120:123], v[40:43], v[124:127], 0
v_add_f32 v9, v9, v69
v_add_f32 v9, v9, v70
v_mfma_f32_16x16x32_bf16 a[100:103], v[36:39], v[120:123], 0
buffer_load_dwordx4 v5, s[36:39], s49 offen offset:1088 lds
v_mfma_f32_16x16x32_bf16 a[116:119], v[36:39], v[124:127], 0
v_exp_f32 v72, v72
v_mfma_f32_16x16x32_bf16 a[96:99], v[32:35], v[120:123], 0
v_exp_f32 v73, v73
v_mfma_f32_16x16x32_bf16 a[112:115], v[32:35], v[124:127], 0
ds_read_b64_tr_b16 v[112:113], v2 offset:384
s_waitcnt lgkmcnt(5)
v_mfma_f32_16x16x32_bf16 a[128:131], v[32:35], v[96:99], 0
ds_read_b64_tr_b16 v[114:115], v2 offset:9088
ds_read_b64_tr_b16 v[116:117], v3 offset:384
v_mfma_f32_16x16x32_bf16 a[144:147], v[32:35], v[100:103], 0
ds_read_b64_tr_b16 v[118:119], v3 offset:9088
v_add_f32 v9, v9, v71
v_mfma_f32_16x16x32_bf16 a[132:135], v[36:39], v[96:99], 0
ds_read_b64_tr_b16 v[120:121], v2 offset:448
ds_read_b64_tr_b16 v[122:123], v2 offset:9152
v_mfma_f32_16x16x32_bf16 a[148:151], v[36:39], v[100:103], 0
ds_read_b64_tr_b16 v[124:125], v3 offset:448
v_add_f32 v10, v10, v72
v_mfma_f32_16x16x32_bf16 a[136:139], v[40:43], v[96:99], 0
buffer_load_dwordx4 v6, s[36:39], s49 offen offset:2176 lds
v_mfma_f32_16x16x32_bf16 a[152:155], v[40:43], v[100:103], 0
ds_read_b64_tr_b16 v[126:127], v3 offset:9152
v_add_f32 v10, v10, v73
v_mfma_f32_16x16x32_bf16 a[140:143], v[44:47], v[96:99], 0
v_exp_f32 v74, v74
v_mfma_f32_16x16x32_bf16 a[156:159], v[44:47], v[100:103], 0
v_add_f32 v10, v10, v74
s_waitcnt lgkmcnt(8)
v_mfma_f32_16x16x32_bf16 a[172:175], v[44:47], v[104:107], 0
v_exp_f32 v75, v75
v_mfma_f32_16x16x32_bf16 a[188:191], v[44:47], v[108:111], 0
v_exp_f32 v76, v76
v_mfma_f32_16x16x32_bf16 a[168:171], v[40:43], v[104:107], 0
buffer_load_dwordx4 v7, s[36:39], s49 offen offset:3264 lds
v_mfma_f32_16x16x32_bf16 a[184:187], v[40:43], v[108:111], 0
v_exp_f32 v77, v77
v_mfma_f32_16x16x32_bf16 a[164:167], v[36:39], v[104:107], 0
v_add_f32 v10, v10, v75
v_add_f32 v11, v11, v76
v_mfma_f32_16x16x32_bf16 a[180:183], v[36:39], v[108:111], 0
v_exp_f32 v78, v78
v_mfma_f32_16x16x32_bf16 a[160:163], v[32:35], v[104:107], 0
v_exp_f32 v79, v79
v_mfma_f32_16x16x32_bf16 a[176:179], v[32:35], v[108:111], 0
s_waitcnt vmcnt(16) lgkmcnt(0)
s_barrier
v_mfma_f32_16x16x32_bf16 a[192:195], v[32:35], v[112:115], 0
ds_read_b128 v[96:99], v12 offset:0
v_add_f32 v11, v11, v77
v_mfma_f32_16x16x32_bf16 a[208:211], v[32:35], v[116:119], 0
ds_read_b128 v[100:103], v12 offset:64
v_add_f32 v11, v11, v78
v_mfma_f32_16x16x32_bf16 a[196:199], v[36:39], v[112:115], 0
v_exp_f32 v80, v80
v_mfma_f32_16x16x32_bf16 a[212:215], v[36:39], v[116:119], 0
v_exp_f32 v81, v81
v_mfma_f32_16x16x32_bf16 a[200:203], v[40:43], v[112:115], 0
v_add_f32 v11, v11, v79
s_add_u32 s44, s44, 32768
v_mfma_f32_16x16x32_bf16 a[216:219], v[40:43], v[116:119], 0
s_add_u32 s49, s49, 32768
v_add_f32 v8, v8, v80
v_mfma_f32_16x16x32_bf16 a[204:207], v[44:47], v[112:115], 0
ds_read_b128 v[104:107], v12 offset:128
ds_read_b128 v[108:111], v12 offset:192
v_mfma_f32_16x16x32_bf16 a[220:223], v[44:47], v[116:119], 0
v_add_f32 v8, v8, v81
v_mfma_f32_16x16x32_bf16 a[236:239], v[44:47], v[120:123], 0
v_mfma_f32_16x16x32_bf16 a[252:255], v[44:47], v[124:127], 0
v_mfma_f32_16x16x32_bf16 a[232:235], v[40:43], v[120:123], 0
v_mfma_f32_16x16x32_bf16 a[248:251], v[40:43], v[124:127], 0
v_mfma_f32_16x16x32_bf16 a[228:231], v[36:39], v[120:123], 0
v_mfma_f32_16x16x32_bf16 a[244:247], v[36:39], v[124:127], 0
v_mfma_f32_16x16x32_bf16 a[224:227], v[32:35], v[120:123], 0
v_mfma_f32_16x16x32_bf16 a[240:243], v[32:35], v[124:127], 0
L_p1%=_next:
s_mov_b32 s47, 14
.p2align 6
L_loop%=:
s_waitcnt lgkmcnt(2)
v_mfma_f32_16x16x32_bf16 v[32:35], v[96:99], v[192:195], v[16:19]
v_exp_f32 v82, v82
v_mfma_f32_16x16x32_bf16 v[32:35], v[100:103], v[196:199], v[32:35]
ds_read_b128 v[112:115], v12 offset:256
s_add_u32 m0, s46, 0
v_mfma_f32_16x16x32_bf16 v[36:39], v[96:99], v[224:227], v[20:23]
buffer_load_dwordx4 v4, s[40:43], s44 offen offset:0 lds
v_mfma_f32_16x16x32_bf16 v[36:39], v[100:103], v[228:231], v[36:39]
ds_read_b128 v[116:119], v12 offset:320
v_add_f32 v8, v8, v82
v_mfma_f32_16x16x32_bf16 v[40:43], v[96:99], v[128:131], v[24:27]
v_exp_f32 v83, v83
v_mfma_f32_16x16x32_bf16 v[40:43], v[100:103], v[132:135], v[40:43]
v_exp_f32 v84, v84
v_mfma_f32_16x16x32_bf16 v[44:47], v[96:99], v[160:163], v[28:31]
buffer_load_dwordx4 v5, s[40:43], s44 offen offset:1088 lds
v_mfma_f32_16x16x32_bf16 v[44:47], v[100:103], v[164:167], v[44:47]
ds_read_b128 v[120:123], v12 offset:384
s_waitcnt lgkmcnt(3)
v_mfma_f32_16x16x32_bf16 v[32:35], v[104:107], v[200:203], v[32:35]
ds_read_b128 v[124:127], v12 offset:448
v_add_f32 v8, v8, v83
v_mfma_f32_16x16x32_bf16 v[32:35], v[108:111], v[204:207], v[32:35]
v_exp_f32 v85, v85
v_mfma_f32_16x16x32_bf16 v[36:39], v[104:107], v[232:235], v[36:39]
v_add_f32 v9, v9, v84
v_add_f32 v9, v9, v85
v_mfma_f32_16x16x32_bf16 v[36:39], v[108:111], v[236:239], v[36:39]
buffer_load_dwordx4 v6, s[40:43], s44 offen offset:2176 lds
v_mfma_f32_16x16x32_bf16 v[40:43], v[104:107], v[136:139], v[40:43]
v_exp_f32 v86, v86
v_mfma_f32_16x16x32_bf16 v[40:43], v[108:111], v[140:143], v[40:43]
v_exp_f32 v87, v87
v_mfma_f32_16x16x32_bf16 v[44:47], v[104:107], v[168:171], v[44:47]
ds_read_b128 v[96:99], v12 offset:8704
ds_read_b128 v[100:103], v12 offset:8768
v_mfma_f32_16x16x32_bf16 v[44:47], v[108:111], v[172:175], v[44:47]
v_add_f32 v9, v9, v86
s_waitcnt lgkmcnt(4)
v_mfma_f32_16x16x32_bf16 v[32:35], v[112:115], v[208:211], v[32:35]
v_exp_f32 v88, v88
v_mfma_f32_16x16x32_bf16 v[32:35], v[116:119], v[212:215], v[32:35]
buffer_load_dwordx4 v7, s[40:43], s44 offen offset:3264 lds
v_mfma_f32_16x16x32_bf16 v[36:39], v[112:115], v[240:243], v[36:39]
ds_read_b128 v[104:107], v12 offset:8832
v_add_f32 v9, v9, v87
v_mfma_f32_16x16x32_bf16 v[36:39], v[116:119], v[244:247], v[36:39]
ds_read_b128 v[108:111], v12 offset:8896
s_add_u32 m0, s46, 4352
v_mfma_f32_16x16x32_bf16 v[40:43], v[112:115], v[144:147], v[40:43]
v_exp_f32 v89, v89
v_mfma_f32_16x16x32_bf16 v[40:43], v[116:119], v[148:151], v[40:43]
v_add_f32 v10, v10, v88
v_add_f32 v10, v10, v89
v_mfma_f32_16x16x32_bf16 v[44:47], v[112:115], v[176:179], v[44:47]
buffer_load_dwordx4 v4, s[40:43], s49 offen offset:0 lds
v_mfma_f32_16x16x32_bf16 v[44:47], v[116:119], v[180:183], v[44:47]
ds_read_b128 v[112:115], v12 offset:8960
s_waitcnt lgkmcnt(5)
v_mfma_f32_16x16x32_bf16 v[32:35], v[120:123], v[216:219], v[32:35]
v_exp_f32 v90, v90
v_mfma_f32_16x16x32_bf16 v[32:35], v[124:127], v[220:223], v[32:35]
v_exp_f32 v91, v91
v_mfma_f32_16x16x32_bf16 v[36:39], v[120:123], v[248:251], v[36:39]
v_exp_f32 v92, v92
v_mfma_f32_16x16x32_bf16 v[36:39], v[124:127], v[252:255], v[36:39]
ds_read_b128 v[116:119], v12 offset:9024
v_add_f32 v10, v10, v90
v_mfma_f32_16x16x32_bf16 v[40:43], v[120:123], v[152:155], v[40:43]
buffer_load_dwordx4 v5, s[40:43], s49 offen offset:1088 lds
v_mfma_f32_16x16x32_bf16 v[40:43], v[124:127], v[156:159], v[40:43]
v_add_f32 v10, v10, v91
v_add_f32 v11, v11, v92
v_mfma_f32_16x16x32_bf16 v[44:47], v[120:123], v[184:187], v[44:47]
v_exp_f32 v93, v93
v_mfma_f32_16x16x32_bf16 v[44:47], v[124:127], v[188:191], v[44:47]
ds_read_b128 v[120:123], v12 offset:9088
s_waitcnt lgkmcnt(5)
v_mfma_f32_16x16x32_bf16 v[48:51], v[96:99], v[192:195], v[16:19]
ds_read_b128 v[124:127], v12 offset:9152
v_add_f32 v11, v11, v93
v_mfma_f32_16x16x32_bf16 v[48:51], v[100:103], v[196:199], v[48:51]
v_exp_f32 v94, v94
v_mfma_f32_16x16x32_bf16 v[52:55], v[96:99], v[224:227], v[20:23]
buffer_load_dwordx4 v6, s[40:43], s49 offen offset:2176 lds
v_mfma_f32_16x16x32_bf16 v[52:55], v[100:103], v[228:231], v[52:55]
v_exp_f32 v95, v95
v_mfma_f32_16x16x32_bf16 v[56:59], v[96:99], v[128:131], v[24:27]
v_add_f32 v11, v11, v94
v_add_f32 v11, v11, v95
v_mfma_f32_16x16x32_bf16 v[56:59], v[100:103], v[132:135], v[56:59]
v_cvt_pk_bf16_f32 v64, v64, v65
v_cvt_pk_bf16_f32 v65, v66, v67
v_mfma_f32_16x16x32_bf16 v[60:63], v[96:99], v[160:163], v[28:31]
v_cvt_pk_bf16_f32 v66, v80, v81
v_cvt_pk_bf16_f32 v67, v82, v83
v_mfma_f32_16x16x32_bf16 v[60:63], v[100:103], v[164:167], v[60:63]
v_cvt_pk_bf16_f32 v68, v68, v69
s_waitcnt lgkmcnt(4)
v_mfma_f32_16x16x32_bf16 v[48:51], v[104:107], v[200:203], v[48:51]
v_cvt_pk_bf16_f32 v69, v70, v71
v_cvt_pk_bf16_f32 v70, v84, v85
v_mfma_f32_16x16x32_bf16 v[48:51], v[108:111], v[204:207], v[48:51]
buffer_load_dwordx4 v7, s[40:43], s49 offen offset:3264 lds
v_mfma_f32_16x16x32_bf16 v[52:55], v[104:107], v[232:235], v[52:55]
v_cvt_pk_bf16_f32 v71, v86, v87
v_cvt_pk_bf16_f32 v72, v72, v73
v_mfma_f32_16x16x32_bf16 v[52:55], v[108:111], v[236:239], v[52:55]
v_cvt_pk_bf16_f32 v73, v74, v75
v_cvt_pk_bf16_f32 v74, v88, v89
v_mfma_f32_16x16x32_bf16 v[56:59], v[104:107], v[136:139], v[56:59]
v_cvt_pk_bf16_f32 v75, v90, v91
v_cvt_pk_bf16_f32 v76, v76, v77
v_mfma_f32_16x16x32_bf16 v[56:59], v[108:111], v[140:143], v[56:59]
v_cvt_pk_bf16_f32 v77, v78, v79
v_cvt_pk_bf16_f32 v78, v92, v93
v_mfma_f32_16x16x32_bf16 v[60:63], v[104:107], v[168:171], v[60:63]
v_cvt_pk_bf16_f32 v79, v94, v95
v_max3_f32 v13, v32, v33, v34
v_mfma_f32_16x16x32_bf16 v[60:63], v[108:111], v[172:175], v[60:63]
s_waitcnt vmcnt(16) lgkmcnt(0)
s_barrier
v_mfma_f32_16x16x32_bf16 v[48:51], v[112:115], v[208:211], v[48:51]
ds_read_b64_tr_b16 v[96:97], v2 offset:34816
ds_read_b64_tr_b16 v[98:99], v2 offset:43520
v_mfma_f32_16x16x32_bf16 v[48:51], v[116:119], v[212:215], v[48:51]
ds_read_b64_tr_b16 v[100:101], v3 offset:34816
v_max3_f32 v14, v35, v36, v37
v_mfma_f32_16x16x32_bf16 v[52:55], v[112:115], v[240:243], v[52:55]
ds_read_b64_tr_b16 v[102:103], v3 offset:43520
ds_read_b64_tr_b16 v[104:105], v2 offset:34880
v_mfma_f32_16x16x32_bf16 v[52:55], v[116:119], v[244:247], v[52:55]
ds_read_b64_tr_b16 v[106:107], v2 offset:43584
v_max3_f32 v13, v13, v38, v39
v_mfma_f32_16x16x32_bf16 v[56:59], v[112:115], v[144:147], v[56:59]
ds_read_b64_tr_b16 v[108:109], v3 offset:34880
s_add_u32 m0, s45, 0
v_mfma_f32_16x16x32_bf16 v[56:59], v[116:119], v[148:151], v[56:59]
buffer_load_dwordx4 v4, s[36:39], s44 offen offset:0 lds
v_mfma_f32_16x16x32_bf16 v[60:63], v[112:115], v[176:179], v[60:63]
ds_read_b64_tr_b16 v[110:111], v3 offset:43584
v_max3_f32 v14, v14, v40, v41
v_mfma_f32_16x16x32_bf16 v[60:63], v[116:119], v[180:183], v[60:63]
v_max3_f32 v13, v13, v42, v43
v_max3_f32 v14, v14, v44, v45
v_mfma_f32_16x16x32_bf16 v[48:51], v[120:123], v[216:219], v[48:51]
ds_read_b64_tr_b16 v[112:113], v2 offset:34944
v_max3_f32 v13, v13, v46, v47
v_mfma_f32_16x16x32_bf16 v[48:51], v[124:127], v[220:223], v[48:51]
ds_read_b64_tr_b16 v[114:115], v2 offset:43648
ds_read_b64_tr_b16 v[116:117], v3 offset:34944
v_mfma_f32_16x16x32_bf16 v[52:55], v[120:123], v[248:251], v[52:55]
ds_read_b64_tr_b16 v[118:119], v3 offset:43648
v_mfma_f32_16x16x32_bf16 v[52:55], v[124:127], v[252:255], v[52:55]
v_mfma_f32_16x16x32_bf16 v[56:59], v[120:123], v[152:155], v[56:59]
buffer_load_dwordx4 v5, s[36:39], s44 offen offset:1088 lds
v_mfma_f32_16x16x32_bf16 v[56:59], v[124:127], v[156:159], v[56:59]
v_mfma_f32_16x16x32_bf16 v[60:63], v[120:123], v[184:187], v[60:63]
v_mfma_f32_16x16x32_bf16 v[60:63], v[124:127], v[188:191], v[60:63]
ds_read_b64_tr_b16 v[120:121], v2 offset:35008
s_waitcnt lgkmcnt(9)
v_mfma_f32_16x16x32_bf16 a[0:3], v[64:67], v[96:99], a[0:3]
ds_read_b64_tr_b16 v[122:123], v2 offset:43712
ds_read_b64_tr_b16 v[124:125], v3 offset:35008
v_mfma_f32_16x16x32_bf16 a[16:19], v[64:67], v[100:103], a[16:19]
ds_read_b64_tr_b16 v[126:127], v3 offset:43712
v_mfma_f32_16x16x32_bf16 a[4:7], v[68:71], v[96:99], a[4:7]
v_max3_f32 v14, v14, v48, v49
v_max3_f32 v13, v13, v50, v51
v_mfma_f32_16x16x32_bf16 a[20:23], v[68:71], v[100:103], a[20:23]
v_max3_f32 v14, v14, v52, v53
v_max3_f32 v13, v13, v54, v55
v_mfma_f32_16x16x32_bf16 a[8:11], v[72:75], v[96:99], a[8:11]
buffer_load_dwordx4 v6, s[36:39], s44 offen offset:2176 lds
v_mfma_f32_16x16x32_bf16 a[24:27], v[72:75], v[100:103], a[24:27]
v_max3_f32 v14, v14, v56, v57
v_max3_f32 v13, v13, v58, v59
v_mfma_f32_16x16x32_bf16 a[12:15], v[76:79], v[96:99], a[12:15]
v_max3_f32 v14, v14, v60, v61
v_max3_f32 v13, v13, v62, v63
v_mfma_f32_16x16x32_bf16 a[28:31], v[76:79], v[100:103], a[28:31]
v_max_f32 v13, v13, v14
s_waitcnt lgkmcnt(8)
v_mfma_f32_16x16x32_bf16 a[44:47], v[76:79], v[104:107], a[44:47]
ds_read_b64_tr_b16 v[96:97], v2 offset:35072
v_cmp_lt_f32 vcc, s48, v13
v_mfma_f32_16x16x32_bf16 a[60:63], v[76:79], v[108:111], a[60:63]
ds_read_b64_tr_b16 v[98:99], v2 offset:43776
ds_read_b64_tr_b16 v[100:101], v3 offset:35072
v_mfma_f32_16x16x32_bf16 a[40:43], v[72:75], v[104:107], a[40:43]
s_cbranch_vccnz L_l2%=_rare
ds_read_b64_tr_b16 v[102:103], v3 offset:43776
v_mfma_f32_16x16x32_bf16 a[56:59], v[72:75], v[108:111], a[56:59]
v_exp_f32 v32, v32
v_mfma_f32_16x16x32_bf16 a[36:39], v[68:71], v[104:107], a[36:39]
buffer_load_dwordx4 v7, s[36:39], s44 offen offset:3264 lds
v_mfma_f32_16x16x32_bf16 a[52:55], v[68:71], v[108:111], a[52:55]
v_exp_f32 v33, v33
v_mfma_f32_16x16x32_bf16 a[32:35], v[64:67], v[104:107], a[32:35]
v_exp_f32 v34, v34
v_mfma_f32_16x16x32_bf16 a[48:51], v[64:67], v[108:111], a[48:51]
v_add_f32 v8, v8, v32
s_waitcnt lgkmcnt(8)
v_mfma_f32_16x16x32_bf16 a[64:67], v[64:67], v[112:115], a[64:67]
v_exp_f32 v35, v35
v_mfma_f32_16x16x32_bf16 a[80:83], v[64:67], v[116:119], a[80:83]
s_add_u32 m0, s45, 4352
v_add_f32 v8, v8, v33
v_mfma_f32_16x16x32_bf16 a[68:71], v[68:71], v[112:115], a[68:71]
v_exp_f32 v36, v36
v_mfma_f32_16x16x32_bf16 a[84:87], v[68:71], v[116:119], a[84:87]
v_add_f32 v8, v8, v34
v_add_f32 v8, v8, v35
v_mfma_f32_16x16x32_bf16 a[72:75], v[72:75], v[112:115], a[72:75]
buffer_load_dwordx4 v4, s[36:39], s49 offen offset:0 lds
v_mfma_f32_16x16x32_bf16 a[88:91], v[72:75], v[116:119], a[88:91]
v_exp_f32 v37, v37
v_mfma_f32_16x16x32_bf16 a[76:79], v[76:79], v[112:115], a[76:79]
v_exp_f32 v38, v38
v_mfma_f32_16x16x32_bf16 a[92:95], v[76:79], v[116:119], a[92:95]
ds_read_b64_tr_b16 v[104:105], v2 offset:35136
s_waitcnt lgkmcnt(5)
v_mfma_f32_16x16x32_bf16 a[108:111], v[76:79], v[120:123], a[108:111]
ds_read_b64_tr_b16 v[106:107], v2 offset:43840
ds_read_b64_tr_b16 v[108:109], v3 offset:35136
v_mfma_f32_16x16x32_bf16 a[124:127], v[76:79], v[124:127], a[124:127]
ds_read_b64_tr_b16 v[110:111], v3 offset:43840
v_add_f32 v9, v9, v36
v_mfma_f32_16x16x32_bf16 a[104:107], v[72:75], v[120:123], a[104:107]
v_exp_f32 v39, v39
v_mfma_f32_16x16x32_bf16 a[120:123], v[72:75], v[124:127], a[120:123]
v_add_f32 v9, v9, v37
v_add_f32 v9, v9, v38
v_mfma_f32_16x16x32_bf16 a[100:103], v[68:71], v[120:123], a[100:103]
buffer_load_dwordx4 v5, s[36:39], s49 offen offset:1088 lds
v_mfma_f32_16x16x32_bf16 a[116:119], v[68:71], v[124:127], a[116:119]
v_exp_f32 v40, v40
v_mfma_f32_16x16x32_bf16 a[96:99], v[64:67], v[120:123], a[96:99]
v_exp_f32 v41, v41
v_mfma_f32_16x16x32_bf16 a[112:115], v[64:67], v[124:127], a[112:115]
ds_read_b64_tr_b16 v[112:113], v2 offset:35200
s_waitcnt lgkmcnt(5)
v_mfma_f32_16x16x32_bf16 a[128:131], v[64:67], v[96:99], a[128:131]
ds_read_b64_tr_b16 v[114:115], v2 offset:43904
ds_read_b64_tr_b16 v[116:117], v3 offset:35200
v_mfma_f32_16x16x32_bf16 a[144:147], v[64:67], v[100:103], a[144:147]
ds_read_b64_tr_b16 v[118:119], v3 offset:43904
v_add_f32 v9, v9, v39
v_mfma_f32_16x16x32_bf16 a[132:135], v[68:71], v[96:99], a[132:135]
ds_read_b64_tr_b16 v[120:121], v2 offset:35264
ds_read_b64_tr_b16 v[122:123], v2 offset:43968
v_mfma_f32_16x16x32_bf16 a[148:151], v[68:71], v[100:103], a[148:151]
ds_read_b64_tr_b16 v[124:125], v3 offset:35264
v_add_f32 v10, v10, v40
v_mfma_f32_16x16x32_bf16 a[136:139], v[72:75], v[96:99], a[136:139]
buffer_load_dwordx4 v6, s[36:39], s49 offen offset:2176 lds
v_mfma_f32_16x16x32_bf16 a[152:155], v[72:75], v[100:103], a[152:155]
ds_read_b64_tr_b16 v[126:127], v3 offset:43968
v_add_f32 v10, v10, v41
v_mfma_f32_16x16x32_bf16 a[140:143], v[76:79], v[96:99], a[140:143]
v_exp_f32 v42, v42
v_mfma_f32_16x16x32_bf16 a[156:159], v[76:79], v[100:103], a[156:159]
v_add_f32 v10, v10, v42
s_waitcnt lgkmcnt(8)
v_mfma_f32_16x16x32_bf16 a[172:175], v[76:79], v[104:107], a[172:175]
v_exp_f32 v43, v43
v_mfma_f32_16x16x32_bf16 a[188:191], v[76:79], v[108:111], a[188:191]
v_exp_f32 v44, v44
v_mfma_f32_16x16x32_bf16 a[168:171], v[72:75], v[104:107], a[168:171]
buffer_load_dwordx4 v7, s[36:39], s49 offen offset:3264 lds
v_mfma_f32_16x16x32_bf16 a[184:187], v[72:75], v[108:111], a[184:187]
v_exp_f32 v45, v45
v_mfma_f32_16x16x32_bf16 a[164:167], v[68:71], v[104:107], a[164:167]
v_add_f32 v10, v10, v43
v_add_f32 v11, v11, v44
v_mfma_f32_16x16x32_bf16 a[180:183], v[68:71], v[108:111], a[180:183]
v_exp_f32 v46, v46
v_mfma_f32_16x16x32_bf16 a[160:163], v[64:67], v[104:107], a[160:163]
v_exp_f32 v47, v47
v_mfma_f32_16x16x32_bf16 a[176:179], v[64:67], v[108:111], a[176:179]
s_waitcnt vmcnt(16) lgkmcnt(0)
s_barrier
v_mfma_f32_16x16x32_bf16 a[192:195], v[64:67], v[112:115], a[192:195]
ds_read_b128 v[96:99], v12 offset:34816
v_add_f32 v11, v11, v45
v_mfma_f32_16x16x32_bf16 a[208:211], v[64:67], v[116:119], a[208:211]
ds_read_b128 v[100:103], v12 offset:34880
v_add_f32 v11, v11, v46
v_mfma_f32_16x16x32_bf16 a[196:199], v[68:71], v[112:115], a[196:199]
v_exp_f32 v48, v48
v_mfma_f32_16x16x32_bf16 a[212:215], v[68:71], v[116:119], a[212:215]
v_exp_f32 v49, v49
v_mfma_f32_16x16x32_bf16 a[200:203], v[72:75], v[112:115], a[200:203]
v_add_f32 v11, v11, v47
s_add_u32 s44, s44, 32768
v_mfma_f32_16x16x32_bf16 a[216:219], v[72:75], v[116:119], a[216:219]
s_add_u32 s49, s49, 32768
v_add_f32 v8, v8, v48
v_mfma_f32_16x16x32_bf16 a[204:207], v[76:79], v[112:115], a[204:207]
ds_read_b128 v[104:107], v12 offset:34944
ds_read_b128 v[108:111], v12 offset:35008
v_mfma_f32_16x16x32_bf16 a[220:223], v[76:79], v[116:119], a[220:223]
v_add_f32 v8, v8, v49
v_mfma_f32_16x16x32_bf16 a[236:239], v[76:79], v[120:123], a[236:239]
v_mfma_f32_16x16x32_bf16 a[252:255], v[76:79], v[124:127], a[252:255]
v_mfma_f32_16x16x32_bf16 a[232:235], v[72:75], v[120:123], a[232:235]
v_mfma_f32_16x16x32_bf16 a[248:251], v[72:75], v[124:127], a[248:251]
v_mfma_f32_16x16x32_bf16 a[228:231], v[68:71], v[120:123], a[228:231]
v_mfma_f32_16x16x32_bf16 a[244:247], v[68:71], v[124:127], a[244:247]
v_mfma_f32_16x16x32_bf16 a[224:227], v[64:67], v[120:123], a[224:227]
v_mfma_f32_16x16x32_bf16 a[240:243], v[64:67], v[124:127], a[240:243]
L_l2%=_next:
s_waitcnt lgkmcnt(2)
v_mfma_f32_16x16x32_bf16 v[64:67], v[96:99], v[192:195], v[16:19]
v_exp_f32 v50, v50
v_mfma_f32_16x16x32_bf16 v[64:67], v[100:103], v[196:199], v[64:67]
ds_read_b128 v[112:115], v12 offset:35072
s_add_u32 m0, s46, 34816
v_mfma_f32_16x16x32_bf16 v[68:71], v[96:99], v[224:227], v[20:23]
buffer_load_dwordx4 v4, s[40:43], s44 offen offset:0 lds
v_mfma_f32_16x16x32_bf16 v[68:71], v[100:103], v[228:231], v[68:71]
ds_read_b128 v[116:119], v12 offset:35136
v_add_f32 v8, v8, v50
v_mfma_f32_16x16x32_bf16 v[72:75], v[96:99], v[128:131], v[24:27]
v_exp_f32 v51, v51
v_mfma_f32_16x16x32_bf16 v[72:75], v[100:103], v[132:135], v[72:75]
v_exp_f32 v52, v52
v_mfma_f32_16x16x32_bf16 v[76:79], v[96:99], v[160:163], v[28:31]
buffer_load_dwordx4 v5, s[40:43], s44 offen offset:1088 lds
v_mfma_f32_16x16x32_bf16 v[76:79], v[100:103], v[164:167], v[76:79]
ds_read_b128 v[120:123], v12 offset:35200
s_waitcnt lgkmcnt(3)
v_mfma_f32_16x16x32_bf16 v[64:67], v[104:107], v[200:203], v[64:67]
ds_read_b128 v[124:127], v12 offset:35264
v_add_f32 v8, v8, v51
v_mfma_f32_16x16x32_bf16 v[64:67], v[108:111], v[204:207], v[64:67]
v_exp_f32 v53, v53
v_mfma_f32_16x16x32_bf16 v[68:71], v[104:107], v[232:235], v[68:71]
v_add_f32 v9, v9, v52
v_add_f32 v9, v9, v53
v_mfma_f32_16x16x32_bf16 v[68:71], v[108:111], v[236:239], v[68:71]
buffer_load_dwordx4 v6, s[40:43], s44 offen offset:2176 lds
v_mfma_f32_16x16x32_bf16 v[72:75], v[104:107], v[136:139], v[72:75]
v_exp_f32 v54, v54
v_mfma_f32_16x16x32_bf16 v[72:75], v[108:111], v[140:143], v[72:75]
v_exp_f32 v55, v55
v_mfma_f32_16x16x32_bf16 v[76:79], v[104:107], v[168:171], v[76:79]
ds_read_b128 v[96:99], v12 offset:43520
ds_read_b128 v[100:103], v12 offset:43584
v_mfma_f32_16x16x32_bf16 v[76:79], v[108:111], v[172:175], v[76:79]
v_add_f32 v9, v9, v54
s_waitcnt lgkmcnt(4)
v_mfma_f32_16x16x32_bf16 v[64:67], v[112:115], v[208:211], v[64:67]
v_exp_f32 v56, v56
v_mfma_f32_16x16x32_bf16 v[64:67], v[116:119], v[212:215], v[64:67]
buffer_load_dwordx4 v7, s[40:43], s44 offen offset:3264 lds
v_mfma_f32_16x16x32_bf16 v[68:71], v[112:115], v[240:243], v[68:71]
ds_read_b128 v[104:107], v12 offset:43648
v_add_f32 v9, v9, v55
v_mfma_f32_16x16x32_bf16 v[68:71], v[116:119], v[244:247], v[68:71]
ds_read_b128 v[108:111], v12 offset:43712
s_add_u32 m0, s46, 39168
v_mfma_f32_16x16x32_bf16 v[72:75], v[112:115], v[144:147], v[72:75]
v_exp_f32 v57, v57
v_mfma_f32_16x16x32_bf16 v[72:75], v[116:119], v[148:151], v[72:75]
v_add_f32 v10, v10, v56
v_add_f32 v10, v10, v57
v_mfma_f32_16x16x32_bf16 v[76:79], v[112:115], v[176:179], v[76:79]
buffer_load_dwordx4 v4, s[40:43], s49 offen offset:0 lds
v_mfma_f32_16x16x32_bf16 v[76:79], v[116:119], v[180:183], v[76:79]
ds_read_b128 v[112:115], v12 offset:43776
s_waitcnt lgkmcnt(5)
v_mfma_f32_16x16x32_bf16 v[64:67], v[120:123], v[216:219], v[64:67]
v_exp_f32 v58, v58
v_mfma_f32_16x16x32_bf16 v[64:67], v[124:127], v[220:223], v[64:67]
v_exp_f32 v59, v59
v_mfma_f32_16x16x32_bf16 v[68:71], v[120:123], v[248:251], v[68:71]
v_exp_f32 v60, v60
v_mfma_f32_16x16x32_bf16 v[68:71], v[124:127], v[252:255], v[68:71]
ds_read_b128 v[116:119], v12 offset:43840
v_add_f32 v10, v10, v58
v_mfma_f32_16x16x32_bf16 v[72:75], v[120:123], v[152:155], v[72:75]
buffer_load_dwordx4 v5, s[40:43], s49 offen offset:1088 lds
v_mfma_f32_16x16x32_bf16 v[72:75], v[124:127], v[156:159], v[72:75]
v_add_f32 v10, v10, v59
v_add_f32 v11, v11, v60
v_mfma_f32_16x16x32_bf16 v[76:79], v[120:123], v[184:187], v[76:79]
v_exp_f32 v61, v61
v_mfma_f32_16x16x32_bf16 v[76:79], v[124:127], v[188:191], v[76:79]
ds_read_b128 v[120:123], v12 offset:43904
s_waitcnt lgkmcnt(5)
v_mfma_f32_16x16x32_bf16 v[80:83], v[96:99], v[192:195], v[16:19]
ds_read_b128 v[124:127], v12 offset:43968
v_add_f32 v11, v11, v61
v_mfma_f32_16x16x32_bf16 v[80:83], v[100:103], v[196:199], v[80:83]
v_exp_f32 v62, v62
v_mfma_f32_16x16x32_bf16 v[84:87], v[96:99], v[224:227], v[20:23]
buffer_load_dwordx4 v6, s[40:43], s49 offen offset:2176 lds
v_mfma_f32_16x16x32_bf16 v[84:87], v[100:103], v[228:231], v[84:87]
v_exp_f32 v63, v63
v_mfma_f32_16x16x32_bf16 v[88:91], v[96:99], v[128:131], v[24:27]
v_add_f32 v11, v11, v62
v_add_f32 v11, v11, v63
v_mfma_f32_16x16x32_bf16 v[88:91], v[100:103], v[132:135], v[88:91]
v_cvt_pk_bf16_f32 v32, v32, v33
v_cvt_pk_bf16_f32 v33, v34, v35
v_mfma_f32_16x16x32_bf16 v[92:95], v[96:99], v[160:163], v[28:31]
v_cvt_pk_bf16_f32 v34, v48, v49
v_cvt_pk_bf16_f32 v35, v50, v51
v_mfma_f32_16x16x32_bf16 v[92:95], v[100:103], v[164:167], v[92:95]
v_cvt_pk_bf16_f32 v36, v36, v37
s_waitcnt lgkmcnt(4)
v_mfma_f32_16x16x32_bf16 v[80:83], v[104:107], v[200:203], v[80:83]
v_cvt_pk_bf16_f32 v37, v38, v39
v_cvt_pk_bf16_f32 v38, v52, v53
v_mfma_f32_16x16x32_bf16 v[80:83], v[108:111], v[204:207], v[80:83]
buffer_load_dwordx4 v7, s[40:43], s49 offen offset:3264 lds
v_mfma_f32_16x16x32_bf16 v[84:87], v[104:107], v[232:235], v[84:87]
v_cvt_pk_bf16_f32 v39, v54, v55
v_cvt_pk_bf16_f32 v40, v40, v41
v_mfma_f32_16x16x32_bf16 v[84:87], v[108:111], v[236:239], v[84:87]
v_cvt_pk_bf16_f32 v41, v42, v43
v_cvt_pk_bf16_f32 v42, v56, v57
v_mfma_f32_16x16x32_bf16 v[88:91], v[104:107], v[136:139], v[88:91]
v_cvt_pk_bf16_f32 v43, v58, v59
v_cvt_pk_bf16_f32 v44, v44, v45
v_mfma_f32_16x16x32_bf16 v[88:91], v[108:111], v[140:143], v[88:91]
v_cvt_pk_bf16_f32 v45, v46, v47
v_cvt_pk_bf16_f32 v46, v60, v61
v_mfma_f32_16x16x32_bf16 v[92:95], v[104:107], v[168:171], v[92:95]
v_cvt_pk_bf16_f32 v47, v62, v63
v_max3_f32 v13, v64, v65, v66
v_mfma_f32_16x16x32_bf16 v[92:95], v[108:111], v[172:175], v[92:95]
s_waitcnt vmcnt(16) lgkmcnt(0)
s_barrier
v_mfma_f32_16x16x32_bf16 v[80:83], v[112:115], v[208:211], v[80:83]
ds_read_b64_tr_b16 v[96:97], v2 offset:0
ds_read_b64_tr_b16 v[98:99], v2 offset:8704
v_mfma_f32_16x16x32_bf16 v[80:83], v[116:119], v[212:215], v[80:83]
ds_read_b64_tr_b16 v[100:101], v3 offset:0
v_max3_f32 v14, v67, v68, v69
v_mfma_f32_16x16x32_bf16 v[84:87], v[112:115], v[240:243], v[84:87]
ds_read_b64_tr_b16 v[102:103], v3 offset:8704
ds_read_b64_tr_b16 v[104:105], v2 offset:64
v_mfma_f32_16x16x32_bf16 v[84:87], v[116:119], v[244:247], v[84:87]
ds_read_b64_tr_b16 v[106:107], v2 offset:8768
v_max3_f32 v13, v13, v70, v71
v_mfma_f32_16x16x32_bf16 v[88:91], v[112:115], v[144:147], v[88:91]
ds_read_b64_tr_b16 v[108:109], v3 offset:64
s_add_u32 m0, s45, 34816
v_mfma_f32_16x16x32_bf16 v[88:91], v[116:119], v[148:151], v[88:91]
buffer_load_dwordx4 v4, s[36:39], s44 offen offset:0 lds
v_mfma_f32_16x16x32_bf16 v[92:95], v[112:115], v[176:179], v[92:95]
ds_read_b64_tr_b16 v[110:111], v3 offset:8768
v_max3_f32 v14, v14, v72, v73
v_mfma_f32_16x16x32_bf16 v[92:95], v[116:119], v[180:183], v[92:95]
v_max3_f32 v13, v13, v74, v75
v_max3_f32 v14, v14, v76, v77
v_mfma_f32_16x16x32_bf16 v[80:83], v[120:123], v[216:219], v[80:83]
ds_read_b64_tr_b16 v[112:113], v2 offset:128
v_max3_f32 v13, v13, v78, v79
v_mfma_f32_16x16x32_bf16 v[80:83], v[124:127], v[220:223], v[80:83]
ds_read_b64_tr_b16 v[114:115], v2 offset:8832
ds_read_b64_tr_b16 v[116:117], v3 offset:128
v_mfma_f32_16x16x32_bf16 v[84:87], v[120:123], v[248:251], v[84:87]
ds_read_b64_tr_b16 v[118:119], v3 offset:8832
v_mfma_f32_16x16x32_bf16 v[84:87], v[124:127], v[252:255], v[84:87]
v_mfma_f32_16x16x32_bf16 v[88:91], v[120:123], v[152:155], v[88:91]
buffer_load_dwordx4 v5, s[36:39], s44 offen offset:1088 lds
v_mfma_f32_16x16x32_bf16 v[88:91], v[124:127], v[156:159], v[88:91]
v_mfma_f32_16x16x32_bf16 v[92:95], v[120:123], v[184:187], v[92:95]
v_mfma_f32_16x16x32_bf16 v[92:95], v[124:127], v[188:191], v[92:95]
ds_read_b64_tr_b16 v[120:121], v2 offset:192
s_waitcnt lgkmcnt(9)
v_mfma_f32_16x16x32_bf16 a[0:3], v[32:35], v[96:99], a[0:3]
ds_read_b64_tr_b16 v[122:123], v2 offset:8896
ds_read_b64_tr_b16 v[124:125], v3 offset:192
v_mfma_f32_16x16x32_bf16 a[16:19], v[32:35], v[100:103], a[16:19]
ds_read_b64_tr_b16 v[126:127], v3 offset:8896
v_mfma_f32_16x16x32_bf16 a[4:7], v[36:39], v[96:99], a[4:7]
v_max3_f32 v14, v14, v80, v81
v_max3_f32 v13, v13, v82, v83
v_mfma_f32_16x16x32_bf16 a[20:23], v[36:39], v[100:103], a[20:23]
v_max3_f32 v14, v14, v84, v85
v_max3_f32 v13, v13, v86, v87
v_mfma_f32_16x16x32_bf16 a[8:11], v[40:43], v[96:99], a[8:11]
buffer_load_dwordx4 v6, s[36:39], s44 offen offset:2176 lds
v_mfma_f32_16x16x32_bf16 a[24:27], v[40:43], v[100:103], a[24:27]
v_max3_f32 v14, v14, v88, v89
v_max3_f32 v13, v13, v90, v91
v_mfma_f32_16x16x32_bf16 a[12:15], v[44:47], v[96:99], a[12:15]
v_max3_f32 v14, v14, v92, v93
v_max3_f32 v13, v13, v94, v95
v_mfma_f32_16x16x32_bf16 a[28:31], v[44:47], v[100:103], a[28:31]
v_max_f32 v13, v13, v14
s_waitcnt lgkmcnt(8)
v_mfma_f32_16x16x32_bf16 a[44:47], v[44:47], v[104:107], a[44:47]
ds_read_b64_tr_b16 v[96:97], v2 offset:256
v_cmp_lt_f32 vcc, s48, v13
v_mfma_f32_16x16x32_bf16 a[60:63], v[44:47], v[108:111], a[60:63]
ds_read_b64_tr_b16 v[98:99], v2 offset:8960
ds_read_b64_tr_b16 v[100:101], v3 offset:256
v_mfma_f32_16x16x32_bf16 a[40:43], v[40:43], v[104:107], a[40:43]
s_cbranch_vccnz L_l3%=_rare
ds_read_b64_tr_b16 v[102:103], v3 offset:8960
v_mfma_f32_16x16x32_bf16 a[56:59], v[40:43], v[108:111], a[56:59]
v_exp_f32 v64, v64
v_mfma_f32_16x16x32_bf16 a[36:39], v[36:39], v[104:107], a[36:39]
buffer_load_dwordx4 v7, s[36:39], s44 offen offset:3264 lds
v_mfma_f32_16x16x32_bf16 a[52:55], v[36:39], v[108:111], a[52:55]
v_exp_f32 v65, v65
v_mfma_f32_16x16x32_bf16 a[32:35], v[32:35], v[104:107], a[32:35]
v_exp_f32 v66, v66
v_mfma_f32_16x16x32_bf16 a[48:51], v[32:35], v[108:111], a[48:51]
v_add_f32 v8, v8, v64
s_waitcnt lgkmcnt(8)
v_mfma_f32_16x16x32_bf16 a[64:67], v[32:35], v[112:115], a[64:67]
v_exp_f32 v67, v67
v_mfma_f32_16x16x32_bf16 a[80:83], v[32:35], v[116:119], a[80:83]
s_add_u32 m0, s45, 39168
v_add_f32 v8, v8, v65
v_mfma_f32_16x16x32_bf16 a[68:71], v[36:39], v[112:115], a[68:71]
v_exp_f32 v68, v68
v_mfma_f32_16x16x32_bf16 a[84:87], v[36:39], v[116:119], a[84:87]
v_add_f32 v8, v8, v66
v_add_f32 v8, v8, v67
v_mfma_f32_16x16x32_bf16 a[72:75], v[40:43], v[112:115], a[72:75]
buffer_load_dwordx4 v4, s[36:39], s49 offen offset:0 lds
v_mfma_f32_16x16x32_bf16 a[88:91], v[40:43], v[116:119], a[88:91]
v_exp_f32 v69, v69
v_mfma_f32_16x16x32_bf16 a[76:79], v[44:47], v[112:115], a[76:79]
v_exp_f32 v70, v70
v_mfma_f32_16x16x32_bf16 a[92:95], v[44:47], v[116:119], a[92:95]
ds_read_b64_tr_b16 v[104:105], v2 offset:320
s_waitcnt lgkmcnt(5)
v_mfma_f32_16x16x32_bf16 a[108:111], v[44:47], v[120:123], a[108:111]
ds_read_b64_tr_b16 v[106:107], v2 offset:9024
ds_read_b64_tr_b16 v[108:109], v3 offset:320
v_mfma_f32_16x16x32_bf16 a[124:127], v[44:47], v[124:127], a[124:127]
ds_read_b64_tr_b16 v[110:111], v3 offset:9024
v_add_f32 v9, v9, v68
v_mfma_f32_16x16x32_bf16 a[104:107], v[40:43], v[120:123], a[104:107]
v_exp_f32 v71, v71
v_mfma_f32_16x16x32_bf16 a[120:123], v[40:43], v[124:127], a[120:123]
v_add_f32 v9, v9, v69
v_add_f32 v9, v9, v70
v_mfma_f32_16x16x32_bf16 a[100:103], v[36:39], v[120:123], a[100:103]
buffer_load_dwordx4 v5, s[36:39], s49 offen offset:1088 lds
v_mfma_f32_16x16x32_bf16 a[116:119], v[36:39], v[124:127], a[116:119]
v_exp_f32 v72, v72
v_mfma_f32_16x16x32_bf16 a[96:99], v[32:35], v[120:123], a[96:99]
v_exp_f32 v73, v73
v_mfma_f32_16x16x32_bf16 a[112:115], v[32:35], v[124:127], a[112:115]
ds_read_b64_tr_b16 v[112:113], v2 offset:384
s_waitcnt lgkmcnt(5)
v_mfma_f32_16x16x32_bf16 a[128:131], v[32:35], v[96:99], a[128:131]
ds_read_b64_tr_b16 v[114:115], v2 offset:9088
ds_read_b64_tr_b16 v[116:117], v3 offset:384
v_mfma_f32_16x16x32_bf16 a[144:147], v[32:35], v[100:103], a[144:147]
ds_read_b64_tr_b16 v[118:119], v3 offset:9088
v_add_f32 v9, v9, v71
v_mfma_f32_16x16x32_bf16 a[132:135], v[36:39], v[96:99], a[132:135]
ds_read_b64_tr_b16 v[120:121], v2 offset:448
ds_read_b64_tr_b16 v[122:123], v2 offset:9152
v_mfma_f32_16x16x32_bf16 a[148:151], v[36:39], v[100:103], a[148:151]
ds_read_b64_tr_b16 v[124:125], v3 offset:448
v_add_f32 v10, v10, v72
v_mfma_f32_16x16x32_bf16 a[136:139], v[40:43], v[96:99], a[136:139]
buffer_load_dwordx4 v6, s[36:39], s49 offen offset:2176 lds
v_mfma_f32_16x16x32_bf16 a[152:155], v[40:43], v[100:103], a[152:155]
ds_read_b64_tr_b16 v[126:127], v3 offset:9152
v_add_f32 v10, v10, v73
v_mfma_f32_16x16x32_bf16 a[140:143], v[44:47], v[96:99], a[140:143]
v_exp_f32 v74, v74
v_mfma_f32_16x16x32_bf16 a[156:159], v[44:47], v[100:103], a[156:159]
v_add_f32 v10, v10, v74
s_waitcnt lgkmcnt(8)
v_mfma_f32_16x16x32_bf16 a[172:175], v[44:47], v[104:107], a[172:175]
v_exp_f32 v75, v75
v_mfma_f32_16x16x32_bf16 a[188:191], v[44:47], v[108:111], a[188:191]
v_exp_f32 v76, v76
v_mfma_f32_16x16x32_bf16 a[168:171], v[40:43], v[104:107], a[168:171]
buffer_load_dwordx4 v7, s[36:39], s49 offen offset:3264 lds
v_mfma_f32_16x16x32_bf16 a[184:187], v[40:43], v[108:111], a[184:187]
v_exp_f32 v77, v77
v_mfma_f32_16x16x32_bf16 a[164:167], v[36:39], v[104:107], a[164:167]
v_add_f32 v10, v10, v75
v_add_f32 v11, v11, v76
v_mfma_f32_16x16x32_bf16 a[180:183], v[36:39], v[108:111], a[180:183]
v_exp_f32 v78, v78
v_mfma_f32_16x16x32_bf16 a[160:163], v[32:35], v[104:107], a[160:163]
v_exp_f32 v79, v79
v_mfma_f32_16x16x32_bf16 a[176:179], v[32:35], v[108:111], a[176:179]
s_waitcnt vmcnt(16) lgkmcnt(0)
s_barrier
v_mfma_f32_16x16x32_bf16 a[192:195], v[32:35], v[112:115], a[192:195]
ds_read_b128 v[96:99], v12 offset:0
v_add_f32 v11, v11, v77
v_mfma_f32_16x16x32_bf16 a[208:211], v[32:35], v[116:119], a[208:211]
ds_read_b128 v[100:103], v12 offset:64
v_add_f32 v11, v11, v78
v_mfma_f32_16x16x32_bf16 a[196:199], v[36:39], v[112:115], a[196:199]
v_exp_f32 v80, v80
v_mfma_f32_16x16x32_bf16 a[212:215], v[36:39], v[116:119], a[212:215]
v_exp_f32 v81, v81
v_mfma_f32_16x16x32_bf16 a[200:203], v[40:43], v[112:115], a[200:203]
v_add_f32 v11, v11, v79
s_add_u32 s44, s44, 32768
v_mfma_f32_16x16x32_bf16 a[216:219], v[40:43], v[116:119], a[216:219]
s_add_u32 s49, s49, 32768
v_add_f32 v8, v8, v80
v_mfma_f32_16x16x32_bf16 a[204:207], v[44:47], v[112:115], a[204:207]
ds_read_b128 v[104:107], v12 offset:128
ds_read_b128 v[108:111], v12 offset:192
v_mfma_f32_16x16x32_bf16 a[220:223], v[44:47], v[116:119], a[220:223]
v_add_f32 v8, v8, v81
v_mfma_f32_16x16x32_bf16 a[236:239], v[44:47], v[120:123], a[236:239]
v_mfma_f32_16x16x32_bf16 a[252:255], v[44:47], v[124:127], a[252:255]
v_mfma_f32_16x16x32_bf16 a[232:235], v[40:43], v[120:123], a[232:235]
v_mfma_f32_16x16x32_bf16 a[248:251], v[40:43], v[124:127], a[248:251]
v_mfma_f32_16x16x32_bf16 a[228:231], v[36:39], v[120:123], a[228:231]
v_mfma_f32_16x16x32_bf16 a[244:247], v[36:39], v[124:127], a[244:247]
v_mfma_f32_16x16x32_bf16 a[224:227], v[32:35], v[120:123], a[224:227]
s_sub_u32 s47, s47, 1
s_cmp_lg_u32 s47, 0
v_mfma_f32_16x16x32_bf16 a[240:243], v[32:35], v[124:127], a[240:243]
L_l3%=_next:
s_cbranch_scc1 L_loop%=
s_waitcnt lgkmcnt(2)
v_mfma_f32_16x16x32_bf16 v[32:35], v[96:99], v[192:195], v[16:19]
v_exp_f32 v82, v82
v_mfma_f32_16x16x32_bf16 v[32:35], v[100:103], v[196:199], v[32:35]
ds_read_b128 v[112:115], v12 offset:256
s_add_u32 m0, s46, 0
v_mfma_f32_16x16x32_bf16 v[36:39], v[96:99], v[224:227], v[20:23]
buffer_load_dwordx4 v4, s[40:43], s44 offen offset:0 lds
v_mfma_f32_16x16x32_bf16 v[36:39], v[100:103], v[228:231], v[36:39]
ds_read_b128 v[116:119], v12 offset:320
v_add_f32 v8, v8, v82
v_mfma_f32_16x16x32_bf16 v[40:43], v[96:99], v[128:131], v[24:27]
v_exp_f32 v83, v83
v_mfma_f32_16x16x32_bf16 v[40:43], v[100:103], v[132:135], v[40:43]
v_exp_f32 v84, v84
v_mfma_f32_16x16x32_bf16 v[44:47], v[96:99], v[160:163], v[28:31]
buffer_load_dwordx4 v5, s[40:43], s44 offen offset:1088 lds
v_mfma_f32_16x16x32_bf16 v[44:47], v[100:103], v[164:167], v[44:47]
ds_read_b128 v[120:123], v12 offset:384
s_waitcnt lgkmcnt(3)
v_mfma_f32_16x16x32_bf16 v[32:35], v[104:107], v[200:203], v[32:35]
ds_read_b128 v[124:127], v12 offset:448
v_add_f32 v8, v8, v83
v_mfma_f32_16x16x32_bf16 v[32:35], v[108:111], v[204:207], v[32:35]
v_exp_f32 v85, v85
v_mfma_f32_16x16x32_bf16 v[36:39], v[104:107], v[232:235], v[36:39]
v_add_f32 v9, v9, v84
v_add_f32 v9, v9, v85
v_mfma_f32_16x16x32_bf16 v[36:39], v[108:111], v[236:239], v[36:39]
buffer_load_dwordx4 v6, s[40:43], s44 offen offset:2176 lds
v_mfma_f32_16x16x32_bf16 v[40:43], v[104:107], v[136:139], v[40:43]
v_exp_f32 v86, v86
v_mfma_f32_16x16x32_bf16 v[40:43], v[108:111], v[140:143], v[40:43]
v_exp_f32 v87, v87
v_mfma_f32_16x16x32_bf16 v[44:47], v[104:107], v[168:171], v[44:47]
ds_read_b128 v[96:99], v12 offset:8704
ds_read_b128 v[100:103], v12 offset:8768
v_mfma_f32_16x16x32_bf16 v[44:47], v[108:111], v[172:175], v[44:47]
v_add_f32 v9, v9, v86
s_waitcnt lgkmcnt(4)
v_mfma_f32_16x16x32_bf16 v[32:35], v[112:115], v[208:211], v[32:35]
v_exp_f32 v88, v88
v_mfma_f32_16x16x32_bf16 v[32:35], v[116:119], v[212:215], v[32:35]
buffer_load_dwordx4 v7, s[40:43], s44 offen offset:3264 lds
v_mfma_f32_16x16x32_bf16 v[36:39], v[112:115], v[240:243], v[36:39]
ds_read_b128 v[104:107], v12 offset:8832
v_add_f32 v9, v9, v87
v_mfma_f32_16x16x32_bf16 v[36:39], v[116:119], v[244:247], v[36:39]
ds_read_b128 v[108:111], v12 offset:8896
s_add_u32 m0, s46, 4352
v_mfma_f32_16x16x32_bf16 v[40:43], v[112:115], v[144:147], v[40:43]
v_exp_f32 v89, v89
v_mfma_f32_16x16x32_bf16 v[40:43], v[116:119], v[148:151], v[40:43]
v_add_f32 v10, v10, v88
v_add_f32 v10, v10, v89
v_mfma_f32_16x16x32_bf16 v[44:47], v[112:115], v[176:179], v[44:47]
buffer_load_dwordx4 v4, s[40:43], s49 offen offset:0 lds
v_mfma_f32_16x16x32_bf16 v[44:47], v[116:119], v[180:183], v[44:47]
ds_read_b128 v[112:115], v12 offset:8960
s_waitcnt lgkmcnt(5)
v_mfma_f32_16x16x32_bf16 v[32:35], v[120:123], v[216:219], v[32:35]
v_exp_f32 v90, v90
v_mfma_f32_16x16x32_bf16 v[32:35], v[124:127], v[220:223], v[32:35]
v_exp_f32 v91, v91
v_mfma_f32_16x16x32_bf16 v[36:39], v[120:123], v[248:251], v[36:39]
v_exp_f32 v92, v92
v_mfma_f32_16x16x32_bf16 v[36:39], v[124:127], v[252:255], v[36:39]
ds_read_b128 v[116:119], v12 offset:9024
v_add_f32 v10, v10, v90
v_mfma_f32_16x16x32_bf16 v[40:43], v[120:123], v[152:155], v[40:43]
buffer_load_dwordx4 v5, s[40:43], s49 offen offset:1088 lds
v_mfma_f32_16x16x32_bf16 v[40:43], v[124:127], v[156:159], v[40:43]
v_add_f32 v10, v10, v91
v_add_f32 v11, v11, v92
v_mfma_f32_16x16x32_bf16 v[44:47], v[120:123], v[184:187], v[44:47]
v_exp_f32 v93, v93
v_mfma_f32_16x16x32_bf16 v[44:47], v[124:127], v[188:191], v[44:47]
ds_read_b128 v[120:123], v12 offset:9088
s_waitcnt lgkmcnt(5)
v_mfma_f32_16x16x32_bf16 v[48:51], v[96:99], v[192:195], v[16:19]
ds_read_b128 v[124:127], v12 offset:9152
v_add_f32 v11, v11, v93
v_mfma_f32_16x16x32_bf16 v[48:51], v[100:103], v[196:199], v[48:51]
v_exp_f32 v94, v94
v_mfma_f32_16x16x32_bf16 v[52:55], v[96:99], v[224:227], v[20:23]
buffer_load_dwordx4 v6, s[40:43], s49 offen offset:2176 lds
v_mfma_f32_16x16x32_bf16 v[52:55], v[100:103], v[228:231], v[52:55]
v_exp_f32 v95, v95
v_mfma_f32_16x16x32_bf16 v[56:59], v[96:99], v[128:131], v[24:27]
v_add_f32 v11, v11, v94
v_add_f32 v11, v11, v95
v_mfma_f32_16x16x32_bf16 v[56:59], v[100:103], v[132:135], v[56:59]
v_cvt_pk_bf16_f32 v64, v64, v65
v_cvt_pk_bf16_f32 v65, v66, v67
v_mfma_f32_16x16x32_bf16 v[60:63], v[96:99], v[160:163], v[28:31]
v_cvt_pk_bf16_f32 v66, v80, v81
v_cvt_pk_bf16_f32 v67, v82, v83
v_mfma_f32_16x16x32_bf16 v[60:63], v[100:103], v[164:167], v[60:63]
v_cvt_pk_bf16_f32 v68, v68, v69
s_waitcnt lgkmcnt(4)
v_mfma_f32_16x16x32_bf16 v[48:51], v[104:107], v[200:203], v[48:51]
v_cvt_pk_bf16_f32 v69, v70, v71
v_cvt_pk_bf16_f32 v70, v84, v85
v_mfma_f32_16x16x32_bf16 v[48:51], v[108:111], v[204:207], v[48:51]
buffer_load_dwordx4 v7, s[40:43], s49 offen offset:3264 lds
v_mfma_f32_16x16x32_bf16 v[52:55], v[104:107], v[232:235], v[52:55]
v_cvt_pk_bf16_f32 v71, v86, v87
v_cvt_pk_bf16_f32 v72, v72, v73
v_mfma_f32_16x16x32_bf16 v[52:55], v[108:111], v[236:239], v[52:55]
v_cvt_pk_bf16_f32 v73, v74, v75
v_cvt_pk_bf16_f32 v74, v88, v89
v_mfma_f32_16x16x32_bf16 v[56:59], v[104:107], v[136:139], v[56:59]
v_cvt_pk_bf16_f32 v75, v90, v91
v_lshlrev_b32 v0, 3, v1
v_mfma_f32_16x16x32_bf16 v[56:59], v[108:111], v[140:143], v[56:59]
v_cvt_pk_bf16_f32 v76, v76, v77
v_cvt_pk_bf16_f32 v77, v78, v79
v_mfma_f32_16x16x32_bf16 v[60:63], v[104:107], v[168:171], v[60:63]
v_cvt_pk_bf16_f32 v78, v92, v93
v_cvt_pk_bf16_f32 v79, v94, v95
v_mfma_f32_16x16x32_bf16 v[60:63], v[108:111], v[172:175], v[60:63]
s_waitcnt vmcnt(16) lgkmcnt(0)
s_barrier
v_mfma_f32_16x16x32_bf16 v[48:51], v[112:115], v[208:211], v[48:51]
ds_read_b64_tr_b16 v[96:97], v2 offset:34816
ds_read_b64_tr_b16 v[98:99], v2 offset:43520
v_mfma_f32_16x16x32_bf16 v[48:51], v[116:119], v[212:215], v[48:51]
ds_read_b64_tr_b16 v[100:101], v3 offset:34816
v_max3_f32 v13, v32, v33, v34
v_mfma_f32_16x16x32_bf16 v[52:55], v[112:115], v[240:243], v[52:55]
ds_read_b64_tr_b16 v[102:103], v3 offset:43520
ds_read_b64_tr_b16 v[104:105], v2 offset:34880
v_mfma_f32_16x16x32_bf16 v[52:55], v[116:119], v[244:247], v[52:55]
ds_read_b64_tr_b16 v[106:107], v2 offset:43584
v_max3_f32 v14, v35, v36, v37
v_mfma_f32_16x16x32_bf16 v[56:59], v[112:115], v[144:147], v[56:59]
ds_read_b64_tr_b16 v[108:109], v3 offset:34880
ds_read_b64_tr_b16 v[110:111], v3 offset:43584
v_mfma_f32_16x16x32_bf16 v[56:59], v[116:119], v[148:151], v[56:59]
v_max3_f32 v13, v13, v38, v39
v_max3_f32 v14, v14, v40, v41
v_mfma_f32_16x16x32_bf16 v[60:63], v[112:115], v[176:179], v[60:63]
v_max3_f32 v13, v13, v42, v43
v_max3_f32 v14, v14, v44, v45
v_mfma_f32_16x16x32_bf16 v[60:63], v[116:119], v[180:183], v[60:63]
ds_read_b64_tr_b16 v[112:113], v2 offset:34944
v_max3_f32 v13, v13, v46, v47
v_mfma_f32_16x16x32_bf16 v[48:51], v[120:123], v[216:219], v[48:51]
ds_read_b64_tr_b16 v[114:115], v2 offset:43648
ds_read_b64_tr_b16 v[116:117], v3 offset:34944
v_mfma_f32_16x16x32_bf16 v[48:51], v[124:127], v[220:223], v[48:51]
ds_read_b64_tr_b16 v[118:119], v3 offset:43648
v_mfma_f32_16x16x32_bf16 v[52:55], v[120:123], v[248:251], v[52:55]
v_mfma_f32_16x16x32_bf16 v[52:55], v[124:127], v[252:255], v[52:55]
v_mfma_f32_16x16x32_bf16 v[56:59], v[120:123], v[152:155], v[56:59]
s_getpc_b64 s[12:13]
L_pcctail%=:
s_add_u32 s12, s12, L_ctail%=-L_pcctail%=
s_addc_u32 s13, s13, 0
s_and_b32 s13, s13, 0xffff
s_mov_b32 s14, L_end%=-L_ctail%=
s_mov_b32 s15, 131072
s_lshl_b32 s53, s35, 2
s_add_u32 s53, s53, s80
s_lshl_b32 s53, s53, 13
s_min_u32 s53, s53, 1048576
s_mov_b32 m0, 159744
v_mfma_f32_16x16x32_bf16 v[56:59], v[124:127], v[156:159], v[56:59]
s_mov_b32 s54, 131072
v_mfma_f32_16x16x32_bf16 v[60:63], v[120:123], v[184:187], v[60:63]
v_mfma_f32_16x16x32_bf16 v[60:63], v[124:127], v[188:191], v[60:63]
ds_read_b64_tr_b16 v[120:121], v2 offset:35008
s_waitcnt lgkmcnt(9)
v_mfma_f32_16x16x32_bf16 a[0:3], v[64:67], v[96:99], a[0:3]
buffer_load_dwordx4 v[88:91], v1, s[60:63], s54 offen offset:0 sc1
v_mfma_f32_16x16x32_bf16 a[16:19], v[64:67], v[100:103], a[16:19]
ds_read_b64_tr_b16 v[122:123], v2 offset:43712
ds_read_b64_tr_b16 v[124:125], v3 offset:35008
v_mfma_f32_16x16x32_bf16 a[4:7], v[68:71], v[96:99], a[4:7]
buffer_load_dwordx4 v[92:95], v1, s[60:63], s54 offen offset:1024 sc1
v_mfma_f32_16x16x32_bf16 a[20:23], v[68:71], v[100:103], a[20:23]
v_max3_f32 v14, v14, v48, v49
v_max3_f32 v13, v13, v50, v51
v_mfma_f32_16x16x32_bf16 a[8:11], v[72:75], v[96:99], a[8:11]
v_max3_f32 v14, v14, v52, v53
v_max3_f32 v13, v13, v54, v55
v_mfma_f32_16x16x32_bf16 a[24:27], v[72:75], v[100:103], a[24:27]
v_max3_f32 v14, v14, v56, v57
v_max3_f32 v13, v13, v58, v59
v_mfma_f32_16x16x32_bf16 a[12:15], v[76:79], v[96:99], a[12:15]
buffer_load_dword v0, s[60:63], s2 offen sc1 lds
v_mfma_f32_16x16x32_bf16 a[28:31], v[76:79], v[100:103], a[28:31]
v_max3_f32 v14, v14, v60, v61
s_waitcnt lgkmcnt(7)
v_mfma_f32_16x16x32_bf16 a[44:47], v[76:79], v[104:107], a[44:47]
v_max3_f32 v13, v13, v62, v63
v_max_f32 v13, v13, v14
v_mfma_f32_16x16x32_bf16 a[60:63], v[76:79], v[108:111], a[60:63]
ds_read_b64_tr_b16 v[126:127], v3 offset:43712
v_cmp_lt_f32 vcc, s48, v13
v_mfma_f32_16x16x32_bf16 a[40:43], v[72:75], v[104:107], a[40:43]
ds_read_b64_tr_b16 v[96:97], v2 offset:35072
ds_read_b64_tr_b16 v[98:99], v2 offset:43776
v_mfma_f32_16x16x32_bf16 a[56:59], v[72:75], v[108:111], a[56:59]
s_cbranch_vccnz L_p30%=_rare
ds_read_b64_tr_b16 v[100:101], v3 offset:35072
v_mfma_f32_16x16x32_bf16 a[36:39], v[68:71], v[104:107], a[36:39]
buffer_load_dword v0, s[12:15], s53 offen sc1 lds
v_mfma_f32_16x16x32_bf16 a[52:55], v[68:71], v[108:111], a[52:55]
v_exp_f32 v32, v32
v_mfma_f32_16x16x32_bf16 a[32:35], v[64:67], v[104:107], a[32:35]
v_exp_f32 v33, v33
v_mfma_f32_16x16x32_bf16 a[48:51], v[64:67], v[108:111], a[48:51]
ds_read_b64_tr_b16 v[102:103], v3 offset:43776
s_waitcnt lgkmcnt(8)
v_mfma_f32_16x16x32_bf16 a[64:67], v[64:67], v[112:115], a[64:67]
v_exp_f32 v34, v34
v_mfma_f32_16x16x32_bf16 a[80:83], v[64:67], v[116:119], a[80:83]
v_add_f32 v8, v8, v32
v_add_f32 v8, v8, v33
v_mfma_f32_16x16x32_bf16 a[68:71], v[68:71], v[112:115], a[68:71]
v_exp_f32 v35, v35
v_mfma_f32_16x16x32_bf16 a[84:87], v[68:71], v[116:119], a[84:87]
v_exp_f32 v36, v36
v_mfma_f32_16x16x32_bf16 a[72:75], v[72:75], v[112:115], a[72:75]
v_add_f32 v8, v8, v34
v_add_f32 v8, v8, v35
v_mfma_f32_16x16x32_bf16 a[88:91], v[72:75], v[116:119], a[88:91]
v_exp_f32 v37, v37
v_mfma_f32_16x16x32_bf16 a[76:79], v[76:79], v[112:115], a[76:79]
v_exp_f32 v38, v38
v_mfma_f32_16x16x32_bf16 a[92:95], v[76:79], v[116:119], a[92:95]
ds_read_b64_tr_b16 v[104:105], v2 offset:35136
s_waitcnt lgkmcnt(5)
v_mfma_f32_16x16x32_bf16 a[108:111], v[76:79], v[120:123], a[108:111]
ds_read_b64_tr_b16 v[106:107], v2 offset:43840
ds_read_b64_tr_b16 v[108:109], v3 offset:35136
v_mfma_f32_16x16x32_bf16 a[124:127], v[76:79], v[124:127], a[124:127]
ds_read_b64_tr_b16 v[110:111], v3 offset:43840
v_add_f32 v9, v9, v36
v_mfma_f32_16x16x32_bf16 a[104:107], v[72:75], v[120:123], a[104:107]
v_exp_f32 v39, v39
v_mfma_f32_16x16x32_bf16 a[120:123], v[72:75], v[124:127], a[120:123]
v_add_f32 v9, v9, v37
v_add_f32 v9, v9, v38
v_mfma_f32_16x16x32_bf16 a[100:103], v[68:71], v[120:123], a[100:103]
v_exp_f32 v40, v40
v_mfma_f32_16x16x32_bf16 a[116:119], v[68:71], v[124:127], a[116:119]
v_exp_f32 v41, v41
v_mfma_f32_16x16x32_bf16 a[96:99], v[64:67], v[120:123], a[96:99]
ds_read_b64_tr_b16 v[112:113], v2 offset:35200
ds_read_b64_tr_b16 v[114:115], v2 offset:43904
v_mfma_f32_16x16x32_bf16 a[112:115], v[64:67], v[124:127], a[112:115]
ds_read_b64_tr_b16 v[116:117], v3 offset:35200
s_waitcnt lgkmcnt(7)
v_mfma_f32_16x16x32_bf16 a[128:131], v[64:67], v[96:99], a[128:131]
ds_read_b64_tr_b16 v[118:119], v3 offset:43904
ds_read_b64_tr_b16 v[120:121], v2 offset:35264
v_mfma_f32_16x16x32_bf16 a[144:147], v[64:67], v[100:103], a[144:147]
ds_read_b64_tr_b16 v[122:123], v2 offset:43968
v_add_f32 v9, v9, v39
v_mfma_f32_16x16x32_bf16 a[132:135], v[68:71], v[96:99], a[132:135]
ds_read_b64_tr_b16 v[124:125], v3 offset:35264
ds_read_b64_tr_b16 v[126:127], v3 offset:43968
v_mfma_f32_16x16x32_bf16 a[148:151], v[68:71], v[100:103], a[148:151]
v_exp_f32 v42, v42
v_mfma_f32_16x16x32_bf16 a[136:139], v[72:75], v[96:99], a[136:139]
v_add_f32 v10, v10, v40
v_add_f32 v10, v10, v41
v_mfma_f32_16x16x32_bf16 a[152:155], v[72:75], v[100:103], a[152:155]
v_exp_f32 v43, v43
v_mfma_f32_16x16x32_bf16 a[140:143], v[76:79], v[96:99], a[140:143]
s_waitcnt vmcnt(2)
v_xor_b32 v88, s76, v88
v_mfma_f32_16x16x32_bf16 a[156:159], v[76:79], v[100:103], a[156:159]
v_xor_b32 v89, s77, v89
s_waitcnt lgkmcnt(8)
v_mfma_f32_16x16x32_bf16 a[172:175], v[76:79], v[104:107], a[172:175]
v_xor_b32 v90, s76, v90
v_xor_b32 v91, s77, v91
v_mfma_f32_16x16x32_bf16 a[188:191], v[76:79], v[108:111], a[188:191]
v_xor_b32 v92, s76, v92
v_xor_b32 v93, s77, v93
v_mfma_f32_16x16x32_bf16 a[168:171], v[72:75], v[104:107], a[168:171]
v_xor_b32 v94, s76, v94
v_xor_b32 v95, s77, v95
v_mfma_f32_16x16x32_bf16 a[184:187], v[72:75], v[108:111], a[184:187]
v_or3_b32 v88, v88, v89, v90
v_or3_b32 v92, v92, v93, v94
v_mfma_f32_16x16x32_bf16 a[164:167], v[68:71], v[104:107], a[164:167]
v_exp_f32 v44, v44
v_mfma_f32_16x16x32_bf16 a[180:183], v[68:71], v[108:111], a[180:183]
v_or3_b32 v88, v88, v91, v95
v_or_b32 v88, v88, v92
v_mfma_f32_16x16x32_bf16 a[160:163], v[64:67], v[104:107], a[160:163]
v_cmp_ne_u32 vcc, 0, v88
v_add_f32 v10, v10, v42
v_mfma_f32_16x16x32_bf16 a[176:179], v[64:67], v[108:111], a[176:179]
s_waitcnt vmcnt(10) lgkmcnt(0)
s_barrier
v_exp_f32 v45, v45
v_mfma_f32_16x16x32_bf16 a[192:195], v[64:67], v[112:115], a[192:195]
v_add_f32 v10, v10, v43
v_add_f32 v11, v11, v44
v_mfma_f32_16x16x32_bf16 a[208:211], v[64:67], v[116:119], a[208:211]
s_cbranch_vccz 1
s_call_b64 s[30:31], L_alr%=
v_mfma_f32_16x16x32_bf16 a[196:199], v[68:71], v[112:115], a[196:199]
v_exp_f32 v46, v46
v_mfma_f32_16x16x32_bf16 a[212:215], v[68:71], v[116:119], a[212:215]
s_add_u32 m0, s10, 0
s_add_u32 s55, s3, 0
v_mfma_f32_16x16x32_bf16 a[200:203], v[72:75], v[112:115], a[200:203]
buffer_load_dwordx4 v1, s[60:63], s55 offen offset:0 sc1 lds
v_exp_f32 v47, v47
v_mfma_f32_16x16x32_bf16 a[216:219], v[72:75], v[116:119], a[216:219]
ds_read_b128 v[96:99], v12 offset:34816
v_add_f32 v11, v11, v45
v_mfma_f32_16x16x32_bf16 a[204:207], v[76:79], v[112:115], a[204:207]
ds_read_b128 v[100:103], v12 offset:34880
buffer_load_dwordx4 v1, s[60:63], s55 offen offset:1024 sc1 lds
v_mfma_f32_16x16x32_bf16 a[220:223], v[76:79], v[116:119], a[220:223]
v_exp_f32 v48, v48
v_mfma_f32_16x16x32_bf16 a[236:239], v[76:79], v[120:123], a[236:239]
buffer_load_dwordx4 v1, s[60:63], s55 offen offset:2048 sc1 lds
v_add_f32 v11, v11, v46
v_mfma_f32_16x16x32_bf16 a[252:255], v[76:79], v[124:127], a[252:255]
v_exp_f32 v49, v49
v_mfma_f32_16x16x32_bf16 a[232:235], v[72:75], v[120:123], a[232:235]
buffer_load_dwordx4 v1, s[60:63], s55 offen offset:3072 sc1 lds
v_add_f32 v11, v11, v47
v_mfma_f32_16x16x32_bf16 a[248:251], v[72:75], v[124:127], a[248:251]
s_add_u32 m0, s11, 16384
s_add_u32 s55, s16, 16384
v_mfma_f32_16x16x32_bf16 a[228:231], v[68:71], v[120:123], a[228:231]
buffer_load_dwordx4 v1, s[60:63], s55 offen offset:0 sc1 lds
v_mfma_f32_16x16x32_bf16 a[244:247], v[68:71], v[124:127], a[244:247]
s_add_u32 s44, s44, 32768
s_add_u32 s49, s49, 32768
v_add_f32 v8, v8, v48
v_mfma_f32_16x16x32_bf16 a[224:227], v[64:67], v[120:123], a[224:227]
ds_read_b128 v[104:107], v12 offset:34944
ds_read_b128 v[108:111], v12 offset:35008
buffer_load_dwordx4 v1, s[60:63], s55 offen offset:1024 sc1 lds
v_add_f32 v8, v8, v49
v_mfma_f32_16x16x32_bf16 a[240:243], v[64:67], v[124:127], a[240:243]
L_ctail%=:
L_p30%=_next:
s_waitcnt lgkmcnt(2)
v_mfma_f32_16x16x32_bf16 v[64:67], v[96:99], v[192:195], v[16:19]
v_exp_f32 v50, v50
v_mfma_f32_16x16x32_bf16 v[64:67], v[100:103], v[196:199], v[64:67]
ds_read_b128 v[112:115], v12 offset:35072
s_add_u32 m0, s46, 34816
v_mfma_f32_16x16x32_bf16 v[68:71], v[96:99], v[224:227], v[20:23]
buffer_load_dwordx4 v4, s[40:43], s44 offen offset:0 lds
v_mfma_f32_16x16x32_bf16 v[68:71], v[100:103], v[228:231], v[68:71]
ds_read_b128 v[116:119], v12 offset:35136
v_add_f32 v8, v8, v50
v_mfma_f32_16x16x32_bf16 v[72:75], v[96:99], v[128:131], v[24:27]
v_exp_f32 v51, v51
v_mfma_f32_16x16x32_bf16 v[72:75], v[100:103], v[132:135], v[72:75]
v_exp_f32 v52, v52
v_mfma_f32_16x16x32_bf16 v[76:79], v[96:99], v[160:163], v[28:31]
buffer_load_dwordx4 v5, s[40:43], s44 offen offset:1088 lds
v_mfma_f32_16x16x32_bf16 v[76:79], v[100:103], v[164:167], v[76:79]
ds_read_b128 v[120:123], v12 offset:35200
s_waitcnt lgkmcnt(3)
v_mfma_f32_16x16x32_bf16 v[64:67], v[104:107], v[200:203], v[64:67]
ds_read_b128 v[124:127], v12 offset:35264
v_add_f32 v8, v8, v51
v_mfma_f32_16x16x32_bf16 v[64:67], v[108:111], v[204:207], v[64:67]
v_exp_f32 v53, v53
v_mfma_f32_16x16x32_bf16 v[68:71], v[104:107], v[232:235], v[68:71]
v_add_f32 v9, v9, v52
v_add_f32 v9, v9, v53
v_mfma_f32_16x16x32_bf16 v[68:71], v[108:111], v[236:239], v[68:71]
buffer_load_dwordx4 v6, s[40:43], s44 offen offset:2176 lds
v_mfma_f32_16x16x32_bf16 v[72:75], v[104:107], v[136:139], v[72:75]
v_exp_f32 v54, v54
v_mfma_f32_16x16x32_bf16 v[72:75], v[108:111], v[140:143], v[72:75]
v_exp_f32 v55, v55
v_mfma_f32_16x16x32_bf16 v[76:79], v[104:107], v[168:171], v[76:79]
ds_read_b128 v[96:99], v12 offset:43520
ds_read_b128 v[100:103], v12 offset:43584
v_mfma_f32_16x16x32_bf16 v[76:79], v[108:111], v[172:175], v[76:79]
v_add_f32 v9, v9, v54
s_waitcnt lgkmcnt(4)
v_mfma_f32_16x16x32_bf16 v[64:67], v[112:115], v[208:211], v[64:67]
v_exp_f32 v56, v56
v_mfma_f32_16x16x32_bf16 v[64:67], v[116:119], v[212:215], v[64:67]
buffer_load_dwordx4 v7, s[40:43], s44 offen offset:3264 lds
v_mfma_f32_16x16x32_bf16 v[68:71], v[112:115], v[240:243], v[68:71]
ds_read_b128 v[104:107], v12 offset:43648
v_add_f32 v9, v9, v55
v_mfma_f32_16x16x32_bf16 v[68:71], v[116:119], v[244:247], v[68:71]
ds_read_b128 v[108:111], v12 offset:43712
s_add_u32 m0, s46, 39168
v_mfma_f32_16x16x32_bf16 v[72:75], v[112:115], v[144:147], v[72:75]
v_exp_f32 v57, v57
v_mfma_f32_16x16x32_bf16 v[72:75], v[116:119], v[148:151], v[72:75]
v_add_f32 v10, v10, v56
v_add_f32 v10, v10, v57
v_mfma_f32_16x16x32_bf16 v[76:79], v[112:115], v[176:179], v[76:79]
buffer_load_dwordx4 v4, s[40:43], s49 offen offset:0 lds
v_mfma_f32_16x16x32_bf16 v[76:79], v[116:119], v[180:183], v[76:79]
ds_read_b128 v[112:115], v12 offset:43776
s_waitcnt lgkmcnt(5)
v_mfma_f32_16x16x32_bf16 v[64:67], v[120:123], v[216:219], v[64:67]
v_exp_f32 v58, v58
v_mfma_f32_16x16x32_bf16 v[64:67], v[124:127], v[220:223], v[64:67]
v_exp_f32 v59, v59
v_mfma_f32_16x16x32_bf16 v[68:71], v[120:123], v[248:251], v[68:71]
v_exp_f32 v60, v60
v_mfma_f32_16x16x32_bf16 v[68:71], v[124:127], v[252:255], v[68:71]
ds_read_b128 v[116:119], v12 offset:43840
v_add_f32 v10, v10, v58
v_mfma_f32_16x16x32_bf16 v[72:75], v[120:123], v[152:155], v[72:75]
buffer_load_dwordx4 v5, s[40:43], s49 offen offset:1088 lds
v_mfma_f32_16x16x32_bf16 v[72:75], v[124:127], v[156:159], v[72:75]
v_add_f32 v10, v10, v59
v_add_f32 v11, v11, v60
v_mfma_f32_16x16x32_bf16 v[76:79], v[120:123], v[184:187], v[76:79]
v_exp_f32 v61, v61
v_mfma_f32_16x16x32_bf16 v[76:79], v[124:127], v[188:191], v[76:79]
ds_read_b128 v[120:123], v12 offset:43904
s_waitcnt lgkmcnt(5)
v_mfma_f32_16x16x32_bf16 v[80:83], v[96:99], v[192:195], v[16:19]
ds_read_b128 v[124:127], v12 offset:43968
v_add_f32 v11, v11, v61
v_mfma_f32_16x16x32_bf16 v[80:83], v[100:103], v[196:199], v[80:83]
v_exp_f32 v62, v62
v_mfma_f32_16x16x32_bf16 v[84:87], v[96:99], v[224:227], v[20:23]
buffer_load_dwordx4 v6, s[40:43], s49 offen offset:2176 lds
v_mfma_f32_16x16x32_bf16 v[84:87], v[100:103], v[228:231], v[84:87]
v_exp_f32 v63, v63
v_mfma_f32_16x16x32_bf16 v[88:91], v[96:99], v[128:131], v[24:27]
v_add_f32 v11, v11, v62
v_add_f32 v11, v11, v63
v_mfma_f32_16x16x32_bf16 v[88:91], v[100:103], v[132:135], v[88:91]
v_cvt_pk_bf16_f32 v32, v32, v33
v_cvt_pk_bf16_f32 v33, v34, v35
v_mfma_f32_16x16x32_bf16 v[92:95], v[96:99], v[160:163], v[28:31]
v_cvt_pk_bf16_f32 v34, v48, v49
v_cvt_pk_bf16_f32 v35, v50, v51
v_mfma_f32_16x16x32_bf16 v[92:95], v[100:103], v[164:167], v[92:95]
v_cvt_pk_bf16_f32 v36, v36, v37
s_waitcnt lgkmcnt(4)
v_mfma_f32_16x16x32_bf16 v[80:83], v[104:107], v[200:203], v[80:83]
v_cvt_pk_bf16_f32 v37, v38, v39
v_cvt_pk_bf16_f32 v38, v52, v53
v_mfma_f32_16x16x32_bf16 v[80:83], v[108:111], v[204:207], v[80:83]
buffer_load_dwordx4 v7, s[40:43], s49 offen offset:3264 lds
v_mfma_f32_16x16x32_bf16 v[84:87], v[104:107], v[232:235], v[84:87]
v_cvt_pk_bf16_f32 v39, v54, v55
v_cvt_pk_bf16_f32 v40, v40, v41
v_mfma_f32_16x16x32_bf16 v[84:87], v[108:111], v[236:239], v[84:87]
v_cvt_pk_bf16_f32 v41, v42, v43
v_cvt_pk_bf16_f32 v42, v56, v57
v_mfma_f32_16x16x32_bf16 v[88:91], v[104:107], v[136:139], v[88:91]
v_cvt_pk_bf16_f32 v43, v58, v59
v_cvt_pk_bf16_f32 v44, v44, v45
v_mfma_f32_16x16x32_bf16 v[88:91], v[108:111], v[140:143], v[88:91]
v_cvt_pk_bf16_f32 v45, v46, v47
v_cvt_pk_bf16_f32 v46, v60, v61
v_mfma_f32_16x16x32_bf16 v[92:95], v[104:107], v[168:171], v[92:95]
v_cvt_pk_bf16_f32 v47, v62, v63
v_max3_f32 v13, v64, v65, v66
v_mfma_f32_16x16x32_bf16 v[92:95], v[108:111], v[172:175], v[92:95]
s_waitcnt vmcnt(62) lgkmcnt(0)
s_barrier
v_mfma_f32_16x16x32_bf16 v[80:83], v[112:115], v[208:211], v[80:83]
ds_read_b64_tr_b16 v[96:97], v2 offset:0
ds_read_b64_tr_b16 v[98:99], v2 offset:8704
v_mfma_f32_16x16x32_bf16 v[80:83], v[116:119], v[212:215], v[80:83]
ds_read_b64_tr_b16 v[100:101], v3 offset:0
v_max3_f32 v14, v67, v68, v69
v_mfma_f32_16x16x32_bf16 v[84:87], v[112:115], v[240:243], v[84:87]
ds_read_b64_tr_b16 v[102:103], v3 offset:8704
ds_read_b64_tr_b16 v[104:105], v2 offset:64
v_mfma_f32_16x16x32_bf16 v[84:87], v[116:119], v[244:247], v[84:87]
ds_read_b64_tr_b16 v[106:107], v2 offset:8768
v_max3_f32 v13, v13, v70, v71
v_mfma_f32_16x16x32_bf16 v[88:91], v[112:115], v[144:147], v[88:91]
ds_read_b64_tr_b16 v[108:109], v3 offset:64
ds_read_b64_tr_b16 v[110:111], v3 offset:8768
v_mfma_f32_16x16x32_bf16 v[88:91], v[116:119], v[148:151], v[88:91]
v_max3_f32 v14, v14, v72, v73
v_max3_f32 v13, v13, v74, v75
v_mfma_f32_16x16x32_bf16 v[92:95], v[112:115], v[176:179], v[92:95]
v_max3_f32 v14, v14, v76, v77
v_max3_f32 v13, v13, v78, v79
v_mfma_f32_16x16x32_bf16 v[92:95], v[116:119], v[180:183], v[92:95]
ds_read_b64_tr_b16 v[112:113], v2 offset:128
ds_read_b64_tr_b16 v[114:115], v2 offset:8832
v_mfma_f32_16x16x32_bf16 v[80:83], v[120:123], v[216:219], v[80:83]
ds_read_b64_tr_b16 v[116:117], v3 offset:128
v_mfma_f32_16x16x32_bf16 v[80:83], v[124:127], v[220:223], v[80:83]
ds_read_b64_tr_b16 v[118:119], v3 offset:8832
v_mfma_f32_16x16x32_bf16 v[84:87], v[120:123], v[248:251], v[84:87]
v_mfma_f32_16x16x32_bf16 v[84:87], v[124:127], v[252:255], v[84:87]
v_mfma_f32_16x16x32_bf16 v[88:91], v[120:123], v[152:155], v[88:91]
v_mfma_f32_16x16x32_bf16 v[88:91], v[124:127], v[156:159], v[88:91]
v_mfma_f32_16x16x32_bf16 v[92:95], v[120:123], v[184:187], v[92:95]
v_mfma_f32_16x16x32_bf16 v[92:95], v[124:127], v[188:191], v[92:95]
ds_read_b64_tr_b16 v[120:121], v2 offset:192
s_waitcnt lgkmcnt(9)
v_mfma_f32_16x16x32_bf16 a[0:3], v[32:35], v[96:99], a[0:3]
ds_read_b64_tr_b16 v[122:123], v2 offset:8896
ds_read_b64_tr_b16 v[124:125], v3 offset:192
v_mfma_f32_16x16x32_bf16 a[16:19], v[32:35], v[100:103], a[16:19]
ds_read_b64_tr_b16 v[126:127], v3 offset:8896
v_mfma_f32_16x16x32_bf16 a[4:7], v[36:39], v[96:99], a[4:7]
v_max3_f32 v14, v14, v80, v81
v_max3_f32 v13, v13, v82, v83
v_mfma_f32_16x16x32_bf16 a[20:23], v[36:39], v[100:103], a[20:23]
v_max3_f32 v14, v14, v84, v85
v_max3_f32 v13, v13, v86, v87
v_mfma_f32_16x16x32_bf16 a[8:11], v[40:43], v[96:99], a[8:11]
v_max3_f32 v14, v14, v88, v89
v_max3_f32 v13, v13, v90, v91
v_mfma_f32_16x16x32_bf16 a[24:27], v[40:43], v[100:103], a[24:27]
v_max3_f32 v14, v14, v92, v93
v_max3_f32 v13, v13, v94, v95
v_mfma_f32_16x16x32_bf16 a[12:15], v[44:47], v[96:99], a[12:15]
v_max_f32 v13, v13, v14
v_cmp_lt_f32 vcc, s48, v13
v_mfma_f32_16x16x32_bf16 a[28:31], v[44:47], v[100:103], a[28:31]
ds_read_b64_tr_b16 v[96:97], v2 offset:256
s_waitcnt lgkmcnt(9)
v_mfma_f32_16x16x32_bf16 a[44:47], v[44:47], v[104:107], a[44:47]
s_cbranch_vccnz L_p31%=_rare
ds_read_b64_tr_b16 v[98:99], v2 offset:8960
v_mfma_f32_16x16x32_bf16 a[60:63], v[44:47], v[108:111], a[60:63]
v_exp_f32 v64, v64
v_mfma_f32_16x16x32_bf16 a[40:43], v[40:43], v[104:107], a[40:43]
v_exp_f32 v65, v65
v_mfma_f32_16x16x32_bf16 a[56:59], v[40:43], v[108:111], a[56:59]
v_exp_f32 v66, v66
v_mfma_f32_16x16x32_bf16 a[36:39], v[36:39], v[104:107], a[36:39]
v_exp_f32 v67, v67
v_mfma_f32_16x16x32_bf16 a[52:55], v[36:39], v[108:111], a[52:55]
v_exp_f32 v68, v68
v_mfma_f32_16x16x32_bf16 a[32:35], v[32:35], v[104:107], a[32:35]
ds_read_b64_tr_b16 v[100:101], v3 offset:256
ds_read_b64_tr_b16 v[102:103], v3 offset:8960
v_mfma_f32_16x16x32_bf16 a[48:51], v[32:35], v[108:111], a[48:51]
v_add_f32 v8, v8, v64
s_waitcnt lgkmcnt(8)
v_mfma_f32_16x16x32_bf16 a[64:67], v[32:35], v[112:115], a[64:67]
v_exp_f32 v69, v69
v_mfma_f32_16x16x32_bf16 a[80:83], v[32:35], v[116:119], a[80:83]
v_add_f32 v8, v8, v65
v_add_f32 v8, v8, v66
v_mfma_f32_16x16x32_bf16 a[68:71], v[36:39], v[112:115], a[68:71]
v_exp_f32 v70, v70
v_mfma_f32_16x16x32_bf16 a[84:87], v[36:39], v[116:119], a[84:87]
v_add_f32 v8, v8, v67
v_add_f32 v9, v9, v68
v_mfma_f32_16x16x32_bf16 a[72:75], v[40:43], v[112:115], a[72:75]
v_exp_f32 v71, v71
v_mfma_f32_16x16x32_bf16 a[88:91], v[40:43], v[116:119], a[88:91]
v_exp_f32 v72, v72
v_mfma_f32_16x16x32_bf16 a[76:79], v[44:47], v[112:115], a[76:79]
ds_read_b64_tr_b16 v[104:105], v2 offset:320
v_add_f32 v9, v9, v69
v_mfma_f32_16x16x32_bf16 a[92:95], v[44:47], v[116:119], a[92:95]
ds_read_b64_tr_b16 v[106:107], v2 offset:9024
s_waitcnt lgkmcnt(6)
v_mfma_f32_16x16x32_bf16 a[108:111], v[44:47], v[120:123], a[108:111]
v_exp_f32 v73, v73
v_mfma_f32_16x16x32_bf16 a[124:127], v[44:47], v[124:127], a[124:127]
ds_read_b64_tr_b16 v[108:109], v3 offset:320
ds_read_b64_tr_b16 v[110:111], v3 offset:9024
v_mfma_f32_16x16x32_bf16 a[104:107], v[40:43], v[120:123], a[104:107]
v_exp_f32 v74, v74
v_mfma_f32_16x16x32_bf16 a[120:123], v[40:43], v[124:127], a[120:123]
v_add_f32 v9, v9, v70
v_add_f32 v9, v9, v71
v_mfma_f32_16x16x32_bf16 a[100:103], v[36:39], v[120:123], a[100:103]
v_exp_f32 v75, v75
v_mfma_f32_16x16x32_bf16 a[116:119], v[36:39], v[124:127], a[116:119]
v_exp_f32 v76, v76
v_mfma_f32_16x16x32_bf16 a[96:99], v[32:35], v[120:123], a[96:99]
v_add_f32 v10, v10, v72
v_add_f32 v10, v10, v73
v_mfma_f32_16x16x32_bf16 a[112:115], v[32:35], v[124:127], a[112:115]
v_add_f32 v10, v10, v74
s_waitcnt lgkmcnt(4)
v_mfma_f32_16x16x32_bf16 a[128:131], v[32:35], v[96:99], a[128:131]
v_exp_f32 v77, v77
v_mfma_f32_16x16x32_bf16 a[144:147], v[32:35], v[100:103], a[144:147]
v_exp_f32 v78, v78
v_mfma_f32_16x16x32_bf16 a[132:135], v[36:39], v[96:99], a[132:135]
ds_read_b64_tr_b16 v[112:113], v2 offset:384
ds_read_b64_tr_b16 v[114:115], v2 offset:9088
v_mfma_f32_16x16x32_bf16 a[148:151], v[36:39], v[100:103], a[148:151]
ds_read_b64_tr_b16 v[116:117], v3 offset:384
ds_read_b64_tr_b16 v[118:119], v3 offset:9088
v_mfma_f32_16x16x32_bf16 a[136:139], v[40:43], v[96:99], a[136:139]
v_exp_f32 v79, v79
v_mfma_f32_16x16x32_bf16 a[152:155], v[40:43], v[100:103], a[152:155]
ds_read_b64_tr_b16 v[120:121], v2 offset:448
ds_read_b64_tr_b16 v[122:123], v2 offset:9152
v_mfma_f32_16x16x32_bf16 a[140:143], v[44:47], v[96:99], a[140:143]
ds_read_b64_tr_b16 v[124:125], v3 offset:448
ds_read_b64_tr_b16 v[126:127], v3 offset:9152
v_mfma_f32_16x16x32_bf16 a[156:159], v[44:47], v[100:103], a[156:159]
v_add_f32 v10, v10, v75
s_waitcnt lgkmcnt(8)
v_mfma_f32_16x16x32_bf16 a[172:175], v[44:47], v[104:107], a[172:175]
v_add_f32 v11, v11, v76
v_add_f32 v11, v11, v77
v_mfma_f32_16x16x32_bf16 a[188:191], v[44:47], v[108:111], a[188:191]
v_exp_f32 v80, v80
v_mfma_f32_16x16x32_bf16 a[168:171], v[40:43], v[104:107], a[168:171]
v_exp_f32 v81, v81
v_mfma_f32_16x16x32_bf16 a[184:187], v[40:43], v[108:111], a[184:187]
v_exp_f32 v82, v82
v_mfma_f32_16x16x32_bf16 a[164:167], v[36:39], v[104:107], a[164:167]
v_add_f32 v11, v11, v78
v_add_f32 v11, v11, v79
v_mfma_f32_16x16x32_bf16 a[180:183], v[36:39], v[108:111], a[180:183]
v_exp_f32 v83, v83
v_mfma_f32_16x16x32_bf16 a[160:163], v[32:35], v[104:107], a[160:163]
v_exp_f32 v84, v84
v_mfma_f32_16x16x32_bf16 a[176:179], v[32:35], v[108:111], a[176:179]
v_add_f32 v8, v8, v80
v_add_f32 v8, v8, v81
s_waitcnt lgkmcnt(4)
v_mfma_f32_16x16x32_bf16 a[192:195], v[32:35], v[112:115], a[192:195]
v_exp_f32 v85, v85
v_mfma_f32_16x16x32_bf16 a[208:211], v[32:35], v[116:119], a[208:211]
v_exp_f32 v86, v86
v_mfma_f32_16x16x32_bf16 a[196:199], v[36:39], v[112:115], a[196:199]
v_add_f32 v8, v8, v82
v_add_f32 v8, v8, v83
v_mfma_f32_16x16x32_bf16 a[212:215], v[36:39], v[116:119], a[212:215]
v_exp_f32 v87, v87
v_mfma_f32_16x16x32_bf16 a[200:203], v[40:43], v[112:115], a[200:203]
v_exp_f32 v88, v88
v_mfma_f32_16x16x32_bf16 a[216:219], v[40:43], v[116:119], a[216:219]
v_add_f32 v9, v9, v84
v_add_f32 v9, v9, v85
v_mfma_f32_16x16x32_bf16 a[204:207], v[44:47], v[112:115], a[204:207]
v_exp_f32 v89, v89
v_mfma_f32_16x16x32_bf16 a[220:223], v[44:47], v[116:119], a[220:223]
v_exp_f32 v90, v90
s_waitcnt lgkmcnt(0)
v_mfma_f32_16x16x32_bf16 a[236:239], v[44:47], v[120:123], a[236:239]
v_add_f32 v9, v9, v86
v_add_f32 v9, v9, v87
v_mfma_f32_16x16x32_bf16 a[252:255], v[44:47], v[124:127], a[252:255]
v_exp_f32 v91, v91
v_mfma_f32_16x16x32_bf16 a[232:235], v[40:43], v[120:123], a[232:235]
v_add_f32 v10, v10, v88
s_add_u32 s44, s44, 32768
v_mfma_f32_16x16x32_bf16 a[248:251], v[40:43], v[124:127], a[248:251]
s_add_u32 s49, s49, 32768
v_add_f32 v10, v10, v89
v_mfma_f32_16x16x32_bf16 a[228:231], v[36:39], v[120:123], a[228:231]
v_add_f32 v10, v10, v90
v_add_f32 v10, v10, v91
v_mfma_f32_16x16x32_bf16 a[244:247], v[36:39], v[124:127], a[244:247]
v_mfma_f32_16x16x32_bf16 a[224:227], v[32:35], v[120:123], a[224:227]
v_mfma_f32_16x16x32_bf16 a[240:243], v[32:35], v[124:127], a[240:243]
L_p31%=_next:
s_waitcnt vmcnt(0) lgkmcnt(0)
s_barrier
ds_read_b64_tr_b16 v[192:193], v2 offset:34816
ds_read_b64_tr_b16 v[194:195], v2 offset:43520
ds_read_b64_tr_b16 v[196:197], v3 offset:34816
ds_read_b64_tr_b16 v[198:199], v3 offset:43520
ds_read_b64_tr_b16 v[200:201], v2 offset:34880
ds_read_b64_tr_b16 v[202:203], v2 offset:43584
ds_read_b64_tr_b16 v[204:205], v3 offset:34880
ds_read_b64_tr_b16 v[206:207], v3 offset:43584
ds_read_b64_tr_b16 v[208:209], v2 offset:34944
ds_read_b64_tr_b16 v[210:211], v2 offset:43648
ds_read_b64_tr_b16 v[212:213], v3 offset:34944
ds_read_b64_tr_b16 v[214:215], v3 offset:43648
ds_read_b64_tr_b16 v[216:217], v2 offset:35008
ds_read_b64_tr_b16 v[218:219], v2 offset:43712
ds_read_b64_tr_b16 v[220:221], v3 offset:35008
ds_read_b64_tr_b16 v[222:223], v3 offset:43712
ds_read_b64_tr_b16 v[224:225], v2 offset:35072
ds_read_b64_tr_b16 v[226:227], v2 offset:43776
ds_read_b64_tr_b16 v[228:229], v3 offset:35072
ds_read_b64_tr_b16 v[230:231], v3 offset:43776
ds_read_b64_tr_b16 v[232:233], v2 offset:35136
ds_read_b64_tr_b16 v[234:235], v2 offset:43840
ds_read_b64_tr_b16 v[236:237], v3 offset:35136
ds_read_b64_tr_b16 v[238:239], v3 offset:43840
ds_read_b64_tr_b16 v[240:241], v2 offset:35200
ds_read_b64_tr_b16 v[242:243], v2 offset:43904
ds_read_b64_tr_b16 v[244:245], v3 offset:35200
ds_read_b64_tr_b16 v[246:247], v3 offset:43904
ds_read_b64_tr_b16 v[248:249], v2 offset:35264
ds_read_b64_tr_b16 v[250:251], v2 offset:43968
ds_read_b64_tr_b16 v[252:253], v3 offset:35264
ds_read_b64_tr_b16 v[254:255], v3 offset:43968
v_exp_f32 v92, v92
s_nop 0
v_add_f32 v11, v11, v92
v_cvt_pk_bf16_f32 v64, v64, v65
v_exp_f32 v93, v93
s_nop 0
v_add_f32 v11, v11, v93
v_cvt_pk_bf16_f32 v65, v66, v67
v_exp_f32 v94, v94
s_nop 0
v_add_f32 v11, v11, v94
v_cvt_pk_bf16_f32 v66, v80, v81
v_exp_f32 v95, v95
s_mul_i32 s53, s80, 20480
s_add_u32 s53, s53, 28672
s_cmp_eq_u32 s80, 3
s_cselect_b32 s53, 139264, s53
s_mul_i32 s54, s80, 12288
s_add_u32 s54, s54, 90112
v_add_u32 v4, s53, v1
v_add_u32 v5, s54, v1
v_add_f32 v11, v11, v95
v_cvt_pk_bf16_f32 v67, v82, v83
v_cvt_pk_bf16_f32 v68, v68, v69
v_cvt_pk_bf16_f32 v69, v70, v71
v_cvt_pk_bf16_f32 v70, v84, v85
v_cvt_pk_bf16_f32 v71, v86, v87
v_cvt_pk_bf16_f32 v72, v72, v73
v_cvt_pk_bf16_f32 v73, v74, v75
v_cvt_pk_bf16_f32 v74, v88, v89
v_cvt_pk_bf16_f32 v75, v90, v91
v_cvt_pk_bf16_f32 v76, v76, v77
v_cvt_pk_bf16_f32 v77, v78, v79
v_cvt_pk_bf16_f32 v78, v92, v93
v_cvt_pk_bf16_f32 v79, v94, v95
s_xor_b32 s52, s80, 2
s_mul_i32 s53, s52, 20480
s_add_u32 s53, s53, 28672
s_waitcnt lgkmcnt(14)
v_mfma_f32_16x16x32_bf16 a[0:3], v[64:67], v[192:195], a[0:3]
s_cmp_eq_u32 s52, 3
s_cselect_b32 s53, 139264, s53
v_mfma_f32_16x16x32_bf16 a[16:19], v[64:67], v[196:199], a[16:19]
s_mul_i32 s54, s52, 12288
s_add_u32 s54, s54, 90112
v_mfma_f32_16x16x32_bf16 a[4:7], v[68:71], v[192:195], a[4:7]
v_add_u32 v6, s53, v1
v_add_u32 v7, s54, v1
v_mfma_f32_16x16x32_bf16 a[20:23], v[68:71], v[196:199], a[20:23]
v_lshrrev_b32 v56, 4, v1
s_lshl_b32 s55, s80, 9
v_mfma_f32_16x16x32_bf16 a[8:11], v[72:75], v[192:195], a[8:11]
s_add_u32 s55, s55, 159744
v_and_b32 v57, 15, v56
v_mfma_f32_16x16x32_bf16 a[24:27], v[72:75], v[196:199], a[24:27]
v_lshlrev_b32 v58, 2, v57
v_add_u32 v14, s55, v58
v_mfma_f32_16x16x32_bf16 a[12:15], v[76:79], v[192:195], a[12:15]
s_waitcnt lgkmcnt(0)
s_barrier
v_mfma_f32_16x16x32_bf16 a[28:31], v[76:79], v[196:199], a[28:31]
v_lshrrev_b32 v58, 4, v56
v_lshlrev_b32 v58, 4, v58
v_mfma_f32_16x16x32_bf16 a[44:47], v[76:79], v[200:203], a[44:47]
v_add_u32 v15, s55, v58
s_lshl_b32 s55, s52, 9
v_mfma_f32_16x16x32_bf16 a[60:63], v[76:79], v[204:207], a[60:63]
s_add_u32 s55, s55, 159744
v_add_u32 v60, s55, v58
v_mfma_f32_16x16x32_bf16 a[40:43], v[72:75], v[200:203], a[40:43]
ds_write_b128 v4, a[8:11] offset:0
v_mfma_f32_16x16x32_bf16 a[56:59], v[72:75], v[204:207], a[56:59]
v_mov_b32 v52, v8
v_mov_b32 v53, v9
v_mfma_f32_16x16x32_bf16 a[36:39], v[68:71], v[200:203], a[36:39]
ds_write_b128 v4, a[24:27] offset:2048
v_mfma_f32_16x16x32_bf16 a[52:55], v[68:71], v[204:207], a[52:55]
ds_write_b128 v4, a[12:15] offset:1024
v_mfma_f32_16x16x32_bf16 a[32:35], v[64:67], v[200:203], a[32:35]
v_mov_b32 v54, v10
v_mov_b32 v55, v11
v_mfma_f32_16x16x32_bf16 a[48:51], v[64:67], v[204:207], a[48:51]
ds_write_b128 v4, a[28:31] offset:3072
v_mfma_f32_16x16x32_bf16 a[64:67], v[64:67], v[208:211], a[64:67]
ds_write_b128 v4, a[44:47] offset:5120
v_permlane32_swap_b32 v8, v52
v_mfma_f32_16x16x32_bf16 a[80:83], v[64:67], v[212:215], a[80:83]
ds_write_b128 v4, a[60:63] offset:7168
v_permlane32_swap_b32 v9, v53
v_mfma_f32_16x16x32_bf16 a[68:71], v[68:71], v[208:211], a[68:71]
ds_write_b128 v4, a[40:43] offset:4096
v_permlane32_swap_b32 v10, v54
v_mfma_f32_16x16x32_bf16 a[84:87], v[68:71], v[212:215], a[84:87]
ds_write_b128 v4, a[56:59] offset:6144
v_permlane32_swap_b32 v11, v55
v_mfma_f32_16x16x32_bf16 a[72:75], v[72:75], v[208:211], a[72:75]
v_add_f32 v8, v8, v52
v_add_f32 v9, v9, v53
v_mfma_f32_16x16x32_bf16 a[88:91], v[72:75], v[212:215], a[88:91]
v_add_f32 v10, v10, v54
v_add_f32 v11, v11, v55
v_mfma_f32_16x16x32_bf16 a[76:79], v[76:79], v[208:211], a[76:79]
v_mov_b32 v52, v8
v_mov_b32 v53, v9
v_mfma_f32_16x16x32_bf16 a[92:95], v[76:79], v[212:215], a[92:95]
v_mov_b32 v54, v10
v_mov_b32 v55, v11
v_mfma_f32_16x16x32_bf16 a[108:111], v[76:79], v[216:219], a[108:111]
v_permlane16_swap_b32 v8, v52
v_mfma_f32_16x16x32_bf16 a[124:127], v[76:79], v[220:223], a[124:127]
ds_write_b128 v4, a[72:75] offset:8192
v_mfma_f32_16x16x32_bf16 a[104:107], v[72:75], v[216:219], a[104:107]
v_permlane16_swap_b32 v9, v53
v_mfma_f32_16x16x32_bf16 a[120:123], v[72:75], v[220:223], a[120:123]
ds_write_b128 v4, a[88:91] offset:10240
v_mfma_f32_16x16x32_bf16 a[100:103], v[68:71], v[216:219], a[100:103]
v_permlane16_swap_b32 v10, v54
v_mfma_f32_16x16x32_bf16 a[116:119], v[68:71], v[220:223], a[116:119]
ds_write_b128 v4, a[76:79] offset:9216
v_mfma_f32_16x16x32_bf16 a[96:99], v[64:67], v[216:219], a[96:99]
v_permlane16_swap_b32 v11, v55
v_mfma_f32_16x16x32_bf16 a[112:115], v[64:67], v[220:223], a[112:115]
ds_write_b128 v4, a[92:95] offset:11264
v_mfma_f32_16x16x32_bf16 a[128:131], v[64:67], v[224:227], a[128:131]
ds_write_b128 v4, a[108:111] offset:13312
v_add_f32 v8, v8, v52
v_mfma_f32_16x16x32_bf16 a[144:147], v[64:67], v[228:231], a[144:147]
ds_write_b128 v4, a[124:127] offset:15360
v_add_f32 v9, v9, v53
v_mfma_f32_16x16x32_bf16 a[132:135], v[68:71], v[224:227], a[132:135]
ds_write_b128 v4, a[104:107] offset:12288
v_add_f32 v10, v10, v54
v_mfma_f32_16x16x32_bf16 a[148:151], v[68:71], v[228:231], a[148:151]
ds_write_b128 v4, a[120:123] offset:14336
v_add_f32 v11, v11, v55
v_mfma_f32_16x16x32_bf16 a[136:139], v[72:75], v[224:227], a[136:139]
ds_write_b32 v14, v16 offset:0
ds_write_b32 v14, v8 offset:256
v_mfma_f32_16x16x32_bf16 a[152:155], v[72:75], v[228:231], a[152:155]
ds_write_b32 v14, v20 offset:64
ds_write_b32 v14, v9 offset:320
v_mfma_f32_16x16x32_bf16 a[140:143], v[76:79], v[224:227], a[140:143]
ds_write_b32 v14, v24 offset:128
ds_write_b32 v14, v10 offset:384
ds_write_b32 v14, v28 offset:192
v_mfma_f32_16x16x32_bf16 a[156:159], v[76:79], v[228:231], a[156:159]
ds_write_b32 v14, v11 offset:448
v_mfma_f32_16x16x32_bf16 a[172:175], v[76:79], v[232:235], a[172:175]
ds_write_b128 v4, a[136:139] offset:16384
v_mfma_f32_16x16x32_bf16 a[188:191], v[76:79], v[236:239], a[188:191]
ds_write_b128 v4, a[152:155] offset:18432
v_mfma_f32_16x16x32_bf16 a[168:171], v[72:75], v[232:235], a[168:171]
s_waitcnt lgkmcnt(0)
s_barrier
v_mfma_f32_16x16x32_bf16 a[184:187], v[72:75], v[236:239], a[184:187]
ds_read_b128 v[32:35], v15 offset:0
ds_read_b128 v[36:39], v15 offset:256
s_add_u32 s54, s5, 0
v_mfma_f32_16x16x32_bf16 a[164:167], v[68:71], v[232:235], a[164:167]
ds_read_b128 v[40:43], v60 offset:128
ds_read_b128 v[44:47], v60 offset:384
v_mfma_f32_16x16x32_bf16 a[180:183], v[68:71], v[236:239], a[180:183]
ds_read_b128 v[80:83], v15 offset:64
ds_read_b128 v[84:87], v15 offset:320
buffer_load_dwordx4 a[8:11], v1, s[60:63], s54 offen offset:0 sc1
v_mfma_f32_16x16x32_bf16 a[160:163], v[64:67], v[232:235], a[160:163]
ds_write_b128 v4, a[140:143] offset:17408
ds_read_b128 v[88:91], v60 offset:192
ds_read_b128 v[92:95], v60 offset:448
v_mfma_f32_16x16x32_bf16 a[176:179], v[64:67], v[236:239], a[176:179]
ds_write_b128 v4, a[156:159] offset:19456
ds_read_b128 v[128:131], v6 offset:0
ds_read_b128 v[132:135], v6 offset:1024
buffer_load_dwordx4 a[12:15], v1, s[60:63], s54 offen offset:1024 sc1
v_mfma_f32_16x16x32_bf16 a[192:195], v[64:67], v[240:243], a[192:195]
ds_write_b128 v5, a[172:175] offset:1024
ds_read_b128 v[136:139], v6 offset:2048
ds_read_b128 v[140:143], v6 offset:3072
v_mfma_f32_16x16x32_bf16 a[208:211], v[64:67], v[244:247], a[208:211]
ds_write_b128 v5, a[188:191] offset:3072
ds_read_b128 v[144:147], v6 offset:4096
ds_read_b128 v[148:151], v6 offset:5120
buffer_load_dwordx4 a[24:27], v1, s[60:63], s54 offen offset:2048 sc1
v_mfma_f32_16x16x32_bf16 a[196:199], v[68:71], v[240:243], a[196:199]
ds_write_b128 v5, a[168:171] offset:0
v_mfma_f32_16x16x32_bf16 a[212:215], v[68:71], v[244:247], a[212:215]
ds_write_b128 v5, a[184:187] offset:2048
buffer_load_dwordx4 a[28:31], v1, s[60:63], s54 offen offset:3072 sc1
s_add_u32 s54, s5, 4096
v_mfma_f32_16x16x32_bf16 a[200:203], v[72:75], v[240:243], a[200:203]
buffer_load_dwordx4 a[40:43], v1, s[60:63], s54 offen offset:0 sc1
v_mfma_f32_16x16x32_bf16 a[216:219], v[72:75], v[244:247], a[216:219]
s_waitcnt lgkmcnt(14)
v_min_f32 v48, v32, v40
v_min_f32 v49, v33, v41
v_mfma_f32_16x16x32_bf16 a[204:207], v[76:79], v[240:243], a[204:207]
buffer_load_dwordx4 a[44:47], v1, s[60:63], s54 offen offset:1024 sc1
v_mfma_f32_16x16x32_bf16 a[220:223], v[76:79], v[244:247], a[220:223]
v_min_f32 v50, v34, v42
v_min_f32 v51, v35, v43
v_mfma_f32_16x16x32_bf16 a[236:239], v[76:79], v[248:251], a[236:239]
buffer_load_dwordx4 a[56:59], v1, s[60:63], s54 offen offset:2048 sc1
v_mfma_f32_16x16x32_bf16 a[252:255], v[76:79], v[252:255], a[252:255]
ds_write_b128 v5, a[200:203] offset:4096
v_mfma_f32_16x16x32_bf16 a[232:235], v[72:75], v[248:251], a[232:235]
buffer_load_dwordx4 a[60:63], v1, s[60:63], s54 offen offset:3072 sc1
v_mfma_f32_16x16x32_bf16 a[248:251], v[72:75], v[252:255], a[248:251]
s_add_u32 s54, s5, 8192
v_sub_f32 v32, v48, v32
v_mfma_f32_16x16x32_bf16 a[228:231], v[68:71], v[248:251], a[228:231]
ds_write_b128 v5, a[216:219] offset:6144
v_mfma_f32_16x16x32_bf16 a[244:247], v[68:71], v[252:255], a[244:247]
buffer_load_dwordx4 a[72:75], v1, s[60:63], s54 offen offset:0 sc1
v_mfma_f32_16x16x32_bf16 a[224:227], v[64:67], v[248:251], a[224:227]
ds_write_b128 v5, a[204:207] offset:5120
ds_write_b128 v5, a[220:223] offset:7168
v_mfma_f32_16x16x32_bf16 a[240:243], v[64:67], v[252:255], a[240:243]
v_sub_f32 v40, v48, v40
v_sub_f32 v33, v49, v33
v_sub_f32 v41, v49, v41
v_sub_f32 v34, v50, v34
v_sub_f32 v42, v50, v42
v_sub_f32 v35, v51, v35
v_sub_f32 v43, v51, v43
v_exp_f32 v32, v32
v_exp_f32 v40, v40
buffer_load_dwordx4 a[76:79], v1, s[60:63], s54 offen offset:1024 sc1
s_waitcnt lgkmcnt(0)
s_nop 7
v_exp_f32 v33, v33
v_exp_f32 v41, v41
v_exp_f32 v34, v34
v_exp_f32 v42, v42
v_exp_f32 v35, v35
v_exp_f32 v43, v43
v_mul_f32 v36, v36, v32
v_mul_f32 v37, v37, v33
v_mul_f32 v38, v38, v34
v_mul_f32 v39, v39, v35
v_fmac_f32 v36, v44, v40
v_fmac_f32 v37, v45, v41
v_fmac_f32 v38, v46, v42
v_fmac_f32 v39, v47, v43
v_rcp_f32 v36, v36
v_rcp_f32 v37, v37
v_rcp_f32 v38, v38
v_rcp_f32 v39, v39
v_mul_f32 v16, v32, v36
v_mul_f32 v24, v40, v36
v_mul_f32 v17, v33, v37
v_mul_f32 v25, v41, v37
v_mul_f32 v18, v34, v38
v_mul_f32 v26, v42, v38
v_mul_f32 v19, v35, v39
v_mul_f32 v27, v43, v39
v_min_f32 v48, v80, v88
v_min_f32 v49, v81, v89
v_min_f32 v50, v82, v90
v_min_f32 v51, v83, v91
v_sub_f32 v80, v48, v80
v_sub_f32 v88, v48, v88
v_sub_f32 v81, v49, v81
v_sub_f32 v89, v49, v89
v_sub_f32 v82, v50, v82
v_sub_f32 v90, v50, v90
v_sub_f32 v83, v51, v83
v_sub_f32 v91, v51, v91
v_exp_f32 v80, v80
v_exp_f32 v88, v88
v_exp_f32 v81, v81
v_exp_f32 v89, v89
v_exp_f32 v82, v82
v_exp_f32 v90, v90
v_exp_f32 v83, v83
v_exp_f32 v91, v91
v_mul_f32 v84, v84, v80
v_mul_f32 v85, v85, v81
v_mul_f32 v86, v86, v82
v_mul_f32 v87, v87, v83
v_fmac_f32 v84, v92, v88
v_fmac_f32 v85, v93, v89
v_fmac_f32 v86, v94, v90
v_fmac_f32 v87, v95, v91
v_rcp_f32 v84, v84
v_rcp_f32 v85, v85
v_rcp_f32 v86, v86
v_rcp_f32 v87, v87
v_mul_f32 v20, v80, v84
v_mul_f32 v28, v88, v84
v_mul_f32 v21, v81, v85
v_mul_f32 v29, v89, v85
v_mul_f32 v22, v82, v86
v_mul_f32 v30, v90, v86
v_mul_f32 v23, v83, v87
v_mul_f32 v31, v91, v87
v_lshrrev_b32 v104, 4, v1
v_bfe_u32 v105, v104, 2, 2
v_lshrrev_b32 v106, 4, v104
v_cmp_eq_u32 vcc, v105, v106
v_and_b32 v105, 1, v104
v_lshlrev_b32 v105, 4, v105
v_mov_b32 v106, 0x3f80
v_lshlrev_b32 v105, v105, v106
v_cndmask_b32 v105, 0, v105, vcc
v_and_b32 v106, 2, v104
v_cmp_eq_u32 vcc, 0, v106
v_mov_b32 v118, 0
v_mov_b32 v119, 0
v_mov_b32 v122, 0
v_mov_b32 v123, 0
v_cndmask_b32 v120, 0, v105, vcc
v_cndmask_b32 v121, v105, 0, vcc
ds_write_b128 v5, a[232:235] offset:8192
ds_write_b128 v5, a[236:239] offset:9216
ds_write_b128 v5, a[248:251] offset:10240
ds_write_b128 v5, a[252:255] offset:11264
ds_read_b128 v[152:155], v6 offset:6144
ds_read_b128 v[156:159], v6 offset:7168
s_add_u32 s54, s5, 8192
buffer_load_dwordx4 a[88:91], v1, s[60:63], s54 offen offset:2048 sc1
buffer_load_dwordx4 a[92:95], v1, s[60:63], s54 offen offset:3072 sc1
s_add_u32 s54, s5, 12288
buffer_load_dwordx4 a[104:107], v1, s[60:63], s54 offen offset:0 sc1
buffer_load_dwordx4 a[108:111], v1, s[60:63], s54 offen offset:1024 sc1
v_accvgpr_read_b32 v96, a0
v_accvgpr_read_b32 v97, a1
v_accvgpr_read_b32 v98, a2
v_accvgpr_read_b32 v99, a3
v_accvgpr_read_b32 v100, a4
v_accvgpr_read_b32 v101, a5
v_accvgpr_read_b32 v102, a6
v_accvgpr_read_b32 v103, a7
v_pk_mul_f32 v[128:129], v[128:129], v[24:25]
v_pk_mul_f32 v[130:131], v[130:131], v[26:27]
v_pk_fma_f32 v[128:129], v[96:97], v[16:17], v[128:129]
v_pk_fma_f32 v[130:131], v[98:99], v[18:19], v[130:131]
v_pk_mul_f32 v[132:133], v[132:133], v[28:29]
v_pk_mul_f32 v[134:135], v[134:135], v[30:31]
v_pk_fma_f32 v[132:133], v[100:101], v[20:21], v[132:133]
v_pk_fma_f32 v[134:135], v[102:103], v[22:23], v[134:135]
v_cvt_pk_bf16_f32 v128, v128, v129
v_cvt_pk_bf16_f32 v129, v130, v131
v_cvt_pk_bf16_f32 v130, v132, v133
v_cvt_pk_bf16_f32 v131, v134, v135
s_nop 1
v_mfma_f32_16x16x32_bf16 v[104:107], v[128:131], v[120:123], 0
v_mfma_f32_16x16x32_bf16 v[108:111], v[128:131], v[118:121], 0
ds_read_b128 v[160:163], v6 offset:8192
ds_read_b128 v[164:167], v6 offset:9216
s_add_u32 s54, s5, 12288
buffer_load_dwordx4 a[120:123], v1, s[60:63], s54 offen offset:2048 sc1
buffer_load_dwordx4 a[124:127], v1, s[60:63], s54 offen offset:3072 sc1
s_add_u32 s54, s5, 16384
buffer_load_dwordx4 a[136:139], v1, s[60:63], s54 offen offset:0 sc1
buffer_load_dwordx4 a[140:143], v1, s[60:63], s54 offen offset:1024 sc1
v_accvgpr_read_b32 v96, a16
v_accvgpr_read_b32 v97, a17
v_accvgpr_read_b32 v98, a18
v_accvgpr_read_b32 v99, a19
v_accvgpr_read_b32 v100, a20
v_accvgpr_read_b32 v101, a21
v_accvgpr_read_b32 v102, a22
v_accvgpr_read_b32 v103, a23
v_pk_mul_f32 v[136:137], v[136:137], v[24:25]
v_pk_mul_f32 v[138:139], v[138:139], v[26:27]
v_pk_fma_f32 v[136:137], v[96:97], v[16:17], v[136:137]
v_pk_fma_f32 v[138:139], v[98:99], v[18:19], v[138:139]
v_pk_mul_f32 v[140:141], v[140:141], v[28:29]
v_pk_mul_f32 v[142:143], v[142:143], v[30:31]
v_pk_fma_f32 v[140:141], v[100:101], v[20:21], v[140:141]
v_pk_fma_f32 v[142:143], v[102:103], v[22:23], v[142:143]
v_cvt_pk_bf16_f32 v136, v136, v137
v_cvt_pk_bf16_f32 v137, v138, v139
v_cvt_pk_bf16_f32 v138, v140, v141
v_cvt_pk_bf16_f32 v139, v142, v143
v_cvt_pk_bf16_f32 v32, v104, v105
v_cvt_pk_bf16_f32 v33, v106, v107
v_cvt_pk_bf16_f32 v64, v108, v109
v_cvt_pk_bf16_f32 v65, v110, v111
v_mfma_f32_16x16x32_bf16 v[112:115], v[136:139], v[120:123], 0
v_mfma_f32_16x16x32_bf16 v[124:127], v[136:139], v[118:121], 0
ds_read_b128 v[168:171], v6 offset:10240
ds_read_b128 v[172:175], v6 offset:11264
s_add_u32 s54, s5, 16384
buffer_load_dwordx4 a[152:155], v1, s[60:63], s54 offen offset:2048 sc1
buffer_load_dwordx4 a[156:159], v1, s[60:63], s54 offen offset:3072 sc1
s_add_u32 s54, s5, 20480
buffer_load_dwordx4 a[168:171], v1, s[60:63], s54 offen offset:0 sc1
buffer_load_dwordx4 a[172:175], v1, s[60:63], s54 offen offset:1024 sc1
v_accvgpr_read_b32 v96, a32
v_accvgpr_read_b32 v97, a33
v_accvgpr_read_b32 v98, a34
v_accvgpr_read_b32 v99, a35
v_accvgpr_read_b32 v100, a36
v_accvgpr_read_b32 v101, a37
v_accvgpr_read_b32 v102, a38
v_accvgpr_read_b32 v103, a39
v_pk_mul_f32 v[144:145], v[144:145], v[24:25]
v_pk_mul_f32 v[146:147], v[146:147], v[26:27]
v_pk_fma_f32 v[144:145], v[96:97], v[16:17], v[144:145]
v_pk_fma_f32 v[146:147], v[98:99], v[18:19], v[146:147]
v_pk_mul_f32 v[148:149], v[148:149], v[28:29]
v_pk_mul_f32 v[150:151], v[150:151], v[30:31]
v_pk_fma_f32 v[148:149], v[100:101], v[20:21], v[148:149]
v_pk_fma_f32 v[150:151], v[102:103], v[22:23], v[150:151]
v_cvt_pk_bf16_f32 v144, v144, v145
v_cvt_pk_bf16_f32 v145, v146, v147
v_cvt_pk_bf16_f32 v146, v148, v149
v_cvt_pk_bf16_f32 v147, v150, v151
v_cvt_pk_bf16_f32 v34, v112, v113
v_cvt_pk_bf16_f32 v35, v114, v115
v_cvt_pk_bf16_f32 v66, v124, v125
v_cvt_pk_bf16_f32 v67, v126, v127
v_mfma_f32_16x16x32_bf16 v[104:107], v[144:147], v[120:123], 0
v_mfma_f32_16x16x32_bf16 v[108:111], v[144:147], v[118:121], 0
s_waitcnt lgkmcnt(0)
s_barrier
ds_read_b128 v[176:179], v6 offset:12288
ds_read_b128 v[180:183], v6 offset:13312
s_add_u32 s54, s5, 20480
buffer_load_dwordx4 a[184:187], v1, s[60:63], s54 offen offset:2048 sc1
buffer_load_dwordx4 a[188:191], v1, s[60:63], s54 offen offset:3072 sc1
s_add_u32 s54, s5, 24576
buffer_load_dwordx4 a[200:203], v1, s[60:63], s54 offen offset:0 sc1
buffer_load_dwordx4 a[204:207], v1, s[60:63], s54 offen offset:1024 sc1
v_accvgpr_read_b32 v96, a48
v_accvgpr_read_b32 v97, a49
v_accvgpr_read_b32 v98, a50
v_accvgpr_read_b32 v99, a51
v_accvgpr_read_b32 v100, a52
v_accvgpr_read_b32 v101, a53
v_accvgpr_read_b32 v102, a54
v_accvgpr_read_b32 v103, a55
v_pk_mul_f32 v[152:153], v[152:153], v[24:25]
v_pk_mul_f32 v[154:155], v[154:155], v[26:27]
v_pk_fma_f32 v[152:153], v[96:97], v[16:17], v[152:153]
v_pk_fma_f32 v[154:155], v[98:99], v[18:19], v[154:155]
v_pk_mul_f32 v[156:157], v[156:157], v[28:29]
v_pk_mul_f32 v[158:159], v[158:159], v[30:31]
v_pk_fma_f32 v[156:157], v[100:101], v[20:21], v[156:157]
v_pk_fma_f32 v[158:159], v[102:103], v[22:23], v[158:159]
v_cvt_pk_bf16_f32 v152, v152, v153
v_cvt_pk_bf16_f32 v153, v154, v155
v_cvt_pk_bf16_f32 v154, v156, v157
v_cvt_pk_bf16_f32 v155, v158, v159
v_cvt_pk_bf16_f32 v36, v104, v105
v_cvt_pk_bf16_f32 v37, v106, v107
v_cvt_pk_bf16_f32 v68, v108, v109
v_cvt_pk_bf16_f32 v69, v110, v111
v_mfma_f32_16x16x32_bf16 v[112:115], v[152:155], v[120:123], 0
v_mfma_f32_16x16x32_bf16 v[124:127], v[152:155], v[118:121], 0
ds_read_b128 v[184:187], v6 offset:14336
ds_read_b128 v[188:191], v6 offset:15360
s_add_u32 s54, s5, 24576
buffer_load_dwordx4 a[216:219], v1, s[60:63], s54 offen offset:2048 sc1
buffer_load_dwordx4 a[220:223], v1, s[60:63], s54 offen offset:3072 sc1
s_add_u32 s54, s5, 28672
buffer_load_dwordx4 a[232:235], v1, s[60:63], s54 offen offset:0 sc1
buffer_load_dwordx4 a[236:239], v1, s[60:63], s54 offen offset:1024 sc1
v_accvgpr_read_b32 v96, a64
v_accvgpr_read_b32 v97, a65
v_accvgpr_read_b32 v98, a66
v_accvgpr_read_b32 v99, a67
v_accvgpr_read_b32 v100, a68
v_accvgpr_read_b32 v101, a69
v_accvgpr_read_b32 v102, a70
v_accvgpr_read_b32 v103, a71
v_pk_mul_f32 v[160:161], v[160:161], v[24:25]
v_pk_mul_f32 v[162:163], v[162:163], v[26:27]
v_pk_fma_f32 v[160:161], v[96:97], v[16:17], v[160:161]
v_pk_fma_f32 v[162:163], v[98:99], v[18:19], v[162:163]
v_pk_mul_f32 v[164:165], v[164:165], v[28:29]
v_pk_mul_f32 v[166:167], v[166:167], v[30:31]
v_pk_fma_f32 v[164:165], v[100:101], v[20:21], v[164:165]
v_pk_fma_f32 v[166:167], v[102:103], v[22:23], v[166:167]
v_cvt_pk_bf16_f32 v160, v160, v161
v_cvt_pk_bf16_f32 v161, v162, v163
v_cvt_pk_bf16_f32 v162, v164, v165
v_cvt_pk_bf16_f32 v163, v166, v167
v_cvt_pk_bf16_f32 v38, v112, v113
v_cvt_pk_bf16_f32 v39, v114, v115
v_cvt_pk_bf16_f32 v70, v124, v125
v_cvt_pk_bf16_f32 v71, v126, v127
v_mfma_f32_16x16x32_bf16 v[104:107], v[160:163], v[120:123], 0
v_mfma_f32_16x16x32_bf16 v[108:111], v[160:163], v[118:121], 0
ds_read_b128 v[192:195], v6 offset:16384
ds_read_b128 v[196:199], v6 offset:17408
s_add_u32 s54, s5, 28672
buffer_load_dwordx4 a[248:251], v1, s[60:63], s54 offen offset:2048 sc1
buffer_load_dwordx4 a[252:255], v1, s[60:63], s54 offen offset:3072 sc1
s_add_u32 s54, s5, 32768
buffer_load_dwordx4 a[0:3], v1, s[60:63], s54 offen offset:0 sc1
buffer_load_dwordx4 a[4:7], v1, s[60:63], s54 offen offset:1024 sc1
v_accvgpr_read_b32 v96, a80
v_accvgpr_read_b32 v97, a81
v_accvgpr_read_b32 v98, a82
v_accvgpr_read_b32 v99, a83
v_accvgpr_read_b32 v100, a84
v_accvgpr_read_b32 v101, a85
v_accvgpr_read_b32 v102, a86
v_accvgpr_read_b32 v103, a87
v_pk_mul_f32 v[168:169], v[168:169], v[24:25]
v_pk_mul_f32 v[170:171], v[170:171], v[26:27]
v_pk_fma_f32 v[168:169], v[96:97], v[16:17], v[168:169]
v_pk_fma_f32 v[170:171], v[98:99], v[18:19], v[170:171]
v_pk_mul_f32 v[172:173], v[172:173], v[28:29]
v_pk_mul_f32 v[174:175], v[174:175], v[30:31]
v_pk_fma_f32 v[172:173], v[100:101], v[20:21], v[172:173]
v_pk_fma_f32 v[174:175], v[102:103], v[22:23], v[174:175]
v_cvt_pk_bf16_f32 v168, v168, v169
v_cvt_pk_bf16_f32 v169, v170, v171
v_cvt_pk_bf16_f32 v170, v172, v173
v_cvt_pk_bf16_f32 v171, v174, v175
v_cvt_pk_bf16_f32 v40, v104, v105
v_cvt_pk_bf16_f32 v41, v106, v107
v_cvt_pk_bf16_f32 v72, v108, v109
v_cvt_pk_bf16_f32 v73, v110, v111
v_mfma_f32_16x16x32_bf16 v[112:115], v[168:171], v[120:123], 0
v_mfma_f32_16x16x32_bf16 v[124:127], v[168:171], v[118:121], 0
ds_read_b128 v[200:203], v6 offset:18432
ds_read_b128 v[204:207], v6 offset:19456
s_add_u32 s54, s5, 32768
buffer_load_dwordx4 a[16:19], v1, s[60:63], s54 offen offset:2048 sc1
buffer_load_dwordx4 a[20:23], v1, s[60:63], s54 offen offset:3072 sc1
s_add_u32 s54, s5, 36864
buffer_load_dwordx4 a[32:35], v1, s[60:63], s54 offen offset:0 sc1
buffer_load_dwordx4 a[36:39], v1, s[60:63], s54 offen offset:1024 sc1
v_accvgpr_read_b32 v96, a96
v_accvgpr_read_b32 v97, a97
v_accvgpr_read_b32 v98, a98
v_accvgpr_read_b32 v99, a99
v_accvgpr_read_b32 v100, a100
v_accvgpr_read_b32 v101, a101
v_accvgpr_read_b32 v102, a102
v_accvgpr_read_b32 v103, a103
s_waitcnt lgkmcnt(7)
v_pk_mul_f32 v[176:177], v[176:177], v[24:25]
v_pk_mul_f32 v[178:179], v[178:179], v[26:27]
v_pk_fma_f32 v[176:177], v[96:97], v[16:17], v[176:177]
v_pk_fma_f32 v[178:179], v[98:99], v[18:19], v[178:179]
s_waitcnt lgkmcnt(6)
v_pk_mul_f32 v[180:181], v[180:181], v[28:29]
v_pk_mul_f32 v[182:183], v[182:183], v[30:31]
v_pk_fma_f32 v[180:181], v[100:101], v[20:21], v[180:181]
v_pk_fma_f32 v[182:183], v[102:103], v[22:23], v[182:183]
v_cvt_pk_bf16_f32 v176, v176, v177
v_cvt_pk_bf16_f32 v177, v178, v179
v_cvt_pk_bf16_f32 v178, v180, v181
v_cvt_pk_bf16_f32 v179, v182, v183
v_cvt_pk_bf16_f32 v42, v112, v113
v_cvt_pk_bf16_f32 v43, v114, v115
v_cvt_pk_bf16_f32 v74, v124, v125
v_cvt_pk_bf16_f32 v75, v126, v127
v_mfma_f32_16x16x32_bf16 v[104:107], v[176:179], v[120:123], 0
v_mfma_f32_16x16x32_bf16 v[108:111], v[176:179], v[118:121], 0
ds_read_b128 v[208:211], v7 offset:0
ds_read_b128 v[212:215], v7 offset:1024
s_add_u32 s54, s5, 36864
buffer_load_dwordx4 a[48:51], v1, s[60:63], s54 offen offset:2048 sc1
buffer_load_dwordx4 a[52:55], v1, s[60:63], s54 offen offset:3072 sc1
s_add_u32 s54, s5, 40960
buffer_load_dwordx4 a[64:67], v1, s[60:63], s54 offen offset:0 sc1
buffer_load_dwordx4 a[68:71], v1, s[60:63], s54 offen offset:1024 sc1
v_accvgpr_read_b32 v96, a112
v_accvgpr_read_b32 v97, a113
v_accvgpr_read_b32 v98, a114
v_accvgpr_read_b32 v99, a115
v_accvgpr_read_b32 v100, a116
v_accvgpr_read_b32 v101, a117
v_accvgpr_read_b32 v102, a118
v_accvgpr_read_b32 v103, a119
s_waitcnt lgkmcnt(7)
v_pk_mul_f32 v[184:185], v[184:185], v[24:25]
v_pk_mul_f32 v[186:187], v[186:187], v[26:27]
v_pk_fma_f32 v[184:185], v[96:97], v[16:17], v[184:185]
v_pk_fma_f32 v[186:187], v[98:99], v[18:19], v[186:187]
s_waitcnt lgkmcnt(6)
v_pk_mul_f32 v[188:189], v[188:189], v[28:29]
v_pk_mul_f32 v[190:191], v[190:191], v[30:31]
v_pk_fma_f32 v[188:189], v[100:101], v[20:21], v[188:189]
v_pk_fma_f32 v[190:191], v[102:103], v[22:23], v[190:191]
v_cvt_pk_bf16_f32 v184, v184, v185
v_cvt_pk_bf16_f32 v185, v186, v187
v_cvt_pk_bf16_f32 v186, v188, v189
v_cvt_pk_bf16_f32 v187, v190, v191
v_cvt_pk_bf16_f32 v44, v104, v105
v_cvt_pk_bf16_f32 v45, v106, v107
v_cvt_pk_bf16_f32 v76, v108, v109
v_cvt_pk_bf16_f32 v77, v110, v111
v_mfma_f32_16x16x32_bf16 v[112:115], v[184:187], v[120:123], 0
v_mfma_f32_16x16x32_bf16 v[124:127], v[184:187], v[118:121], 0
ds_read_b128 v[216:219], v7 offset:2048
ds_read_b128 v[220:223], v7 offset:3072
s_add_u32 s54, s5, 40960
buffer_load_dwordx4 a[80:83], v1, s[60:63], s54 offen offset:2048 sc1
buffer_load_dwordx4 a[84:87], v1, s[60:63], s54 offen offset:3072 sc1
v_accvgpr_read_b32 v96, a128
v_accvgpr_read_b32 v97, a129
v_accvgpr_read_b32 v98, a130
v_accvgpr_read_b32 v99, a131
v_accvgpr_read_b32 v100, a132
v_accvgpr_read_b32 v101, a133
v_accvgpr_read_b32 v102, a134
v_accvgpr_read_b32 v103, a135
s_waitcnt lgkmcnt(7)
v_pk_mul_f32 v[192:193], v[192:193], v[24:25]
v_pk_mul_f32 v[194:195], v[194:195], v[26:27]
v_pk_fma_f32 v[192:193], v[96:97], v[16:17], v[192:193]
v_pk_fma_f32 v[194:195], v[98:99], v[18:19], v[194:195]
s_waitcnt lgkmcnt(6)
v_pk_mul_f32 v[196:197], v[196:197], v[28:29]
v_pk_mul_f32 v[198:199], v[198:199], v[30:31]
v_pk_fma_f32 v[196:197], v[100:101], v[20:21], v[196:197]
v_pk_fma_f32 v[198:199], v[102:103], v[22:23], v[198:199]
v_cvt_pk_bf16_f32 v192, v192, v193
v_cvt_pk_bf16_f32 v193, v194, v195
v_cvt_pk_bf16_f32 v194, v196, v197
v_cvt_pk_bf16_f32 v195, v198, v199
v_cvt_pk_bf16_f32 v46, v112, v113
v_cvt_pk_bf16_f32 v47, v114, v115
v_cvt_pk_bf16_f32 v78, v124, v125
v_cvt_pk_bf16_f32 v79, v126, v127
v_mfma_f32_16x16x32_bf16 v[104:107], v[192:195], v[120:123], 0
v_mfma_f32_16x16x32_bf16 v[108:111], v[192:195], v[118:121], 0
ds_read_b128 v[224:227], v7 offset:4096
ds_read_b128 v[228:231], v7 offset:5120
v_accvgpr_read_b32 v96, a144
v_accvgpr_read_b32 v97, a145
v_accvgpr_read_b32 v98, a146
v_accvgpr_read_b32 v99, a147
v_accvgpr_read_b32 v100, a148
v_accvgpr_read_b32 v101, a149
v_accvgpr_read_b32 v102, a150
v_accvgpr_read_b32 v103, a151
s_waitcnt lgkmcnt(7)
v_pk_mul_f32 v[200:201], v[200:201], v[24:25]
v_pk_mul_f32 v[202:203], v[202:203], v[26:27]
v_pk_fma_f32 v[200:201], v[96:97], v[16:17], v[200:201]
v_pk_fma_f32 v[202:203], v[98:99], v[18:19], v[202:203]
s_waitcnt lgkmcnt(6)
v_pk_mul_f32 v[204:205], v[204:205], v[28:29]
v_pk_mul_f32 v[206:207], v[206:207], v[30:31]
v_pk_fma_f32 v[204:205], v[100:101], v[20:21], v[204:205]
v_pk_fma_f32 v[206:207], v[102:103], v[22:23], v[206:207]
v_cvt_pk_bf16_f32 v200, v200, v201
v_cvt_pk_bf16_f32 v201, v202, v203
v_cvt_pk_bf16_f32 v202, v204, v205
v_cvt_pk_bf16_f32 v203, v206, v207
v_cvt_pk_bf16_f32 v48, v104, v105
v_cvt_pk_bf16_f32 v49, v106, v107
v_cvt_pk_bf16_f32 v80, v108, v109
v_cvt_pk_bf16_f32 v81, v110, v111
v_mfma_f32_16x16x32_bf16 v[112:115], v[200:203], v[120:123], 0
v_mfma_f32_16x16x32_bf16 v[124:127], v[200:203], v[118:121], 0
ds_read_b128 v[232:235], v7 offset:6144
ds_read_b128 v[236:239], v7 offset:7168
v_accvgpr_read_b32 v96, a160
v_accvgpr_read_b32 v97, a161
v_accvgpr_read_b32 v98, a162
v_accvgpr_read_b32 v99, a163
v_accvgpr_read_b32 v100, a164
v_accvgpr_read_b32 v101, a165
v_accvgpr_read_b32 v102, a166
v_accvgpr_read_b32 v103, a167
s_waitcnt lgkmcnt(7)
v_pk_mul_f32 v[208:209], v[208:209], v[24:25]
v_pk_mul_f32 v[210:211], v[210:211], v[26:27]
v_pk_fma_f32 v[208:209], v[96:97], v[16:17], v[208:209]
v_pk_fma_f32 v[210:211], v[98:99], v[18:19], v[210:211]
s_waitcnt lgkmcnt(6)
v_pk_mul_f32 v[212:213], v[212:213], v[28:29]
v_pk_mul_f32 v[214:215], v[214:215], v[30:31]
v_pk_fma_f32 v[212:213], v[100:101], v[20:21], v[212:213]
v_pk_fma_f32 v[214:215], v[102:103], v[22:23], v[214:215]
v_cvt_pk_bf16_f32 v208, v208, v209
v_cvt_pk_bf16_f32 v209, v210, v211
v_cvt_pk_bf16_f32 v210, v212, v213
v_cvt_pk_bf16_f32 v211, v214, v215
v_cvt_pk_bf16_f32 v50, v112, v113
v_cvt_pk_bf16_f32 v51, v114, v115
v_cvt_pk_bf16_f32 v82, v124, v125
v_cvt_pk_bf16_f32 v83, v126, v127
v_mfma_f32_16x16x32_bf16 v[104:107], v[208:211], v[120:123], 0
v_mfma_f32_16x16x32_bf16 v[108:111], v[208:211], v[118:121], 0
ds_read_b128 v[240:243], v7 offset:8192
ds_read_b128 v[244:247], v7 offset:9216
v_accvgpr_read_b32 v96, a176
v_accvgpr_read_b32 v97, a177
v_accvgpr_read_b32 v98, a178
v_accvgpr_read_b32 v99, a179
v_accvgpr_read_b32 v100, a180
v_accvgpr_read_b32 v101, a181
v_accvgpr_read_b32 v102, a182
v_accvgpr_read_b32 v103, a183
s_waitcnt lgkmcnt(7)
v_pk_mul_f32 v[216:217], v[216:217], v[24:25]
v_pk_mul_f32 v[218:219], v[218:219], v[26:27]
v_pk_fma_f32 v[216:217], v[96:97], v[16:17], v[216:217]
v_pk_fma_f32 v[218:219], v[98:99], v[18:19], v[218:219]
s_waitcnt lgkmcnt(6)
v_pk_mul_f32 v[220:221], v[220:221], v[28:29]
v_pk_mul_f32 v[222:223], v[222:223], v[30:31]
v_pk_fma_f32 v[220:221], v[100:101], v[20:21], v[220:221]
v_pk_fma_f32 v[222:223], v[102:103], v[22:23], v[222:223]
v_cvt_pk_bf16_f32 v216, v216, v217
v_cvt_pk_bf16_f32 v217, v218, v219
v_cvt_pk_bf16_f32 v218, v220, v221
v_cvt_pk_bf16_f32 v219, v222, v223
v_cvt_pk_bf16_f32 v52, v104, v105
v_cvt_pk_bf16_f32 v53, v106, v107
v_cvt_pk_bf16_f32 v84, v108, v109
v_cvt_pk_bf16_f32 v85, v110, v111
v_mfma_f32_16x16x32_bf16 v[112:115], v[216:219], v[120:123], 0
v_mfma_f32_16x16x32_bf16 v[124:127], v[216:219], v[118:121], 0
ds_read_b128 v[248:251], v7 offset:10240
ds_read_b128 v[252:255], v7 offset:11264
v_accvgpr_read_b32 v96, a192
v_accvgpr_read_b32 v97, a193
v_accvgpr_read_b32 v98, a194
v_accvgpr_read_b32 v99, a195
v_accvgpr_read_b32 v100, a196
v_accvgpr_read_b32 v101, a197
v_accvgpr_read_b32 v102, a198
v_accvgpr_read_b32 v103, a199
s_waitcnt lgkmcnt(7)
v_pk_mul_f32 v[224:225], v[224:225], v[24:25]
v_pk_mul_f32 v[226:227], v[226:227], v[26:27]
v_pk_fma_f32 v[224:225], v[96:97], v[16:17], v[224:225]
v_pk_fma_f32 v[226:227], v[98:99], v[18:19], v[226:227]
s_waitcnt lgkmcnt(6)
v_pk_mul_f32 v[228:229], v[228:229], v[28:29]
v_pk_mul_f32 v[230:231], v[230:231], v[30:31]
v_pk_fma_f32 v[228:229], v[100:101], v[20:21], v[228:229]
v_pk_fma_f32 v[230:231], v[102:103], v[22:23], v[230:231]
v_cvt_pk_bf16_f32 v224, v224, v225
v_cvt_pk_bf16_f32 v225, v226, v227
v_cvt_pk_bf16_f32 v226, v228, v229
v_cvt_pk_bf16_f32 v227, v230, v231
v_cvt_pk_bf16_f32 v54, v112, v113
v_cvt_pk_bf16_f32 v55, v114, v115
v_cvt_pk_bf16_f32 v86, v124, v125
v_cvt_pk_bf16_f32 v87, v126, v127
v_mfma_f32_16x16x32_bf16 v[104:107], v[224:227], v[120:123], 0
v_mfma_f32_16x16x32_bf16 v[108:111], v[224:227], v[118:121], 0
v_accvgpr_read_b32 v96, a208
v_accvgpr_read_b32 v97, a209
v_accvgpr_read_b32 v98, a210
v_accvgpr_read_b32 v99, a211
v_accvgpr_read_b32 v100, a212
v_accvgpr_read_b32 v101, a213
v_accvgpr_read_b32 v102, a214
v_accvgpr_read_b32 v103, a215
s_waitcnt lgkmcnt(5)
v_pk_mul_f32 v[232:233], v[232:233], v[24:25]
v_pk_mul_f32 v[234:235], v[234:235], v[26:27]
v_pk_fma_f32 v[232:233], v[96:97], v[16:17], v[232:233]
v_pk_fma_f32 v[234:235], v[98:99], v[18:19], v[234:235]
s_waitcnt lgkmcnt(4)
v_pk_mul_f32 v[236:237], v[236:237], v[28:29]
v_pk_mul_f32 v[238:239], v[238:239], v[30:31]
v_pk_fma_f32 v[236:237], v[100:101], v[20:21], v[236:237]
v_pk_fma_f32 v[238:239], v[102:103], v[22:23], v[238:239]
v_cvt_pk_bf16_f32 v232, v232, v233
v_cvt_pk_bf16_f32 v233, v234, v235
v_cvt_pk_bf16_f32 v234, v236, v237
v_cvt_pk_bf16_f32 v235, v238, v239
v_cvt_pk_bf16_f32 v56, v104, v105
v_cvt_pk_bf16_f32 v57, v106, v107
v_cvt_pk_bf16_f32 v88, v108, v109
v_cvt_pk_bf16_f32 v89, v110, v111
v_mfma_f32_16x16x32_bf16 v[112:115], v[232:235], v[120:123], 0
v_mfma_f32_16x16x32_bf16 v[124:127], v[232:235], v[118:121], 0
v_accvgpr_read_b32 v96, a224
v_accvgpr_read_b32 v97, a225
v_accvgpr_read_b32 v98, a226
v_accvgpr_read_b32 v99, a227
v_accvgpr_read_b32 v100, a228
v_accvgpr_read_b32 v101, a229
v_accvgpr_read_b32 v102, a230
v_accvgpr_read_b32 v103, a231
s_waitcnt lgkmcnt(3)
v_pk_mul_f32 v[240:241], v[240:241], v[24:25]
v_pk_mul_f32 v[242:243], v[242:243], v[26:27]
v_pk_fma_f32 v[240:241], v[96:97], v[16:17], v[240:241]
v_pk_fma_f32 v[242:243], v[98:99], v[18:19], v[242:243]
s_waitcnt lgkmcnt(2)
v_pk_mul_f32 v[244:245], v[244:245], v[28:29]
v_pk_mul_f32 v[246:247], v[246:247], v[30:31]
v_pk_fma_f32 v[244:245], v[100:101], v[20:21], v[244:245]
v_pk_fma_f32 v[246:247], v[102:103], v[22:23], v[246:247]
v_cvt_pk_bf16_f32 v240, v240, v241
v_cvt_pk_bf16_f32 v241, v242, v243
v_cvt_pk_bf16_f32 v242, v244, v245
v_cvt_pk_bf16_f32 v243, v246, v247
v_cvt_pk_bf16_f32 v58, v112, v113
v_cvt_pk_bf16_f32 v59, v114, v115
v_cvt_pk_bf16_f32 v90, v124, v125
v_cvt_pk_bf16_f32 v91, v126, v127
v_mfma_f32_16x16x32_bf16 v[104:107], v[240:243], v[120:123], 0
v_mfma_f32_16x16x32_bf16 v[108:111], v[240:243], v[118:121], 0
v_accvgpr_read_b32 v96, a240
v_accvgpr_read_b32 v97, a241
v_accvgpr_read_b32 v98, a242
v_accvgpr_read_b32 v99, a243
v_accvgpr_read_b32 v100, a244
v_accvgpr_read_b32 v101, a245
v_accvgpr_read_b32 v102, a246
v_accvgpr_read_b32 v103, a247
s_waitcnt lgkmcnt(1)
v_pk_mul_f32 v[248:249], v[248:249], v[24:25]
v_pk_mul_f32 v[250:251], v[250:251], v[26:27]
v_pk_fma_f32 v[248:249], v[96:97], v[16:17], v[248:249]
v_pk_fma_f32 v[250:251], v[98:99], v[18:19], v[250:251]
s_waitcnt lgkmcnt(0)
v_pk_mul_f32 v[252:253], v[252:253], v[28:29]
v_pk_mul_f32 v[254:255], v[254:255], v[30:31]
v_pk_fma_f32 v[252:253], v[100:101], v[20:21], v[252:253]
v_pk_fma_f32 v[254:255], v[102:103], v[22:23], v[254:255]
v_cvt_pk_bf16_f32 v248, v248, v249
v_cvt_pk_bf16_f32 v249, v250, v251
v_cvt_pk_bf16_f32 v250, v252, v253
v_cvt_pk_bf16_f32 v251, v254, v255
v_cvt_pk_bf16_f32 v60, v104, v105
v_cvt_pk_bf16_f32 v61, v106, v107
v_cvt_pk_bf16_f32 v92, v108, v109
v_cvt_pk_bf16_f32 v93, v110, v111
v_mfma_f32_16x16x32_bf16 v[112:115], v[248:251], v[120:123], 0
v_mfma_f32_16x16x32_bf16 v[124:127], v[248:251], v[118:121], 0
s_mov_b32 s44, s5
s_mov_b32 s45, s6
s_mov_b32 s46, s7
s_mov_b32 s49, s4
s_mov_b32 s36, s10
s_mov_b32 s37, s3
v_add_u32 v24, 55296, v1
v_add_u32 v25, 110592, v1
s_barrier
v_cvt_pk_bf16_f32 v62, v112, v113
v_cvt_pk_bf16_f32 v63, v114, v115
v_cvt_pk_bf16_f32 v94, v124, v125
v_cvt_pk_bf16_f32 v95, v126, v127
ds_read_b128 v[96:99], v1 offset:0
ds_read_b128 v[100:103], v1 offset:1024
ds_read_b128 v[104:107], v1 offset:2048
ds_read_b128 v[108:111], v1 offset:3072
ds_read_b128 v[112:115], v1 offset:4096
ds_read_b128 v[116:119], v1 offset:5120
s_add_u32 m0, s11, 24576
s_add_u32 s55, s16, 24576
buffer_load_dwordx4 v1, s[60:63], s55 offen offset:0 sc1 lds
buffer_load_dwordx4 v1, s[60:63], s55 offen offset:1024 sc1 lds
s_add_u32 m0, s36, 32768
s_add_u32 s55, s37, 32768
buffer_load_dwordx4 v1, s[60:63], s55 offen offset:0 sc1 lds
buffer_load_dwordx4 v1, s[60:63], s55 offen offset:1024 sc1 lds
buffer_load_dwordx4 v1, s[60:63], s55 offen offset:2048 sc1 lds
buffer_load_dwordx4 v1, s[60:63], s55 offen offset:3072 sc1 lds
s_waitcnt lgkmcnt(4)
v_mfma_f32_16x16x32_bf16 v[128:131], v[96:99], v[32:35], 0
s_add_u32 m0, s36, 49152
s_add_u32 s55, s37, 49152
v_mfma_f32_16x16x32_bf16 v[128:131], v[100:103], v[36:39], v[128:131]
buffer_load_dwordx4 v1, s[60:63], s55 offen offset:0 sc1 lds
v_mfma_f32_16x16x32_bf16 v[136:139], v[96:99], v[64:67], 0
ds_read_b128 v[120:123], v1 offset:6144
ds_read_b128 v[124:127], v1 offset:7168
v_mfma_f32_16x16x32_bf16 v[136:139], v[100:103], v[68:71], v[136:139]
ds_read_b128 v[96:99], v1 offset:8192
s_waitcnt lgkmcnt(5)
v_mfma_f32_16x16x32_bf16 v[128:131], v[104:107], v[40:43], v[128:131]
buffer_load_dwordx4 v1, s[60:63], s55 offen offset:1024 sc1 lds
v_mfma_f32_16x16x32_bf16 v[128:131], v[108:111], v[44:47], v[128:131]
ds_read_b128 v[100:103], v1 offset:9216
v_mfma_f32_16x16x32_bf16 v[136:139], v[104:107], v[72:75], v[136:139]
v_mfma_f32_16x16x32_bf16 v[136:139], v[108:111], v[76:79], v[136:139]
ds_read_b128 v[104:107], v1 offset:10240
s_waitcnt lgkmcnt(5)
v_mfma_f32_16x16x32_bf16 v[128:131], v[112:115], v[48:51], v[128:131]
buffer_load_dwordx4 v1, s[60:63], s55 offen offset:2048 sc1 lds
v_mfma_f32_16x16x32_bf16 v[128:131], v[116:119], v[52:55], v[128:131]
ds_read_b128 v[108:111], v1 offset:11264
v_mfma_f32_16x16x32_bf16 v[136:139], v[112:115], v[80:83], v[136:139]
v_mfma_f32_16x16x32_bf16 v[136:139], v[116:119], v[84:87], v[136:139]
ds_read_b128 v[112:115], v1 offset:12288
s_waitcnt lgkmcnt(5)
v_mfma_f32_16x16x32_bf16 v[128:131], v[120:123], v[56:59], v[128:131]
buffer_load_dwordx4 v1, s[60:63], s55 offen offset:3072 sc1 lds
v_mfma_f32_16x16x32_bf16 v[128:131], v[124:127], v[60:63], v[128:131]
s_add_u32 m0, s36, 65536
s_add_u32 s55, s37, 65536
v_mfma_f32_16x16x32_bf16 v[136:139], v[120:123], v[88:91], v[136:139]
buffer_load_dwordx4 v1, s[60:63], s55 offen offset:0 sc1 lds
v_mfma_f32_16x16x32_bf16 v[136:139], v[124:127], v[92:95], v[136:139]
ds_read_b128 v[116:119], v1 offset:13312
s_waitcnt lgkmcnt(4)
v_mfma_f32_16x16x32_bf16 v[132:135], v[96:99], v[32:35], 0
ds_read_b128 v[120:123], v1 offset:14336
ds_read_b128 v[124:127], v1 offset:15360
v_mfma_f32_16x16x32_bf16 v[132:135], v[100:103], v[36:39], v[132:135]
buffer_load_dwordx4 v1, s[60:63], s55 offen offset:1024 sc1 lds
v_mfma_f32_16x16x32_bf16 v[140:143], v[96:99], v[64:67], 0
v_mfma_f32_16x16x32_bf16 v[140:143], v[100:103], v[68:71], v[140:143]
ds_read_b128 v[96:99], v1 offset:16384
s_waitcnt lgkmcnt(5)
v_mfma_f32_16x16x32_bf16 v[132:135], v[104:107], v[40:43], v[132:135]
buffer_load_dwordx4 v1, s[60:63], s55 offen offset:2048 sc1 lds
v_mfma_f32_16x16x32_bf16 v[132:135], v[108:111], v[44:47], v[132:135]
ds_read_b128 v[100:103], v1 offset:17408
v_lshrrev_b32 v2, 4, v1
v_mfma_f32_16x16x32_bf16 v[140:143], v[104:107], v[72:75], v[140:143]
v_mov_b32 v30, s76
v_mov_b32 v31, s77
v_mfma_f32_16x16x32_bf16 v[140:143], v[108:111], v[76:79], v[140:143]
ds_read_b128 v[104:107], v1 offset:18432
s_waitcnt lgkmcnt(5)
v_mfma_f32_16x16x32_bf16 v[132:135], v[112:115], v[48:51], v[132:135]
buffer_load_dwordx4 v1, s[60:63], s55 offen offset:3072 sc1 lds
v_mfma_f32_16x16x32_bf16 v[132:135], v[116:119], v[52:55], v[132:135]
s_add_u32 m0, s36, 81920
s_add_u32 s55, s37, 81920
v_mfma_f32_16x16x32_bf16 v[140:143], v[112:115], v[80:83], v[140:143]
ds_read_b128 v[108:111], v1 offset:19456
v_min_u32 v29, 1, v2
v_mfma_f32_16x16x32_bf16 v[140:143], v[116:119], v[84:87], v[140:143]
v_lshlrev_b32 v29, 31, v29
s_waitcnt lgkmcnt(4)
v_mfma_f32_16x16x32_bf16 v[132:135], v[120:123], v[56:59], v[132:135]
buffer_load_dwordx4 v1, s[60:63], s55 offen offset:0 sc1 lds
v_mfma_f32_16x16x32_bf16 v[132:135], v[124:127], v[60:63], v[132:135]
ds_read_b128 v[112:115], v1 offset:20480
ds_read_b128 v[116:119], v1 offset:21504
v_mfma_f32_16x16x32_bf16 v[140:143], v[120:123], v[88:91], v[140:143]
v_and_b32 v3, 7, v2
v_bfe_u32 v4, v2, 3, 2
v_mfma_f32_16x16x32_bf16 v[140:143], v[124:127], v[92:95], v[140:143]
ds_read_b128 v[120:123], v1 offset:22528
s_waitcnt lgkmcnt(5)
v_mfma_f32_16x16x32_bf16 v[144:147], v[96:99], v[32:35], 0
buffer_load_dwordx4 v1, s[60:63], s55 offen offset:1024 sc1 lds
v_mfma_f32_16x16x32_bf16 v[144:147], v[100:103], v[36:39], v[144:147]
s_waitcnt vmcnt(10)
s_barrier
v_mfma_f32_16x16x32_bf16 v[152:155], v[96:99], v[64:67], 0
ds_read_b128 v[124:127], v1 offset:23552
v_cvt_pk_bf16_f32 v128, v128, v129
v_mfma_f32_16x16x32_bf16 v[152:155], v[100:103], v[68:71], v[152:155]
v_lshrrev_b32 v5, 5, v2
s_waitcnt lgkmcnt(4)
v_mfma_f32_16x16x32_bf16 v[144:147], v[104:107], v[40:43], v[144:147]
buffer_load_dwordx4 v1, s[60:63], s55 offen offset:2048 sc1 lds
v_mfma_f32_16x16x32_bf16 v[144:147], v[108:111], v[44:47], v[144:147]
v_cvt_pk_bf16_f32 v129, v130, v131
v_cvt_pk_bf16_f32 v130, v132, v133
v_mfma_f32_16x16x32_bf16 v[152:155], v[104:107], v[72:75], v[152:155]
s_lshl_b32 s52, s80, 1
v_cvt_pk_bf16_f32 v131, v134, v135
v_mfma_f32_16x16x32_bf16 v[152:155], v[108:111], v[76:79], v[152:155]
s_add_u32 s53, s56, 0
s_waitcnt lgkmcnt(2)
v_mfma_f32_16x16x32_bf16 v[144:147], v[112:115], v[48:51], v[144:147]
buffer_load_dwordx4 v1, s[60:63], s55 offen offset:3072 sc1 lds
v_mfma_f32_16x16x32_bf16 v[144:147], v[116:119], v[52:55], v[144:147]
ds_read_b128 v[96:99], v1 offset:24576
ds_read_b128 v[100:103], v1 offset:25600
v_mfma_f32_16x16x32_bf16 v[152:155], v[112:115], v[80:83], v[152:155]
v_add_u32 v5, s52, v5
s_add_u32 m0, s36, 98304
v_mfma_f32_16x16x32_bf16 v[152:155], v[116:119], v[84:87], v[152:155]
s_add_u32 s55, s37, 98304
buffer_store_dwordx4 v[128:131], v1, s[60:63], s53 offen offset:0 sc1
s_waitcnt lgkmcnt(2)
v_mfma_f32_16x16x32_bf16 v[144:147], v[120:123], v[56:59], v[144:147]
buffer_load_dwordx4 v1, s[60:63], s55 offen offset:0 sc1 lds
s_and_b32 s52, s35, 1
v_mfma_f32_16x16x32_bf16 v[144:147], v[124:127], v[60:63], v[144:147]
ds_read_b128 v[104:107], v1 offset:26624
ds_read_b128 v[108:111], v1 offset:27648
v_mfma_f32_16x16x32_bf16 v[152:155], v[120:123], v[88:91], v[152:155]
s_lshl_b32 s52, s52, 3
v_lshl_add_u32 v10, v4, 4, v3
v_mfma_f32_16x16x32_bf16 v[152:155], v[124:127], v[92:95], v[152:155]
v_cvt_pk_bf16_f32 v136, v136, v137
s_waitcnt lgkmcnt(2)
v_mfma_f32_16x16x32_bf16 v[148:151], v[96:99], v[32:35], 0
buffer_load_dwordx4 v1, s[60:63], s55 offen offset:1024 sc1 lds
v_mfma_f32_16x16x32_bf16 v[148:151], v[100:103], v[36:39], v[148:151]
ds_read_b128 v[112:115], v1 offset:28672
ds_read_b128 v[116:119], v1 offset:29696
v_mfma_f32_16x16x32_bf16 v[156:159], v[96:99], v[64:67], 0
v_add_u32 v10, s52, v10
v_cvt_pk_bf16_f32 v137, v138, v139
v_mfma_f32_16x16x32_bf16 v[156:159], v[100:103], v[68:71], v[156:159]
v_cvt_pk_bf16_f32 v138, v140, v141
s_waitcnt lgkmcnt(2)
v_mfma_f32_16x16x32_bf16 v[148:151], v[104:107], v[40:43], v[148:151]
buffer_load_dwordx4 v1, s[60:63], s55 offen offset:2048 sc1 lds
v_lshlrev_b32 v10, 4, v10
v_cvt_pk_bf16_f32 v139, v142, v143
s_add_u32 s53, s56, 14336
v_mfma_f32_16x16x32_bf16 v[148:151], v[108:111], v[44:47], v[148:151]
ds_read_b128 v[120:123], v1 offset:30720
ds_read_b128 v[124:127], v1 offset:31744
v_mfma_f32_16x16x32_bf16 v[156:159], v[104:107], v[72:75], v[156:159]
v_lshl_add_u32 v10, v5, 10, v10
v_lshrrev_b32 v12, 5, v2
v_mfma_f32_16x16x32_bf16 v[156:159], v[108:111], v[76:79], v[156:159]
buffer_store_dwordx4 v[136:139], v1, s[60:63], s53 offen offset:0 sc1
s_waitcnt lgkmcnt(2)
v_mfma_f32_16x16x32_bf16 v[148:151], v[112:115], v[48:51], v[148:151]
buffer_load_dwordx4 v1, s[60:63], s55 offen offset:3072 sc1 lds
v_mfma_f32_16x16x32_bf16 v[148:151], v[116:119], v[52:55], v[148:151]
ds_read_b128 v[96:99], v1 offset:32768
ds_read_b128 v[100:103], v1 offset:33792
v_mfma_f32_16x16x32_bf16 v[156:159], v[112:115], v[80:83], v[156:159]
v_lshlrev_b32 v12, 6, v12
v_lshl_add_u32 v12, v4, 3, v12
v_mfma_f32_16x16x32_bf16 v[156:159], v[116:119], v[84:87], v[156:159]
s_add_u32 m0, s36, 114688
s_waitcnt lgkmcnt(2)
v_mfma_f32_16x16x32_bf16 v[148:151], v[120:123], v[56:59], v[148:151]
ds_read_b128 v[104:107], v1 offset:34816
s_add_u32 s55, s37, 114688
v_mfma_f32_16x16x32_bf16 v[148:151], v[124:127], v[60:63], v[148:151]
ds_read_b128 v[108:111], v1 offset:35840
v_add_u32 v12, s46, v12
v_mfma_f32_16x16x32_bf16 v[156:159], v[120:123], v[88:91], v[156:159]
buffer_load_dwordx4 v1, s[60:63], s55 offen offset:0 sc1 lds
v_mfma_f32_16x16x32_bf16 v[156:159], v[124:127], v[92:95], v[156:159]
v_lshl_add_u32 v11, v3, 8, v12
s_waitcnt lgkmcnt(2)
v_mfma_f32_16x16x32_bf16 v[128:131], v[96:99], v[32:35], 0
ds_read_b128 v[112:115], v1 offset:36864
ds_read_b128 v[116:119], v1 offset:37888
v_mfma_f32_16x16x32_bf16 v[128:131], v[100:103], v[36:39], v[128:131]
v_mul_u32_u24 v15, 896, v3
v_lshl_add_u32 v15, v5, 6, v15
v_mfma_f32_16x16x32_bf16 v[136:139], v[96:99], v[64:67], 0
buffer_load_dwordx4 v1, s[60:63], s55 offen offset:1024 sc1 lds
v_mfma_f32_16x16x32_bf16 v[136:139], v[100:103], v[68:71], v[136:139]
v_cvt_pk_bf16_f32 v144, v144, v145
s_waitcnt lgkmcnt(2)
v_mfma_f32_16x16x32_bf16 v[128:131], v[104:107], v[40:43], v[128:131]
ds_read_b128 v[120:123], v1 offset:38912
ds_read_b128 v[124:127], v1 offset:39936
v_mfma_f32_16x16x32_bf16 v[128:131], v[108:111], v[44:47], v[128:131]
v_lshl_add_u32 v15, v4, 3, v15
v_cvt_pk_bf16_f32 v145, v146, v147
v_mfma_f32_16x16x32_bf16 v[136:139], v[104:107], v[72:75], v[136:139]
buffer_load_dwordx4 v1, s[60:63], s55 offen offset:2048 sc1 lds
v_mfma_f32_16x16x32_bf16 v[136:139], v[108:111], v[76:79], v[136:139]
v_cvt_pk_bf16_f32 v146, v148, v149
s_waitcnt lgkmcnt(2)
v_mfma_f32_16x16x32_bf16 v[128:131], v[112:115], v[48:51], v[128:131]
v_add_u32 v15, 107520, v15
v_cvt_pk_bf16_f32 v147, v150, v151
s_add_u32 s53, s56, 0
v_mfma_f32_16x16x32_bf16 v[128:131], v[116:119], v[52:55], v[128:131]
ds_read_b128 v[96:99], v1 offset:40960
ds_read_b128 v[100:103], v1 offset:41984
v_mfma_f32_16x16x32_bf16 v[136:139], v[112:115], v[80:83], v[136:139]
buffer_load_dwordx4 v1, s[60:63], s55 offen offset:3072 sc1 lds
v_lshrrev_b32 v8, 4, v2
v_mfma_f32_16x16x32_bf16 v[136:139], v[116:119], v[84:87], v[136:139]
buffer_store_dwordx4 v[144:147], v1, s[60:63], s53 offen offset:1024 sc1
s_waitcnt lgkmcnt(2)
v_mfma_f32_16x16x32_bf16 v[128:131], v[120:123], v[56:59], v[128:131]
ds_read_b128 v[104:107], v1 offset:43008
v_mul_u32_u24 v8, 896, v8
v_mfma_f32_16x16x32_bf16 v[128:131], v[124:127], v[60:63], v[128:131]
ds_read_b128 v[108:111], v1 offset:44032
v_bfe_u32 v9, v2, 3, 1
v_mfma_f32_16x16x32_bf16 v[136:139], v[120:123], v[88:91], v[136:139]
v_mul_u32_u24 v9, 448, v9
v_cvt_pk_bf16_f32 v152, v152, v153
v_mfma_f32_16x16x32_bf16 v[136:139], v[124:127], v[92:95], v[136:139]
ds_read_b128 v[112:115], v1 offset:45056
s_waitcnt lgkmcnt(3)
v_mfma_f32_16x16x32_bf16 v[132:135], v[96:99], v[32:35], 0
ds_read_b128 v[116:119], v1 offset:46080
v_add_u32 v8, v8, v9
v_mfma_f32_16x16x32_bf16 v[132:135], v[100:103], v[36:39], v[132:135]
s_waitcnt vmcnt(11)
s_barrier
v_mfma_f32_16x16x32_bf16 v[140:143], v[96:99], v[64:67], 0
v_cvt_pk_bf16_f32 v153, v154, v155
s_add_u32 m0, s36, 131072
v_mfma_f32_16x16x32_bf16 v[140:143], v[100:103], v[68:71], v[140:143]
s_add_u32 s55, s37, 131072
v_cvt_pk_bf16_f32 v154, v156, v157
s_waitcnt lgkmcnt(2)
v_mfma_f32_16x16x32_bf16 v[132:135], v[104:107], v[40:43], v[132:135]
buffer_load_dwordx4 v1, s[60:63], s55 offen offset:0 sc1 lds
v_and_b32 v9, 7, v2
v_cvt_pk_bf16_f32 v155, v158, v159
s_add_u32 s53, s56, 14336
v_mfma_f32_16x16x32_bf16 v[132:135], v[108:111], v[44:47], v[132:135]
ds_read_b128 v[120:123], v1 offset:47104
ds_read_b128 v[124:127], v1 offset:48128
v_mfma_f32_16x16x32_bf16 v[140:143], v[104:107], v[72:75], v[140:143]
v_lshl_add_u32 v8, v9, 4, v8
s_lshl_b32 s52, s80, 7
v_mfma_f32_16x16x32_bf16 v[140:143], v[108:111], v[76:79], v[140:143]
buffer_store_dwordx4 v[152:155], v1, s[60:63], s53 offen offset:1024 sc1
s_waitcnt lgkmcnt(2)
v_mfma_f32_16x16x32_bf16 v[132:135], v[112:115], v[48:51], v[132:135]
buffer_load_dwordx4 v1, s[60:63], s55 offen offset:1024 sc1 lds
v_mfma_f32_16x16x32_bf16 v[132:135], v[116:119], v[52:55], v[132:135]
ds_read_b128 v[96:99], v1 offset:49152
ds_read_b128 v[100:103], v1 offset:50176
v_mfma_f32_16x16x32_bf16 v[140:143], v[112:115], v[80:83], v[140:143]
v_add_u32 v8, s52, v8
v_cmp_eq_u32 vcc, 7, v5
v_mfma_f32_16x16x32_bf16 v[140:143], v[116:119], v[84:87], v[140:143]
ds_read_b128 v[104:107], v1 offset:51200
s_waitcnt lgkmcnt(3)
v_mfma_f32_16x16x32_bf16 v[132:135], v[120:123], v[56:59], v[132:135]
buffer_load_dwordx4 v1, s[60:63], s55 offen offset:2048 sc1 lds
v_mfma_f32_16x16x32_bf16 v[132:135], v[124:127], v[60:63], v[132:135]
ds_read_b128 v[108:111], v1 offset:52224
v_lshlrev_b32 v14, 5, v3
v_mfma_f32_16x16x32_bf16 v[140:143], v[120:123], v[88:91], v[140:143]
ds_read_b128 v[112:115], v1 offset:53248
v_add_u32 v14, 157696, v14
v_mfma_f32_16x16x32_bf16 v[140:143], v[124:127], v[92:95], v[140:143]
ds_read_b128 v[116:119], v1 offset:54272
s_waitcnt lgkmcnt(4)
v_mfma_f32_16x16x32_bf16 v[144:147], v[96:99], v[32:35], 0
buffer_load_dwordx4 v1, s[60:63], s55 offen offset:3072 sc1 lds
v_mfma_f32_16x16x32_bf16 v[144:147], v[100:103], v[36:39], v[144:147]
v_cndmask_b32 v4, 0, -1, vcc
v_lshlrev_b32 v4, 30, v4
v_mfma_f32_16x16x32_bf16 v[152:155], v[96:99], v[64:67], 0
ds_read_b128 v[120:123], v24 offset:0
v_cvt_pk_bf16_f32 v128, v128, v129
v_mfma_f32_16x16x32_bf16 v[152:155], v[100:103], v[68:71], v[152:155]
s_add_u32 m0, s36, 147456
s_waitcnt lgkmcnt(3)
v_mfma_f32_16x16x32_bf16 v[144:147], v[104:107], v[40:43], v[144:147]
ds_read_b128 v[124:127], v24 offset:1024
s_add_u32 s55, s37, 147456
v_mfma_f32_16x16x32_bf16 v[144:147], v[108:111], v[44:47], v[144:147]
v_or_b32 v10, v10, v4
v_cvt_pk_bf16_f32 v129, v130, v131
v_mfma_f32_16x16x32_bf16 v[152:155], v[104:107], v[72:75], v[152:155]
buffer_load_dwordx4 v1, s[60:63], s55 offen offset:0 sc1 lds
v_mfma_f32_16x16x32_bf16 v[152:155], v[108:111], v[76:79], v[152:155]
v_cvt_pk_bf16_f32 v130, v132, v133
s_waitcnt lgkmcnt(2)
v_mfma_f32_16x16x32_bf16 v[144:147], v[112:115], v[48:51], v[144:147]
v_cndmask_b32 v28, 1.0, 0, vcc
v_cvt_pk_bf16_f32 v131, v134, v135
s_add_u32 s53, s56, 0
v_mfma_f32_16x16x32_bf16 v[144:147], v[116:119], v[52:55], v[144:147]
ds_read_b128 v[96:99], v24 offset:2048
ds_read_b128 v[100:103], v24 offset:3072
v_mfma_f32_16x16x32_bf16 v[152:155], v[112:115], v[80:83], v[152:155]
buffer_load_dwordx4 v1, s[60:63], s55 offen offset:1024 sc1 lds
v_mov_b32 v4, 158208
v_mfma_f32_16x16x32_bf16 v[152:155], v[116:119], v[84:87], v[152:155]
buffer_store_dwordx4 v[128:131], v1, s[60:63], s53 offen offset:2048 sc1
s_waitcnt lgkmcnt(2)
v_mfma_f32_16x16x32_bf16 v[144:147], v[120:123], v[56:59], v[144:147]
ds_read_b128 v[104:107], v24 offset:4096
v_cndmask_b32 v15, v15, v4, vcc
v_mfma_f32_16x16x32_bf16 v[144:147], v[124:127], v[60:63], v[144:147]
ds_read_b128 v[108:111], v24 offset:5120
s_lshl_b32 s52, s80, 3
v_mfma_f32_16x16x32_bf16 v[152:155], v[120:123], v[88:91], v[152:155]
buffer_load_dwordx4 v1, s[60:63], s55 offen offset:2048 sc1 lds
v_mfma_f32_16x16x32_bf16 v[152:155], v[124:127], v[92:95], v[152:155]
v_add_u32 v13, s52, v14
s_waitcnt lgkmcnt(2)
v_mfma_f32_16x16x32_bf16 v[148:151], v[96:99], v[32:35], 0
ds_read_b128 v[112:115], v24 offset:6144
v_cvt_pk_bf16_f32 v136, v136, v137
v_mfma_f32_16x16x32_bf16 v[148:151], v[100:103], v[36:39], v[148:151]
ds_read_b128 v[116:119], v24 offset:7168
v_cvt_pk_bf16_f32 v137, v138, v139
v_mfma_f32_16x16x32_bf16 v[156:159], v[96:99], v[64:67], 0
buffer_load_dwordx4 v1, s[60:63], s55 offen offset:3072 sc1 lds
v_mfma_f32_16x16x32_bf16 v[156:159], v[100:103], v[68:71], v[156:159]
v_cvt_pk_bf16_f32 v138, v140, v141
s_waitcnt lgkmcnt(2)
v_mfma_f32_16x16x32_bf16 v[148:151], v[104:107], v[40:43], v[148:151]
v_cvt_pk_bf16_f32 v139, v142, v143
s_add_u32 s53, s56, 14336
v_mfma_f32_16x16x32_bf16 v[148:151], v[108:111], v[44:47], v[148:151]
ds_read_b128 v[120:123], v24 offset:8192
ds_read_b128 v[124:127], v24 offset:9216
v_mfma_f32_16x16x32_bf16 v[156:159], v[104:107], v[72:75], v[156:159]
buffer_store_dwordx4 v[136:139], v1, s[60:63], s53 offen offset:2048 sc1
v_mfma_f32_16x16x32_bf16 v[156:159], v[108:111], v[76:79], v[156:159]
s_add_u32 m0, s36, 0
s_waitcnt lgkmcnt(2)
v_mfma_f32_16x16x32_bf16 v[148:151], v[112:115], v[48:51], v[148:151]
ds_read_b128 v[96:99], v24 offset:10240
s_add_u32 s55, s37, 163840
v_mfma_f32_16x16x32_bf16 v[148:151], v[116:119], v[52:55], v[148:151]
ds_read_b128 v[100:103], v24 offset:11264
ds_read_b128 v[104:107], v24 offset:12288
v_mfma_f32_16x16x32_bf16 v[156:159], v[112:115], v[80:83], v[156:159]
buffer_load_dwordx4 v1, s[60:63], s55 offen offset:0 sc1 lds
v_mfma_f32_16x16x32_bf16 v[156:159], v[116:119], v[84:87], v[156:159]
ds_read_b128 v[108:111], v24 offset:13312
s_waitcnt lgkmcnt(4)
v_mfma_f32_16x16x32_bf16 v[148:151], v[120:123], v[56:59], v[148:151]
ds_read_b128 v[112:115], v24 offset:14336
ds_read_b128 v[116:119], v24 offset:15360
v_mfma_f32_16x16x32_bf16 v[148:151], v[124:127], v[60:63], v[148:151]
v_mfma_f32_16x16x32_bf16 v[156:159], v[120:123], v[88:91], v[156:159]
v_mfma_f32_16x16x32_bf16 v[156:159], v[124:127], v[92:95], v[156:159]
ds_read_b128 v[120:123], v24 offset:16384
s_waitcnt lgkmcnt(5)
v_mfma_f32_16x16x32_bf16 v[128:131], v[96:99], v[32:35], 0
buffer_load_dwordx4 v1, s[60:63], s55 offen offset:1024 sc1 lds
v_mfma_f32_16x16x32_bf16 v[128:131], v[100:103], v[36:39], v[128:131]
ds_read_b128 v[124:127], v24 offset:17408
v_mfma_f32_16x16x32_bf16 v[136:139], v[96:99], v[64:67], 0
v_cvt_pk_bf16_f32 v144, v144, v145
v_cvt_pk_bf16_f32 v145, v146, v147
v_mfma_f32_16x16x32_bf16 v[136:139], v[100:103], v[68:71], v[136:139]
v_cvt_pk_bf16_f32 v146, v148, v149
s_waitcnt lgkmcnt(4)
v_mfma_f32_16x16x32_bf16 v[128:131], v[104:107], v[40:43], v[128:131]
v_cvt_pk_bf16_f32 v147, v150, v151
s_add_u32 s53, s56, 0
v_mfma_f32_16x16x32_bf16 v[128:131], v[108:111], v[44:47], v[128:131]
ds_read_b128 v[96:99], v24 offset:18432
ds_read_b128 v[100:103], v24 offset:19456
v_mfma_f32_16x16x32_bf16 v[136:139], v[104:107], v[72:75], v[136:139]
buffer_load_dwordx4 v1, s[60:63], s55 offen offset:2048 sc1 lds
v_mfma_f32_16x16x32_bf16 v[136:139], v[108:111], v[76:79], v[136:139]
ds_read_b128 v[104:107], v24 offset:20480
s_waitcnt lgkmcnt(5)
v_mfma_f32_16x16x32_bf16 v[128:131], v[112:115], v[48:51], v[128:131]
buffer_store_dwordx4 v[144:147], v1, s[60:63], s53 offen offset:3072 sc1
v_mfma_f32_16x16x32_bf16 v[128:131], v[116:119], v[52:55], v[128:131]
ds_read_b128 v[108:111], v24 offset:21504
v_mfma_f32_16x16x32_bf16 v[136:139], v[112:115], v[80:83], v[136:139]
v_mfma_f32_16x16x32_bf16 v[136:139], v[116:119], v[84:87], v[136:139]
ds_read_b128 v[112:115], v24 offset:22528
s_waitcnt lgkmcnt(5)
v_mfma_f32_16x16x32_bf16 v[128:131], v[120:123], v[56:59], v[128:131]
buffer_load_dwordx4 v1, s[60:63], s55 offen offset:3072 sc1 lds
v_mfma_f32_16x16x32_bf16 v[128:131], v[124:127], v[60:63], v[128:131]
ds_read_b128 v[116:119], v24 offset:23552
v_mfma_f32_16x16x32_bf16 v[136:139], v[120:123], v[88:91], v[136:139]
v_cvt_pk_bf16_f32 v152, v152, v153
v_cvt_pk_bf16_f32 v153, v154, v155
v_mfma_f32_16x16x32_bf16 v[136:139], v[124:127], v[92:95], v[136:139]
v_cvt_pk_bf16_f32 v154, v156, v157
s_waitcnt lgkmcnt(4)
v_mfma_f32_16x16x32_bf16 v[132:135], v[96:99], v[32:35], 0
v_cvt_pk_bf16_f32 v155, v158, v159
s_add_u32 s53, s56, 14336
v_mfma_f32_16x16x32_bf16 v[132:135], v[100:103], v[36:39], v[132:135]
s_add_u32 m0, s36, 16384
s_add_u32 s55, s37, 180224
v_mfma_f32_16x16x32_bf16 v[140:143], v[96:99], v[64:67], 0
ds_read_b128 v[120:123], v24 offset:24576
ds_read_b128 v[124:127], v24 offset:25600
v_mfma_f32_16x16x32_bf16 v[140:143], v[100:103], v[68:71], v[140:143]
ds_read_b128 v[96:99], v24 offset:26624
s_waitcnt lgkmcnt(5)
v_mfma_f32_16x16x32_bf16 v[132:135], v[104:107], v[40:43], v[132:135]
buffer_load_dwordx4 v1, s[60:63], s55 offen offset:0 sc1 lds
v_mfma_f32_16x16x32_bf16 v[132:135], v[108:111], v[44:47], v[132:135]
buffer_store_dwordx4 v[152:155], v1, s[60:63], s53 offen offset:3072 sc1
v_mfma_f32_16x16x32_bf16 v[140:143], v[104:107], v[72:75], v[140:143]
ds_read_b128 v[100:103], v24 offset:27648
v_mfma_f32_16x16x32_bf16 v[140:143], v[108:111], v[76:79], v[140:143]
ds_read_b128 v[104:107], v24 offset:28672
s_waitcnt lgkmcnt(5)
v_mfma_f32_16x16x32_bf16 v[132:135], v[112:115], v[48:51], v[132:135]
ds_read_b128 v[108:111], v24 offset:29696
v_mfma_f32_16x16x32_bf16 v[132:135], v[116:119], v[52:55], v[132:135]
v_mfma_f32_16x16x32_bf16 v[140:143], v[112:115], v[80:83], v[140:143]
buffer_load_dwordx4 v1, s[60:63], s55 offen offset:1024 sc1 lds
v_mfma_f32_16x16x32_bf16 v[140:143], v[116:119], v[84:87], v[140:143]
ds_read_b128 v[112:115], v24 offset:30720
s_waitcnt lgkmcnt(5)
v_mfma_f32_16x16x32_bf16 v[132:135], v[120:123], v[56:59], v[132:135]
ds_read_b128 v[116:119], v24 offset:31744
v_mfma_f32_16x16x32_bf16 v[132:135], v[124:127], v[60:63], v[132:135]
v_mfma_f32_16x16x32_bf16 v[140:143], v[120:123], v[88:91], v[140:143]
v_mfma_f32_16x16x32_bf16 v[140:143], v[124:127], v[92:95], v[140:143]
ds_read_b128 v[120:123], v24 offset:32768
s_waitcnt lgkmcnt(5)
v_mfma_f32_16x16x32_bf16 v[144:147], v[96:99], v[32:35], 0
buffer_load_dwordx4 v1, s[60:63], s55 offen offset:2048 sc1 lds
v_mfma_f32_16x16x32_bf16 v[144:147], v[100:103], v[36:39], v[144:147]
ds_read_b128 v[124:127], v24 offset:33792
v_mfma_f32_16x16x32_bf16 v[152:155], v[96:99], v[64:67], 0
v_cvt_pk_bf16_f32 v128, v128, v129
v_cvt_pk_bf16_f32 v129, v130, v131
v_mfma_f32_16x16x32_bf16 v[152:155], v[100:103], v[68:71], v[152:155]
v_cvt_pk_bf16_f32 v130, v132, v133
s_waitcnt lgkmcnt(4)
v_mfma_f32_16x16x32_bf16 v[144:147], v[104:107], v[40:43], v[144:147]
v_cvt_pk_bf16_f32 v131, v134, v135
s_add_u32 s53, s56, 4096
v_mfma_f32_16x16x32_bf16 v[144:147], v[108:111], v[44:47], v[144:147]
ds_read_b128 v[96:99], v24 offset:34816
ds_read_b128 v[100:103], v24 offset:35840
v_mfma_f32_16x16x32_bf16 v[152:155], v[104:107], v[72:75], v[152:155]
buffer_load_dwordx4 v1, s[60:63], s55 offen offset:3072 sc1 lds
v_mfma_f32_16x16x32_bf16 v[152:155], v[108:111], v[76:79], v[152:155]
ds_read_b128 v[104:107], v24 offset:36864
s_waitcnt lgkmcnt(5)
v_mfma_f32_16x16x32_bf16 v[144:147], v[112:115], v[48:51], v[144:147]
buffer_store_dwordx4 v[128:131], v1, s[60:63], s53 offen offset:0 sc1
v_mfma_f32_16x16x32_bf16 v[144:147], v[116:119], v[52:55], v[144:147]
ds_read_b128 v[108:111], v24 offset:37888
v_mfma_f32_16x16x32_bf16 v[152:155], v[112:115], v[80:83], v[152:155]
v_mfma_f32_16x16x32_bf16 v[152:155], v[116:119], v[84:87], v[152:155]
ds_read_b128 v[112:115], v24 offset:38912
s_waitcnt lgkmcnt(5)
v_mfma_f32_16x16x32_bf16 v[144:147], v[120:123], v[56:59], v[144:147]
ds_read_b128 v[116:119], v24 offset:39936
v_mfma_f32_16x16x32_bf16 v[144:147], v[124:127], v[60:63], v[144:147]
v_mfma_f32_16x16x32_bf16 v[152:155], v[120:123], v[88:91], v[152:155]
v_cvt_pk_bf16_f32 v136, v136, v137
v_cvt_pk_bf16_f32 v137, v138, v139
v_mfma_f32_16x16x32_bf16 v[152:155], v[124:127], v[92:95], v[152:155]
v_cvt_pk_bf16_f32 v138, v140, v141
s_waitcnt lgkmcnt(4)
v_mfma_f32_16x16x32_bf16 v[148:151], v[96:99], v[32:35], 0
v_cvt_pk_bf16_f32 v139, v142, v143
s_add_u32 s53, s56, 18432
v_mfma_f32_16x16x32_bf16 v[148:151], v[100:103], v[36:39], v[148:151]
s_waitcnt vmcnt(12)
s_barrier
v_mfma_f32_16x16x32_bf16 v[156:159], v[96:99], v[64:67], 0
s_add_u32 m0, s36, 32768
s_add_u32 s55, s37, 196608
v_mfma_f32_16x16x32_bf16 v[156:159], v[100:103], v[68:71], v[156:159]
ds_read_b128 v[120:123], v24 offset:40960
s_waitcnt lgkmcnt(3)
v_mfma_f32_16x16x32_bf16 v[148:151], v[104:107], v[40:43], v[148:151]
buffer_load_dwordx4 v1, s[60:63], s55 offen offset:0 sc1 lds
v_mfma_f32_16x16x32_bf16 v[148:151], v[108:111], v[44:47], v[148:151]
ds_read_b128 v[124:127], v24 offset:41984
ds_read_b128 v[96:99], v24 offset:43008
v_mfma_f32_16x16x32_bf16 v[156:159], v[104:107], v[72:75], v[156:159]
buffer_store_dwordx4 v[136:139], v1, s[60:63], s53 offen offset:0 sc1
v_mfma_f32_16x16x32_bf16 v[156:159], v[108:111], v[76:79], v[156:159]
ds_read_b128 v[100:103], v24 offset:44032
s_waitcnt lgkmcnt(4)
v_mfma_f32_16x16x32_bf16 v[148:151], v[112:115], v[48:51], v[148:151]
ds_read_b128 v[104:107], v24 offset:45056
ds_read_b128 v[108:111], v24 offset:46080
v_mfma_f32_16x16x32_bf16 v[148:151], v[116:119], v[52:55], v[148:151]
v_mfma_f32_16x16x32_bf16 v[156:159], v[112:115], v[80:83], v[156:159]
buffer_load_dwordx4 v1, s[60:63], s55 offen offset:1024 sc1 lds
v_mfma_f32_16x16x32_bf16 v[156:159], v[116:119], v[84:87], v[156:159]
ds_read_b128 v[112:115], v24 offset:47104
s_waitcnt lgkmcnt(5)
v_mfma_f32_16x16x32_bf16 v[148:151], v[120:123], v[56:59], v[148:151]
ds_read_b128 v[116:119], v24 offset:48128
v_mfma_f32_16x16x32_bf16 v[148:151], v[124:127], v[60:63], v[148:151]
v_mfma_f32_16x16x32_bf16 v[156:159], v[120:123], v[88:91], v[156:159]
v_mfma_f32_16x16x32_bf16 v[156:159], v[124:127], v[92:95], v[156:159]
ds_read_b128 v[120:123], v24 offset:49152
s_waitcnt lgkmcnt(5)
v_mfma_f32_16x16x32_bf16 v[128:131], v[96:99], v[32:35], 0
buffer_load_dwordx4 v1, s[60:63], s55 offen offset:2048 sc1 lds
v_mfma_f32_16x16x32_bf16 v[128:131], v[100:103], v[36:39], v[128:131]
ds_read_b128 v[124:127], v24 offset:50176
v_mfma_f32_16x16x32_bf16 v[136:139], v[96:99], v[64:67], 0
v_cvt_pk_bf16_f32 v144, v144, v145
v_cvt_pk_bf16_f32 v145, v146, v147
v_mfma_f32_16x16x32_bf16 v[136:139], v[100:103], v[68:71], v[136:139]
v_cvt_pk_bf16_f32 v146, v148, v149
s_waitcnt lgkmcnt(4)
v_mfma_f32_16x16x32_bf16 v[128:131], v[104:107], v[40:43], v[128:131]
v_cvt_pk_bf16_f32 v147, v150, v151
s_add_u32 s53, s56, 4096
v_mfma_f32_16x16x32_bf16 v[128:131], v[108:111], v[44:47], v[128:131]
ds_read_b128 v[96:99], v24 offset:51200
ds_read_b128 v[100:103], v24 offset:52224
v_mfma_f32_16x16x32_bf16 v[136:139], v[104:107], v[72:75], v[136:139]
buffer_load_dwordx4 v1, s[60:63], s55 offen offset:3072 sc1 lds
v_mfma_f32_16x16x32_bf16 v[136:139], v[108:111], v[76:79], v[136:139]
ds_read_b128 v[104:107], v24 offset:53248
s_waitcnt lgkmcnt(5)
v_mfma_f32_16x16x32_bf16 v[128:131], v[112:115], v[48:51], v[128:131]
buffer_store_dwordx4 v[144:147], v1, s[60:63], s53 offen offset:1024 sc1
v_mfma_f32_16x16x32_bf16 v[128:131], v[116:119], v[52:55], v[128:131]
ds_read_b128 v[108:111], v24 offset:54272
v_mfma_f32_16x16x32_bf16 v[136:139], v[112:115], v[80:83], v[136:139]
v_mfma_f32_16x16x32_bf16 v[136:139], v[116:119], v[84:87], v[136:139]
s_add_u32 m0, s36, 49152
s_waitcnt lgkmcnt(4)
v_mfma_f32_16x16x32_bf16 v[128:131], v[120:123], v[56:59], v[128:131]
ds_read_b128 v[112:115], v25 offset:0
s_add_u32 s55, s37, 212992
v_mfma_f32_16x16x32_bf16 v[128:131], v[124:127], v[60:63], v[128:131]
ds_read_b128 v[116:119], v25 offset:1024
v_mfma_f32_16x16x32_bf16 v[136:139], v[120:123], v[88:91], v[136:139]
buffer_load_dwordx4 v1, s[60:63], s55 offen offset:0 sc1 lds
v_mfma_f32_16x16x32_bf16 v[136:139], v[124:127], v[92:95], v[136:139]
v_cvt_pk_bf16_f32 v152, v152, v153
s_waitcnt lgkmcnt(4)
v_mfma_f32_16x16x32_bf16 v[132:135], v[96:99], v[32:35], 0
v_cvt_pk_bf16_f32 v153, v154, v155
v_cvt_pk_bf16_f32 v154, v156, v157
v_mfma_f32_16x16x32_bf16 v[132:135], v[100:103], v[36:39], v[132:135]
v_cvt_pk_bf16_f32 v155, v158, v159
s_add_u32 s53, s56, 18432
v_mfma_f32_16x16x32_bf16 v[140:143], v[96:99], v[64:67], 0
ds_read_b128 v[120:123], v25 offset:2048
ds_read_b128 v[124:127], v25 offset:3072
v_mfma_f32_16x16x32_bf16 v[140:143], v[100:103], v[68:71], v[140:143]
ds_read_b128 v[96:99], v25 offset:4096
s_waitcnt lgkmcnt(5)
v_mfma_f32_16x16x32_bf16 v[132:135], v[104:107], v[40:43], v[132:135]
buffer_load_dwordx4 v1, s[60:63], s55 offen offset:1024 sc1 lds
v_mfma_f32_16x16x32_bf16 v[132:135], v[108:111], v[44:47], v[132:135]
buffer_store_dwordx4 v[152:155], v1, s[60:63], s53 offen offset:1024 sc1
v_mfma_f32_16x16x32_bf16 v[140:143], v[104:107], v[72:75], v[140:143]
ds_read_b128 v[100:103], v25 offset:5120
v_mfma_f32_16x16x32_bf16 v[140:143], v[108:111], v[76:79], v[140:143]
ds_read_b128 v[104:107], v25 offset:6144
s_waitcnt lgkmcnt(5)
v_mfma_f32_16x16x32_bf16 v[132:135], v[112:115], v[48:51], v[132:135]
ds_read_b128 v[108:111], v25 offset:7168
v_mfma_f32_16x16x32_bf16 v[132:135], v[116:119], v[52:55], v[132:135]
v_mfma_f32_16x16x32_bf16 v[140:143], v[112:115], v[80:83], v[140:143]
buffer_load_dwordx4 v1, s[60:63], s55 offen offset:2048 sc1 lds
v_mfma_f32_16x16x32_bf16 v[140:143], v[116:119], v[84:87], v[140:143]
ds_read_b128 v[112:115], v25 offset:8192
s_waitcnt lgkmcnt(5)
v_mfma_f32_16x16x32_bf16 v[132:135], v[120:123], v[56:59], v[132:135]
ds_read_b128 v[116:119], v25 offset:9216
v_mfma_f32_16x16x32_bf16 v[132:135], v[124:127], v[60:63], v[132:135]
v_mfma_f32_16x16x32_bf16 v[140:143], v[120:123], v[88:91], v[140:143]
v_mfma_f32_16x16x32_bf16 v[140:143], v[124:127], v[92:95], v[140:143]
ds_read_b128 v[120:123], v25 offset:10240
s_waitcnt lgkmcnt(5)
v_mfma_f32_16x16x32_bf16 v[144:147], v[96:99], v[32:35], 0
buffer_load_dwordx4 v1, s[60:63], s55 offen offset:3072 sc1 lds
v_mfma_f32_16x16x32_bf16 v[144:147], v[100:103], v[36:39], v[144:147]
ds_read_b128 v[124:127], v25 offset:11264
v_mfma_f32_16x16x32_bf16 v[152:155], v[96:99], v[64:67], 0
v_cvt_pk_bf16_f32 v128, v128, v129
v_cvt_pk_bf16_f32 v129, v130, v131
v_mfma_f32_16x16x32_bf16 v[152:155], v[100:103], v[68:71], v[152:155]
v_cvt_pk_bf16_f32 v130, v132, v133
s_waitcnt lgkmcnt(4)
v_mfma_f32_16x16x32_bf16 v[144:147], v[104:107], v[40:43], v[144:147]
v_cvt_pk_bf16_f32 v131, v134, v135
s_add_u32 s53, s56, 4096
v_mfma_f32_16x16x32_bf16 v[144:147], v[108:111], v[44:47], v[144:147]
ds_read_b128 v[96:99], v25 offset:12288
ds_read_b128 v[100:103], v25 offset:13312
v_mfma_f32_16x16x32_bf16 v[152:155], v[104:107], v[72:75], v[152:155]
buffer_store_dwordx4 v[128:131], v1, s[60:63], s53 offen offset:2048 sc1
v_mfma_f32_16x16x32_bf16 v[152:155], v[108:111], v[76:79], v[152:155]
ds_read_b128 v[104:107], v25 offset:14336
s_waitcnt lgkmcnt(5)
v_mfma_f32_16x16x32_bf16 v[144:147], v[112:115], v[48:51], v[144:147]
ds_read_b128 v[108:111], v25 offset:15360
v_mfma_f32_16x16x32_bf16 v[144:147], v[116:119], v[52:55], v[144:147]
v_mfma_f32_16x16x32_bf16 v[152:155], v[112:115], v[80:83], v[152:155]
v_mfma_f32_16x16x32_bf16 v[152:155], v[116:119], v[84:87], v[152:155]
ds_read_b128 v[112:115], v25 offset:16384
s_waitcnt lgkmcnt(5)
v_mfma_f32_16x16x32_bf16 v[144:147], v[120:123], v[56:59], v[144:147]
ds_read_b128 v[116:119], v25 offset:17408
v_mfma_f32_16x16x32_bf16 v[144:147], v[124:127], v[60:63], v[144:147]
v_mfma_f32_16x16x32_bf16 v[152:155], v[120:123], v[88:91], v[152:155]
v_cvt_pk_bf16_f32 v136, v136, v137
v_cvt_pk_bf16_f32 v137, v138, v139
v_mfma_f32_16x16x32_bf16 v[152:155], v[124:127], v[92:95], v[152:155]
v_cvt_pk_bf16_f32 v138, v140, v141
s_waitcnt lgkmcnt(4)
v_mfma_f32_16x16x32_bf16 v[148:151], v[96:99], v[32:35], 0
v_cvt_pk_bf16_f32 v139, v142, v143
s_add_u32 s53, s56, 18432
v_mfma_f32_16x16x32_bf16 v[148:151], v[100:103], v[36:39], v[148:151]
ds_read_b128 v[120:123], v25 offset:18432
ds_read_b128 v[124:127], v25 offset:19456
v_mfma_f32_16x16x32_bf16 v[156:159], v[96:99], v[64:67], 0
buffer_store_dwordx4 v[136:139], v1, s[60:63], s53 offen offset:2048 sc1
v_mfma_f32_16x16x32_bf16 v[156:159], v[100:103], v[68:71], v[156:159]
ds_read_b128 v[96:99], v25 offset:20480
s_waitcnt lgkmcnt(5)
v_mfma_f32_16x16x32_bf16 v[148:151], v[104:107], v[40:43], v[148:151]
ds_read_b128 v[100:103], v25 offset:21504
v_mfma_f32_16x16x32_bf16 v[148:151], v[108:111], v[44:47], v[148:151]
v_mfma_f32_16x16x32_bf16 v[156:159], v[104:107], v[72:75], v[156:159]
v_mfma_f32_16x16x32_bf16 v[156:159], v[108:111], v[76:79], v[156:159]
ds_read_b128 v[104:107], v25 offset:22528
s_waitcnt lgkmcnt(5)
v_mfma_f32_16x16x32_bf16 v[148:151], v[112:115], v[48:51], v[148:151]
ds_read_b128 v[108:111], v25 offset:23552
v_mfma_f32_16x16x32_bf16 v[148:151], v[116:119], v[52:55], v[148:151]
v_mfma_f32_16x16x32_bf16 v[156:159], v[112:115], v[80:83], v[156:159]
v_mfma_f32_16x16x32_bf16 v[156:159], v[116:119], v[84:87], v[156:159]
ds_read_b128 v[112:115], v25 offset:24576
s_waitcnt lgkmcnt(5)
v_mfma_f32_16x16x32_bf16 v[148:151], v[120:123], v[56:59], v[148:151]
ds_read_b128 v[116:119], v25 offset:25600
v_mfma_f32_16x16x32_bf16 v[148:151], v[124:127], v[60:63], v[148:151]
v_mfma_f32_16x16x32_bf16 v[156:159], v[120:123], v[88:91], v[156:159]
v_mfma_f32_16x16x32_bf16 v[156:159], v[124:127], v[92:95], v[156:159]
ds_read_b128 v[120:123], v25 offset:26624
s_waitcnt lgkmcnt(5)
v_mfma_f32_16x16x32_bf16 v[128:131], v[96:99], v[32:35], 0
ds_read_b128 v[124:127], v25 offset:27648
v_mfma_f32_16x16x32_bf16 v[128:131], v[100:103], v[36:39], v[128:131]
v_mfma_f32_16x16x32_bf16 v[136:139], v[96:99], v[64:67], 0
v_cvt_pk_bf16_f32 v144, v144, v145
v_cvt_pk_bf16_f32 v145, v146, v147
v_mfma_f32_16x16x32_bf16 v[136:139], v[100:103], v[68:71], v[136:139]
v_cvt_pk_bf16_f32 v146, v148, v149
s_waitcnt lgkmcnt(4)
v_mfma_f32_16x16x32_bf16 v[128:131], v[104:107], v[40:43], v[128:131]
v_cvt_pk_bf16_f32 v147, v150, v151
s_add_u32 s53, s56, 4096
v_mfma_f32_16x16x32_bf16 v[128:131], v[108:111], v[44:47], v[128:131]
ds_read_b128 v[96:99], v25 offset:28672
ds_read_b128 v[100:103], v25 offset:29696
v_mfma_f32_16x16x32_bf16 v[136:139], v[104:107], v[72:75], v[136:139]
buffer_store_dwordx4 v[144:147], v1, s[60:63], s53 offen offset:3072 sc1
v_mfma_f32_16x16x32_bf16 v[136:139], v[108:111], v[76:79], v[136:139]
ds_read_b128 v[104:107], v25 offset:30720
s_waitcnt lgkmcnt(5)
v_mfma_f32_16x16x32_bf16 v[128:131], v[112:115], v[48:51], v[128:131]
ds_read_b128 v[108:111], v25 offset:31744
v_mfma_f32_16x16x32_bf16 v[128:131], v[116:119], v[52:55], v[128:131]
v_mfma_f32_16x16x32_bf16 v[136:139], v[112:115], v[80:83], v[136:139]
v_mfma_f32_16x16x32_bf16 v[136:139], v[116:119], v[84:87], v[136:139]
ds_read_b128 v[112:115], v25 offset:32768
s_waitcnt lgkmcnt(5)
v_mfma_f32_16x16x32_bf16 v[128:131], v[120:123], v[56:59], v[128:131]
ds_read_b128 v[116:119], v25 offset:33792
v_mfma_f32_16x16x32_bf16 v[128:131], v[124:127], v[60:63], v[128:131]
v_mfma_f32_16x16x32_bf16 v[136:139], v[120:123], v[88:91], v[136:139]
v_cvt_pk_bf16_f32 v152, v152, v153
v_cvt_pk_bf16_f32 v153, v154, v155
v_mfma_f32_16x16x32_bf16 v[136:139], v[124:127], v[92:95], v[136:139]
v_cvt_pk_bf16_f32 v154, v156, v157
s_waitcnt lgkmcnt(4)
v_mfma_f32_16x16x32_bf16 v[132:135], v[96:99], v[32:35], 0
v_cvt_pk_bf16_f32 v155, v158, v159
s_add_u32 s53, s56, 18432
v_mfma_f32_16x16x32_bf16 v[132:135], v[100:103], v[36:39], v[132:135]
ds_read_b128 v[120:123], v25 offset:34816
ds_read_b128 v[124:127], v25 offset:35840
v_mfma_f32_16x16x32_bf16 v[140:143], v[96:99], v[64:67], 0
buffer_store_dwordx4 v[152:155], v1, s[60:63], s53 offen offset:3072 sc1
v_mfma_f32_16x16x32_bf16 v[140:143], v[100:103], v[68:71], v[140:143]
ds_read_b128 v[96:99], v25 offset:36864
s_waitcnt lgkmcnt(5)
v_mfma_f32_16x16x32_bf16 v[132:135], v[104:107], v[40:43], v[132:135]
ds_read_b128 v[100:103], v25 offset:37888
v_mfma_f32_16x16x32_bf16 v[132:135], v[108:111], v[44:47], v[132:135]
v_mfma_f32_16x16x32_bf16 v[140:143], v[104:107], v[72:75], v[140:143]
v_mfma_f32_16x16x32_bf16 v[140:143], v[108:111], v[76:79], v[140:143]
ds_read_b128 v[104:107], v25 offset:38912
s_waitcnt lgkmcnt(5)
v_mfma_f32_16x16x32_bf16 v[132:135], v[112:115], v[48:51], v[132:135]
ds_read_b128 v[108:111], v25 offset:39936
v_mfma_f32_16x16x32_bf16 v[132:135], v[116:119], v[52:55], v[132:135]
v_mfma_f32_16x16x32_bf16 v[140:143], v[112:115], v[80:83], v[140:143]
v_mfma_f32_16x16x32_bf16 v[140:143], v[116:119], v[84:87], v[140:143]
ds_read_b128 v[112:115], v25 offset:40960
s_waitcnt lgkmcnt(5)
v_mfma_f32_16x16x32_bf16 v[132:135], v[120:123], v[56:59], v[132:135]
ds_read_b128 v[116:119], v25 offset:41984
v_mfma_f32_16x16x32_bf16 v[132:135], v[124:127], v[60:63], v[132:135]
v_mfma_f32_16x16x32_bf16 v[140:143], v[120:123], v[88:91], v[140:143]
v_mfma_f32_16x16x32_bf16 v[140:143], v[124:127], v[92:95], v[140:143]
ds_read_b128 v[120:123], v25 offset:43008
s_waitcnt lgkmcnt(5)
v_mfma_f32_16x16x32_bf16 v[144:147], v[96:99], v[32:35], 0
ds_read_b128 v[124:127], v25 offset:44032
v_mfma_f32_16x16x32_bf16 v[144:147], v[100:103], v[36:39], v[144:147]
v_mfma_f32_16x16x32_bf16 v[152:155], v[96:99], v[64:67], 0
v_cvt_pk_bf16_f32 v128, v128, v129
v_cvt_pk_bf16_f32 v129, v130, v131
v_mfma_f32_16x16x32_bf16 v[152:155], v[100:103], v[68:71], v[152:155]
v_cvt_pk_bf16_f32 v130, v132, v133
s_waitcnt lgkmcnt(4)
v_mfma_f32_16x16x32_bf16 v[144:147], v[104:107], v[40:43], v[144:147]
v_cvt_pk_bf16_f32 v131, v134, v135
s_add_u32 s53, s56, 8192
v_mfma_f32_16x16x32_bf16 v[144:147], v[108:111], v[44:47], v[144:147]
ds_read_b128 v[96:99], v25 offset:45056
ds_read_b128 v[100:103], v25 offset:46080
v_mfma_f32_16x16x32_bf16 v[152:155], v[104:107], v[72:75], v[152:155]
buffer_store_dwordx4 v[128:131], v1, s[60:63], s53 offen offset:0 sc1
v_mfma_f32_16x16x32_bf16 v[152:155], v[108:111], v[76:79], v[152:155]
ds_read_b128 v[104:107], v25 offset:47104
s_waitcnt lgkmcnt(5)
v_mfma_f32_16x16x32_bf16 v[144:147], v[112:115], v[48:51], v[144:147]
ds_read_b128 v[108:111], v25 offset:48128
v_mfma_f32_16x16x32_bf16 v[144:147], v[116:119], v[52:55], v[144:147]
v_mfma_f32_16x16x32_bf16 v[152:155], v[112:115], v[80:83], v[152:155]
v_mfma_f32_16x16x32_bf16 v[152:155], v[116:119], v[84:87], v[152:155]
ds_read_b128 v[112:115], v25 offset:49152
s_waitcnt lgkmcnt(5)
v_mfma_f32_16x16x32_bf16 v[144:147], v[120:123], v[56:59], v[144:147]
ds_read_b128 v[116:119], v25 offset:50176
v_mfma_f32_16x16x32_bf16 v[144:147], v[124:127], v[60:63], v[144:147]
v_mfma_f32_16x16x32_bf16 v[152:155], v[120:123], v[88:91], v[152:155]
v_cvt_pk_bf16_f32 v136, v136, v137
v_cvt_pk_bf16_f32 v137, v138, v139
v_mfma_f32_16x16x32_bf16 v[152:155], v[124:127], v[92:95], v[152:155]
v_cvt_pk_bf16_f32 v138, v140, v141
s_waitcnt lgkmcnt(4)
v_mfma_f32_16x16x32_bf16 v[148:151], v[96:99], v[32:35], 0
v_cvt_pk_bf16_f32 v139, v142, v143
s_add_u32 s53, s56, 22528
v_mfma_f32_16x16x32_bf16 v[148:151], v[100:103], v[36:39], v[148:151]
s_waitcnt vmcnt(5)
s_barrier
v_mfma_f32_16x16x32_bf16 v[156:159], v[96:99], v[64:67], 0
ds_read_b128 v[120:123], v25 offset:51200
ds_read_b128 v[124:127], v25 offset:52224
v_mfma_f32_16x16x32_bf16 v[156:159], v[100:103], v[68:71], v[156:159]
ds_read_b128 v[96:99], v1 offset:0
s_waitcnt lgkmcnt(5)
v_mfma_f32_16x16x32_bf16 v[148:151], v[104:107], v[40:43], v[148:151]
buffer_store_dwordx4 v[136:139], v1, s[60:63], s53 offen offset:0 sc1
v_mfma_f32_16x16x32_bf16 v[148:151], v[108:111], v[44:47], v[148:151]
ds_read_b128 v[100:103], v1 offset:1024
v_mfma_f32_16x16x32_bf16 v[156:159], v[104:107], v[72:75], v[156:159]
v_mfma_f32_16x16x32_bf16 v[156:159], v[108:111], v[76:79], v[156:159]
ds_read_b128 v[104:107], v1 offset:2048
s_waitcnt lgkmcnt(5)
v_mfma_f32_16x16x32_bf16 v[148:151], v[112:115], v[48:51], v[148:151]
ds_read_b128 v[108:111], v1 offset:3072
v_mfma_f32_16x16x32_bf16 v[148:151], v[116:119], v[52:55], v[148:151]
v_mfma_f32_16x16x32_bf16 v[156:159], v[112:115], v[80:83], v[156:159]
v_mfma_f32_16x16x32_bf16 v[156:159], v[116:119], v[84:87], v[156:159]
ds_read_b128 v[112:115], v1 offset:4096
s_waitcnt lgkmcnt(5)
v_mfma_f32_16x16x32_bf16 v[148:151], v[120:123], v[56:59], v[148:151]
ds_read_b128 v[116:119], v1 offset:5120
v_mfma_f32_16x16x32_bf16 v[148:151], v[124:127], v[60:63], v[148:151]
v_mfma_f32_16x16x32_bf16 v[156:159], v[120:123], v[88:91], v[156:159]
v_mfma_f32_16x16x32_bf16 v[156:159], v[124:127], v[92:95], v[156:159]
ds_read_b128 v[120:123], v1 offset:6144
s_waitcnt lgkmcnt(5)
v_mfma_f32_16x16x32_bf16 v[128:131], v[96:99], v[32:35], 0
ds_read_b128 v[124:127], v1 offset:7168
v_mfma_f32_16x16x32_bf16 v[128:131], v[100:103], v[36:39], v[128:131]
v_mfma_f32_16x16x32_bf16 v[136:139], v[96:99], v[64:67], 0
v_cvt_pk_bf16_f32 v144, v144, v145
v_cvt_pk_bf16_f32 v145, v146, v147
v_mfma_f32_16x16x32_bf16 v[136:139], v[100:103], v[68:71], v[136:139]
v_cvt_pk_bf16_f32 v146, v148, v149
s_waitcnt lgkmcnt(4)
v_mfma_f32_16x16x32_bf16 v[128:131], v[104:107], v[40:43], v[128:131]
v_cvt_pk_bf16_f32 v147, v150, v151
s_add_u32 s53, s56, 8192
v_mfma_f32_16x16x32_bf16 v[128:131], v[108:111], v[44:47], v[128:131]
s_barrier
ds_read_b128 v[96:99], v1 offset:8192
v_mfma_f32_16x16x32_bf16 v[136:139], v[104:107], v[72:75], v[136:139]
s_mov_b32 m0, s46
s_add_u32 s55, s78, 3584
v_mfma_f32_16x16x32_bf16 v[136:139], v[108:111], v[76:79], v[136:139]
ds_read_b128 v[100:103], v1 offset:9216
s_waitcnt lgkmcnt(4)
v_mfma_f32_16x16x32_bf16 v[128:131], v[112:115], v[48:51], v[128:131]
buffer_store_dwordx4 v[144:147], v1, s[60:63], s53 offen offset:1024 sc1
v_mfma_f32_16x16x32_bf16 v[128:131], v[116:119], v[52:55], v[128:131]
buffer_load_dwordx4 v8, s[84:87], s78 offen offset:0 lds
v_mfma_f32_16x16x32_bf16 v[136:139], v[112:115], v[80:83], v[136:139]
s_waitcnt vmcnt(6)
ds_read_b128 v[104:107], v1 offset:10240
v_mfma_f32_16x16x32_bf16 v[136:139], v[116:119], v[84:87], v[136:139]
s_add_u32 m0, s46, 1024
s_waitcnt lgkmcnt(3)
v_mfma_f32_16x16x32_bf16 v[128:131], v[120:123], v[56:59], v[128:131]
buffer_store_dwordx2 v[30:31], v29, s[60:63], s57 offen offset:0 sc1
v_mfma_f32_16x16x32_bf16 v[128:131], v[124:127], v[60:63], v[128:131]
ds_read_b128 v[108:111], v1 offset:11264
ds_read_b128 v[112:115], v1 offset:12288
v_mfma_f32_16x16x32_bf16 v[136:139], v[120:123], v[88:91], v[136:139]
buffer_load_dwordx4 v8, s[84:87], s55 offen offset:0 lds
v_mfma_f32_16x16x32_bf16 v[136:139], v[124:127], v[92:95], v[136:139]
v_cvt_pk_bf16_f32 v152, v152, v153
s_waitcnt lgkmcnt(3)
v_mfma_f32_16x16x32_bf16 v[132:135], v[96:99], v[32:35], 0
ds_read_b128 v[116:119], v1 offset:13312
s_add_u32 m0, s46, 2048
v_mfma_f32_16x16x32_bf16 v[132:135], v[100:103], v[36:39], v[132:135]
s_mov_b32 s55, 136960
v_cvt_pk_bf16_f32 v153, v154, v155
v_mfma_f32_16x16x32_bf16 v[140:143], v[96:99], v[64:67], 0
v_cvt_pk_bf16_f32 v154, v156, v157
v_cvt_pk_bf16_f32 v155, v158, v159
v_mfma_f32_16x16x32_bf16 v[140:143], v[100:103], v[68:71], v[140:143]
s_add_u32 s53, s56, 22528
s_waitcnt lgkmcnt(2)
v_mfma_f32_16x16x32_bf16 v[132:135], v[104:107], v[40:43], v[132:135]
buffer_load_dwordx4 v8, s[60:63], s55 offen offset:0 sc1 lds
v_mfma_f32_16x16x32_bf16 v[132:135], v[108:111], v[44:47], v[132:135]
ds_read_b128 v[120:123], v1 offset:14336
ds_read_b128 v[124:127], v1 offset:15360
v_mfma_f32_16x16x32_bf16 v[140:143], v[104:107], v[72:75], v[140:143]
buffer_store_dwordx4 v[152:155], v1, s[60:63], s53 offen offset:1024 sc1
v_mfma_f32_16x16x32_bf16 v[140:143], v[108:111], v[76:79], v[140:143]
ds_read_b128 v[96:99], v1 offset:16384
s_waitcnt lgkmcnt(3)
v_mfma_f32_16x16x32_bf16 v[132:135], v[112:115], v[48:51], v[132:135]
ds_read_b128 v[100:103], v1 offset:17408
ds_read_b128 v[104:107], v1 offset:18432
v_mfma_f32_16x16x32_bf16 v[132:135], v[116:119], v[52:55], v[132:135]
ds_read_b128 v[108:111], v1 offset:19456
v_mfma_f32_16x16x32_bf16 v[140:143], v[112:115], v[80:83], v[140:143]
v_mfma_f32_16x16x32_bf16 v[140:143], v[116:119], v[84:87], v[140:143]
ds_read_b128 v[112:115], v1 offset:20480
s_waitcnt lgkmcnt(5)
v_mfma_f32_16x16x32_bf16 v[132:135], v[120:123], v[56:59], v[132:135]
ds_read_b128 v[116:119], v1 offset:21504
v_mfma_f32_16x16x32_bf16 v[132:135], v[124:127], v[60:63], v[132:135]
v_mfma_f32_16x16x32_bf16 v[140:143], v[120:123], v[88:91], v[140:143]
v_mfma_f32_16x16x32_bf16 v[140:143], v[124:127], v[92:95], v[140:143]
ds_read_b128 v[120:123], v1 offset:22528
s_waitcnt lgkmcnt(5)
v_mfma_f32_16x16x32_bf16 v[144:147], v[96:99], v[32:35], 0
ds_read_b128 v[124:127], v1 offset:23552
v_mfma_f32_16x16x32_bf16 v[144:147], v[100:103], v[36:39], v[144:147]
v_mfma_f32_16x16x32_bf16 v[152:155], v[96:99], v[64:67], 0
v_cvt_pk_bf16_f32 v128, v128, v129
v_cvt_pk_bf16_f32 v129, v130, v131
v_mfma_f32_16x16x32_bf16 v[152:155], v[100:103], v[68:71], v[152:155]
v_cvt_pk_bf16_f32 v130, v132, v133
s_waitcnt lgkmcnt(4)
v_mfma_f32_16x16x32_bf16 v[144:147], v[104:107], v[40:43], v[144:147]
v_cvt_pk_bf16_f32 v131, v134, v135
s_add_u32 s53, s56, 8192
v_mfma_f32_16x16x32_bf16 v[144:147], v[108:111], v[44:47], v[144:147]
ds_read_b128 v[96:99], v1 offset:24576
ds_read_b128 v[100:103], v1 offset:25600
v_mfma_f32_16x16x32_bf16 v[152:155], v[104:107], v[72:75], v[152:155]
buffer_store_dwordx4 v[128:131], v1, s[60:63], s53 offen offset:2048 sc1
v_mfma_f32_16x16x32_bf16 v[152:155], v[108:111], v[76:79], v[152:155]
ds_read_b128 v[104:107], v1 offset:26624
s_waitcnt lgkmcnt(5)
v_mfma_f32_16x16x32_bf16 v[144:147], v[112:115], v[48:51], v[144:147]
ds_read_b128 v[108:111], v1 offset:27648
v_mfma_f32_16x16x32_bf16 v[144:147], v[116:119], v[52:55], v[144:147]
v_mfma_f32_16x16x32_bf16 v[152:155], v[112:115], v[80:83], v[152:155]
v_mfma_f32_16x16x32_bf16 v[152:155], v[116:119], v[84:87], v[152:155]
ds_read_b128 v[112:115], v1 offset:28672
s_waitcnt lgkmcnt(5)
v_mfma_f32_16x16x32_bf16 v[144:147], v[120:123], v[56:59], v[144:147]
ds_read_b128 v[116:119], v1 offset:29696
v_mfma_f32_16x16x32_bf16 v[144:147], v[124:127], v[60:63], v[144:147]
v_mfma_f32_16x16x32_bf16 v[152:155], v[120:123], v[88:91], v[152:155]
v_cvt_pk_bf16_f32 v136, v136, v137
v_cvt_pk_bf16_f32 v137, v138, v139
v_mfma_f32_16x16x32_bf16 v[152:155], v[124:127], v[92:95], v[152:155]
v_cvt_pk_bf16_f32 v138, v140, v141
s_waitcnt lgkmcnt(4)
v_mfma_f32_16x16x32_bf16 v[148:151], v[96:99], v[32:35], 0
v_cvt_pk_bf16_f32 v139, v142, v143
s_add_u32 s53, s56, 22528
v_mfma_f32_16x16x32_bf16 v[148:151], v[100:103], v[36:39], v[148:151]
ds_read_b128 v[120:123], v1 offset:30720
ds_read_b128 v[124:127], v1 offset:31744
v_mfma_f32_16x16x32_bf16 v[156:159], v[96:99], v[64:67], 0
buffer_store_dwordx4 v[136:139], v1, s[60:63], s53 offen offset:2048 sc1
v_mfma_f32_16x16x32_bf16 v[156:159], v[100:103], v[68:71], v[156:159]
ds_read_b128 v[96:99], v1 offset:32768
s_waitcnt lgkmcnt(5)
v_mfma_f32_16x16x32_bf16 v[148:151], v[104:107], v[40:43], v[148:151]
ds_read_b128 v[100:103], v1 offset:33792
v_mfma_f32_16x16x32_bf16 v[148:151], v[108:111], v[44:47], v[148:151]
v_mfma_f32_16x16x32_bf16 v[156:159], v[104:107], v[72:75], v[156:159]
v_mfma_f32_16x16x32_bf16 v[156:159], v[108:111], v[76:79], v[156:159]
ds_read_b128 v[104:107], v1 offset:34816
s_waitcnt lgkmcnt(5)
v_mfma_f32_16x16x32_bf16 v[148:151], v[112:115], v[48:51], v[148:151]
ds_read_b128 v[108:111], v1 offset:35840
v_mfma_f32_16x16x32_bf16 v[148:151], v[116:119], v[52:55], v[148:151]
v_mfma_f32_16x16x32_bf16 v[156:159], v[112:115], v[80:83], v[156:159]
v_mfma_f32_16x16x32_bf16 v[156:159], v[116:119], v[84:87], v[156:159]
ds_read_b128 v[112:115], v1 offset:36864
s_waitcnt lgkmcnt(5)
v_mfma_f32_16x16x32_bf16 v[148:151], v[120:123], v[56:59], v[148:151]
ds_read_b128 v[116:119], v1 offset:37888
v_mfma_f32_16x16x32_bf16 v[148:151], v[124:127], v[60:63], v[148:151]
v_mfma_f32_16x16x32_bf16 v[156:159], v[120:123], v[88:91], v[156:159]
v_mfma_f32_16x16x32_bf16 v[156:159], v[124:127], v[92:95], v[156:159]
ds_read_b128 v[120:123], v1 offset:38912
s_waitcnt lgkmcnt(5)
v_mfma_f32_16x16x32_bf16 v[128:131], v[96:99], v[32:35], 0
ds_read_b128 v[124:127], v1 offset:39936
v_mfma_f32_16x16x32_bf16 v[128:131], v[100:103], v[36:39], v[128:131]
v_mfma_f32_16x16x32_bf16 v[136:139], v[96:99], v[64:67], 0
v_cvt_pk_bf16_f32 v144, v144, v145
v_cvt_pk_bf16_f32 v145, v146, v147
v_mfma_f32_16x16x32_bf16 v[136:139], v[100:103], v[68:71], v[136:139]
v_cvt_pk_bf16_f32 v146, v148, v149
s_waitcnt lgkmcnt(4)
v_mfma_f32_16x16x32_bf16 v[128:131], v[104:107], v[40:43], v[128:131]
v_cvt_pk_bf16_f32 v147, v150, v151
s_add_u32 s53, s56, 8192
v_mfma_f32_16x16x32_bf16 v[128:131], v[108:111], v[44:47], v[128:131]
ds_read_b128 v[96:99], v1 offset:40960
ds_read_b128 v[100:103], v1 offset:41984
v_mfma_f32_16x16x32_bf16 v[136:139], v[104:107], v[72:75], v[136:139]
buffer_store_dwordx4 v[144:147], v1, s[60:63], s53 offen offset:3072 sc1
v_mfma_f32_16x16x32_bf16 v[136:139], v[108:111], v[76:79], v[136:139]
ds_read_b128 v[104:107], v1 offset:43008
s_waitcnt lgkmcnt(5)
v_mfma_f32_16x16x32_bf16 v[128:131], v[112:115], v[48:51], v[128:131]
ds_read_b128 v[108:111], v1 offset:44032
v_mfma_f32_16x16x32_bf16 v[128:131], v[116:119], v[52:55], v[128:131]
v_mfma_f32_16x16x32_bf16 v[136:139], v[112:115], v[80:83], v[136:139]
v_mfma_f32_16x16x32_bf16 v[136:139], v[116:119], v[84:87], v[136:139]
ds_read_b128 v[112:115], v1 offset:45056
s_waitcnt lgkmcnt(5)
v_mfma_f32_16x16x32_bf16 v[128:131], v[120:123], v[56:59], v[128:131]
ds_read_b128 v[116:119], v1 offset:46080
v_mfma_f32_16x16x32_bf16 v[128:131], v[124:127], v[60:63], v[128:131]
v_mfma_f32_16x16x32_bf16 v[136:139], v[120:123], v[88:91], v[136:139]
v_cvt_pk_bf16_f32 v152, v152, v153
v_cvt_pk_bf16_f32 v153, v154, v155
v_mfma_f32_16x16x32_bf16 v[136:139], v[124:127], v[92:95], v[136:139]
v_cvt_pk_bf16_f32 v154, v156, v157
s_waitcnt lgkmcnt(4)
v_mfma_f32_16x16x32_bf16 v[132:135], v[96:99], v[32:35], 0
v_cvt_pk_bf16_f32 v155, v158, v159
s_add_u32 s53, s56, 22528
v_mfma_f32_16x16x32_bf16 v[132:135], v[100:103], v[36:39], v[132:135]
ds_read_b128 v[120:123], v1 offset:47104
ds_read_b128 v[124:127], v1 offset:48128
v_mfma_f32_16x16x32_bf16 v[140:143], v[96:99], v[64:67], 0
buffer_store_dwordx4 v[152:155], v1, s[60:63], s53 offen offset:3072 sc1
v_mfma_f32_16x16x32_bf16 v[140:143], v[100:103], v[68:71], v[140:143]
ds_read_b128 v[96:99], v1 offset:49152
s_waitcnt lgkmcnt(5)
v_mfma_f32_16x16x32_bf16 v[132:135], v[104:107], v[40:43], v[132:135]
ds_read_b128 v[100:103], v1 offset:50176
v_mfma_f32_16x16x32_bf16 v[132:135], v[108:111], v[44:47], v[132:135]
v_mfma_f32_16x16x32_bf16 v[140:143], v[104:107], v[72:75], v[140:143]
v_mfma_f32_16x16x32_bf16 v[140:143], v[108:111], v[76:79], v[140:143]
ds_read_b128 v[104:107], v1 offset:51200
s_waitcnt lgkmcnt(5)
v_mfma_f32_16x16x32_bf16 v[132:135], v[112:115], v[48:51], v[132:135]
ds_read_b128 v[108:111], v1 offset:52224
v_mfma_f32_16x16x32_bf16 v[132:135], v[116:119], v[52:55], v[132:135]
v_mfma_f32_16x16x32_bf16 v[140:143], v[112:115], v[80:83], v[140:143]
v_mfma_f32_16x16x32_bf16 v[140:143], v[116:119], v[84:87], v[140:143]
ds_read_b128 v[112:115], v1 offset:53248
s_waitcnt lgkmcnt(5)
v_mfma_f32_16x16x32_bf16 v[132:135], v[120:123], v[56:59], v[132:135]
ds_read_b128 v[116:119], v1 offset:54272
v_mfma_f32_16x16x32_bf16 v[132:135], v[124:127], v[60:63], v[132:135]
v_mfma_f32_16x16x32_bf16 v[140:143], v[120:123], v[88:91], v[140:143]
v_mfma_f32_16x16x32_bf16 v[140:143], v[124:127], v[92:95], v[140:143]
ds_read_b128 v[120:123], v24 offset:0
s_waitcnt lgkmcnt(5)
v_mfma_f32_16x16x32_bf16 v[144:147], v[96:99], v[32:35], 0
ds_read_b128 v[124:127], v24 offset:1024
v_mfma_f32_16x16x32_bf16 v[144:147], v[100:103], v[36:39], v[144:147]
v_mfma_f32_16x16x32_bf16 v[152:155], v[96:99], v[64:67], 0
v_cvt_pk_bf16_f32 v128, v128, v129
v_cvt_pk_bf16_f32 v129, v130, v131
v_mfma_f32_16x16x32_bf16 v[152:155], v[100:103], v[68:71], v[152:155]
v_cvt_pk_bf16_f32 v130, v132, v133
s_waitcnt lgkmcnt(4)
v_mfma_f32_16x16x32_bf16 v[144:147], v[104:107], v[40:43], v[144:147]
v_cvt_pk_bf16_f32 v131, v134, v135
s_add_u32 s53, s56, 12288
v_mfma_f32_16x16x32_bf16 v[144:147], v[108:111], v[44:47], v[144:147]
ds_read_b128 v[96:99], v24 offset:2048
ds_read_b128 v[100:103], v24 offset:3072
v_mfma_f32_16x16x32_bf16 v[152:155], v[104:107], v[72:75], v[152:155]
buffer_store_dwordx4 v[128:131], v1, s[60:63], s53 offen offset:0 sc1
v_mfma_f32_16x16x32_bf16 v[152:155], v[108:111], v[76:79], v[152:155]
ds_read_b128 v[104:107], v24 offset:4096
s_waitcnt lgkmcnt(5)
v_mfma_f32_16x16x32_bf16 v[144:147], v[112:115], v[48:51], v[144:147]
ds_read_b128 v[108:111], v24 offset:5120
v_mfma_f32_16x16x32_bf16 v[144:147], v[116:119], v[52:55], v[144:147]
v_mfma_f32_16x16x32_bf16 v[152:155], v[112:115], v[80:83], v[152:155]
v_mfma_f32_16x16x32_bf16 v[152:155], v[116:119], v[84:87], v[152:155]
ds_read_b128 v[112:115], v24 offset:6144
s_waitcnt lgkmcnt(5)
v_mfma_f32_16x16x32_bf16 v[144:147], v[120:123], v[56:59], v[144:147]
ds_read_b128 v[116:119], v24 offset:7168
v_mfma_f32_16x16x32_bf16 v[144:147], v[124:127], v[60:63], v[144:147]
v_mfma_f32_16x16x32_bf16 v[152:155], v[120:123], v[88:91], v[152:155]
v_cvt_pk_bf16_f32 v136, v136, v137
v_cvt_pk_bf16_f32 v137, v138, v139
v_mfma_f32_16x16x32_bf16 v[152:155], v[124:127], v[92:95], v[152:155]
v_cvt_pk_bf16_f32 v138, v140, v141
s_waitcnt lgkmcnt(4)
v_mfma_f32_16x16x32_bf16 v[148:151], v[96:99], v[32:35], 0
v_cvt_pk_bf16_f32 v139, v142, v143
s_add_u32 s53, s56, 26624
v_mfma_f32_16x16x32_bf16 v[148:151], v[100:103], v[36:39], v[148:151]
ds_read_b128 v[120:123], v24 offset:8192
ds_read_b128 v[124:127], v24 offset:9216
v_mfma_f32_16x16x32_bf16 v[156:159], v[96:99], v[64:67], 0
buffer_store_dwordx4 v[136:139], v1, s[60:63], s53 offen offset:0 sc1
v_mfma_f32_16x16x32_bf16 v[156:159], v[100:103], v[68:71], v[156:159]
s_waitcnt lgkmcnt(4)
v_mfma_f32_16x16x32_bf16 v[148:151], v[104:107], v[40:43], v[148:151]
v_mfma_f32_16x16x32_bf16 v[148:151], v[108:111], v[44:47], v[148:151]
v_mfma_f32_16x16x32_bf16 v[156:159], v[104:107], v[72:75], v[156:159]
v_mfma_f32_16x16x32_bf16 v[156:159], v[108:111], v[76:79], v[156:159]
s_waitcnt lgkmcnt(2)
v_mfma_f32_16x16x32_bf16 v[148:151], v[112:115], v[48:51], v[148:151]
v_mfma_f32_16x16x32_bf16 v[148:151], v[116:119], v[52:55], v[148:151]
v_mfma_f32_16x16x32_bf16 v[156:159], v[112:115], v[80:83], v[156:159]
v_mfma_f32_16x16x32_bf16 v[156:159], v[116:119], v[84:87], v[156:159]
s_waitcnt lgkmcnt(0)
v_mfma_f32_16x16x32_bf16 v[148:151], v[120:123], v[56:59], v[148:151]
v_mfma_f32_16x16x32_bf16 v[148:151], v[124:127], v[60:63], v[148:151]
v_mfma_f32_16x16x32_bf16 v[156:159], v[120:123], v[88:91], v[156:159]
v_mfma_f32_16x16x32_bf16 v[156:159], v[124:127], v[92:95], v[156:159]
s_nop 7
v_cvt_pk_bf16_f32 v144, v144, v145
v_cvt_pk_bf16_f32 v145, v146, v147
v_cvt_pk_bf16_f32 v146, v148, v149
v_cvt_pk_bf16_f32 v147, v150, v151
s_add_u32 s53, s56, 12288
buffer_store_dwordx4 v[144:147], v1, s[60:63], s53 offen offset:1024 sc1
v_cvt_pk_bf16_f32 v152, v152, v153
v_cvt_pk_bf16_f32 v153, v154, v155
v_cvt_pk_bf16_f32 v154, v156, v157
v_cvt_pk_bf16_f32 v155, v158, v159
s_add_u32 s53, s56, 26624
buffer_store_dwordx4 v[152:155], v1, s[60:63], s53 offen offset:1024 sc1
s_barrier
v_lshrrev_b32 v6, 4, v1
s_lshl_b32 s54, s80, 6
v_add_u32 v7, s54, v6
v_mul_u32_u24 v8, 4682, v7
v_lshrrev_b32 v8, 16, v8
v_mul_u32_u24 v9, 14, v8
v_sub_u32 v9, v7, v9
v_lshlrev_b32 v3, 4, v9
v_mul_u32_u24 v2, 896, v8
v_add_u32 v2, v2, v3
v_cmp_le_u32 vcc, 224, v7
v_bfe_u32 v7, v8, 1, 2
v_lshlrev_b32 v9, 1, v9
v_xor_b32 v9, v9, v7
v_mul_u32_u24 v5, 448, v8
v_lshl_add_u32 v5, v9, 4, v5
v_add_u32 v5, 100352, v5
v_cndmask_b32 v7, 0, -1, vcc
v_lshlrev_b32 v7, 30, v7
v_or_b32 v2, v2, v7
v_and_b32 v8, 15, v6
v_lshrrev_b32 v9, 4, v6
v_bfe_u32 v7, v6, 1, 2
v_xor_b32 v9, v9, v7
v_mul_u32_u24 v4, 448, v8
v_lshl_add_u32 v4, v9, 4, v4
v_add_u32 v4, s46, v4
v_add_u32 v26, s46, v1
v_add_u32 v24, s45, v1
v_add_u32 v27, 57344, v1
s_mov_b32 s47, 262144
L_sp1%=:
s_buffer_load_dwordx16 s[0:15], s[60:63], s58 glc
s_add_u32 s52, s58, 64
s_buffer_load_dwordx16 s[16:31], s[60:63], s52 glc
s_waitcnt lgkmcnt(0)
s_xor_b64 s[0:1], s[0:1], s[76:77]
s_xor_b64 s[2:3], s[2:3], s[76:77]
s_xor_b64 s[4:5], s[4:5], s[76:77]
s_xor_b64 s[6:7], s[6:7], s[76:77]
s_xor_b64 s[8:9], s[8:9], s[76:77]
s_xor_b64 s[10:11], s[10:11], s[76:77]
s_xor_b64 s[12:13], s[12:13], s[76:77]
s_xor_b64 s[14:15], s[14:15], s[76:77]
s_xor_b64 s[16:17], s[16:17], s[76:77]
s_xor_b64 s[18:19], s[18:19], s[76:77]
s_xor_b64 s[20:21], s[20:21], s[76:77]
s_xor_b64 s[22:23], s[22:23], s[76:77]
s_xor_b64 s[24:25], s[24:25], s[76:77]
s_xor_b64 s[26:27], s[26:27], s[76:77]
s_xor_b64 s[28:29], s[28:29], s[76:77]
s_xor_b64 s[30:31], s[30:31], s[76:77]
s_or_b64 s[0:1], s[0:1], s[16:17]
s_or_b64 s[2:3], s[2:3], s[18:19]
s_or_b64 s[4:5], s[4:5], s[20:21]
s_or_b64 s[6:7], s[6:7], s[22:23]
s_or_b64 s[8:9], s[8:9], s[24:25]
s_or_b64 s[10:11], s[10:11], s[26:27]
s_or_b64 s[12:13], s[12:13], s[28:29]
s_or_b64 s[14:15], s[14:15], s[30:31]
s_or_b64 s[0:1], s[0:1], s[8:9]
s_or_b64 s[2:3], s[2:3], s[10:11]
s_or_b64 s[4:5], s[4:5], s[12:13]
s_or_b64 s[6:7], s[6:7], s[14:15]
s_or_b64 s[0:1], s[0:1], s[4:5]
s_or_b64 s[2:3], s[2:3], s[6:7]
s_or_b64 s[0:1], s[0:1], s[2:3]
s_cbranch_scc0 L_spd1%=
s_sub_u32 s47, s47, 1
s_cbranch_scc0 L_sp1%=
L_spd1%=:
s_waitcnt vmcnt(0)
buffer_store_dwordx2 v[30:31], v29, s[60:63], s57 offen offset:128 sc1
s_add_u32 s53, s59, 33554432
buffer_load_dwordx4 v[160:163], v10, s[60:63], s53 offen offset:0 sc1
s_add_u32 s53, s53, 1835008
buffer_load_dwordx4 v[164:167], v10, s[60:63], s53 offen offset:0 sc1
s_add_u32 s53, s53, 1835008
buffer_load_dwordx4 v[168:171], v10, s[60:63], s53 offen offset:0 sc1
s_add_u32 s53, s53, 1835008
buffer_load_dwordx4 v[172:175], v10, s[60:63], s53 offen offset:0 sc1
s_add_u32 s53, s53, 1835008
buffer_load_dwordx4 v[176:179], v10, s[60:63], s53 offen offset:0 sc1
s_add_u32 s53, s53, 1835008
buffer_load_dwordx4 v[180:183], v10, s[60:63], s53 offen offset:0 sc1
s_add_u32 s53, s53, 1835008
buffer_load_dwordx4 v[184:187], v10, s[60:63], s53 offen offset:0 sc1
s_add_u32 s53, s53, 1835008
buffer_load_dwordx4 v[188:191], v10, s[60:63], s53 offen offset:0 sc1
s_add_u32 s53, s53, 1835008
buffer_load_dwordx4 v[192:195], v10, s[60:63], s53 offen offset:0 sc1
s_add_u32 s53, s53, 1835008
buffer_load_dwordx4 v[196:199], v10, s[60:63], s53 offen offset:0 sc1
s_add_u32 s53, s53, 1835008
buffer_load_dwordx4 v[200:203], v10, s[60:63], s53 offen offset:0 sc1
s_add_u32 s53, s53, 1835008
buffer_load_dwordx4 v[204:207], v10, s[60:63], s53 offen offset:0 sc1
s_add_u32 s53, s53, 1835008
buffer_load_dwordx4 v[208:211], v10, s[60:63], s53 offen offset:0 sc1
s_add_u32 s53, s53, 1835008
buffer_load_dwordx4 v[212:215], v10, s[60:63], s53 offen offset:0 sc1
s_add_u32 s53, s53, 1835008
buffer_load_dwordx4 v[216:219], v10, s[60:63], s53 offen offset:0 sc1
s_add_u32 s53, s53, 1835008
buffer_load_dwordx4 v[220:223], v10, s[60:63], s53 offen offset:0 sc1
ds_read_b64 v[96:97], v11 offset:0
ds_read_b64 v[104:105], v12 offset:2048
ds_read_b64 v[112:113], v12 offset:2304
ds_read_b64 v[120:121], v12 offset:2560
ds_read_b64 v[98:99], v11 offset:32
ds_read_b64 v[106:107], v12 offset:2080
ds_read_b64 v[114:115], v12 offset:2336
ds_read_b64 v[122:123], v12 offset:2592
ds_read_b64 v[100:101], v11 offset:128
ds_read_b64 v[108:109], v12 offset:2176
ds_read_b64 v[116:117], v12 offset:2432
ds_read_b64 v[124:125], v12 offset:2688
ds_read_b64 v[102:103], v11 offset:160
ds_read_b64 v[110:111], v12 offset:2208
ds_read_b64 v[118:119], v12 offset:2464
ds_read_b64 v[126:127], v12 offset:2720
s_mov_b32 s53, 0x3fb8aa3b
s_waitcnt lgkmcnt(0)
v_lshlrev_b32 v32, 16, v96
v_and_b32 v33, s97, v96
v_lshlrev_b32 v48, 16, v104
v_and_b32 v49, s97, v104
v_lshlrev_b32 v80, 16, v112
v_and_b32 v81, s97, v112
v_lshlrev_b32 v64, 16, v120
v_and_b32 v65, s97, v120
v_add_f32 v64, 1.0, v64
v_add_f32 v65, 1.0, v65
v_cvt_pk_bf16_f32 v16, v64, v65
v_lshlrev_b32 v64, 16, v16
v_and_b32 v65, s97, v16
v_mul_f32 v64, s53, v64
v_mul_f32 v65, s53, v65
v_mul_f32 v80, s53, v80
v_mul_f32 v81, s53, v81
v_mul_f32 v32, v28, v32
v_mul_f32 v33, v28, v33
v_lshlrev_b32 v34, 16, v97
v_and_b32 v35, s97, v97
v_lshlrev_b32 v50, 16, v105
v_and_b32 v51, s97, v105
v_lshlrev_b32 v82, 16, v113
v_and_b32 v83, s97, v113
v_lshlrev_b32 v66, 16, v121
v_and_b32 v67, s97, v121
v_add_f32 v66, 1.0, v66
v_add_f32 v67, 1.0, v67
v_cvt_pk_bf16_f32 v16, v66, v67
v_lshlrev_b32 v66, 16, v16
v_and_b32 v67, s97, v16
v_mul_f32 v66, s53, v66
v_mul_f32 v67, s53, v67
v_mul_f32 v82, s53, v82
v_mul_f32 v83, s53, v83
v_mul_f32 v34, v28, v34
v_mul_f32 v35, v28, v35
v_lshlrev_b32 v36, 16, v98
v_and_b32 v37, s97, v98
v_lshlrev_b32 v52, 16, v106
v_and_b32 v53, s97, v106
v_lshlrev_b32 v84, 16, v114
v_and_b32 v85, s97, v114
v_lshlrev_b32 v68, 16, v122
v_and_b32 v69, s97, v122
v_add_f32 v68, 1.0, v68
v_add_f32 v69, 1.0, v69
v_cvt_pk_bf16_f32 v16, v68, v69
v_lshlrev_b32 v68, 16, v16
v_and_b32 v69, s97, v16
v_mul_f32 v68, s53, v68
v_mul_f32 v69, s53, v69
v_mul_f32 v84, s53, v84
v_mul_f32 v85, s53, v85
v_mul_f32 v36, v28, v36
v_mul_f32 v37, v28, v37
v_lshlrev_b32 v38, 16, v99
v_and_b32 v39, s97, v99
v_lshlrev_b32 v54, 16, v107
v_and_b32 v55, s97, v107
v_lshlrev_b32 v86, 16, v115
v_and_b32 v87, s97, v115
v_lshlrev_b32 v70, 16, v123
v_and_b32 v71, s97, v123
v_add_f32 v70, 1.0, v70
v_add_f32 v71, 1.0, v71
v_cvt_pk_bf16_f32 v16, v70, v71
v_lshlrev_b32 v70, 16, v16
v_and_b32 v71, s97, v16
v_mul_f32 v70, s53, v70
v_mul_f32 v71, s53, v71
v_mul_f32 v86, s53, v86
v_mul_f32 v87, s53, v87
v_mul_f32 v38, v28, v38
v_mul_f32 v39, v28, v39
v_lshlrev_b32 v40, 16, v100
v_and_b32 v41, s97, v100
v_lshlrev_b32 v56, 16, v108
v_and_b32 v57, s97, v108
v_lshlrev_b32 v88, 16, v116
v_and_b32 v89, s97, v116
v_lshlrev_b32 v72, 16, v124
v_and_b32 v73, s97, v124
v_add_f32 v72, 1.0, v72
v_add_f32 v73, 1.0, v73
v_cvt_pk_bf16_f32 v16, v72, v73
v_lshlrev_b32 v72, 16, v16
v_and_b32 v73, s97, v16
v_mul_f32 v72, s53, v72
v_mul_f32 v73, s53, v73
v_mul_f32 v88, s53, v88
v_mul_f32 v89, s53, v89
v_mul_f32 v40, v28, v40
v_mul_f32 v41, v28, v41
v_lshlrev_b32 v42, 16, v101
v_and_b32 v43, s97, v101
v_lshlrev_b32 v58, 16, v109
v_and_b32 v59, s97, v109
v_lshlrev_b32 v90, 16, v117
v_and_b32 v91, s97, v117
v_lshlrev_b32 v74, 16, v125
v_and_b32 v75, s97, v125
v_add_f32 v74, 1.0, v74
v_add_f32 v75, 1.0, v75
v_cvt_pk_bf16_f32 v16, v74, v75
v_lshlrev_b32 v74, 16, v16
v_and_b32 v75, s97, v16
v_mul_f32 v74, s53, v74
v_mul_f32 v75, s53, v75
v_mul_f32 v90, s53, v90
v_mul_f32 v91, s53, v91
v_mul_f32 v42, v28, v42
v_mul_f32 v43, v28, v43
v_lshlrev_b32 v44, 16, v102
v_and_b32 v45, s97, v102
v_lshlrev_b32 v60, 16, v110
v_and_b32 v61, s97, v110
v_lshlrev_b32 v92, 16, v118
v_and_b32 v93, s97, v118
v_lshlrev_b32 v76, 16, v126
v_and_b32 v77, s97, v126
v_add_f32 v76, 1.0, v76
v_add_f32 v77, 1.0, v77
v_cvt_pk_bf16_f32 v16, v76, v77
v_lshlrev_b32 v76, 16, v16
v_and_b32 v77, s97, v16
v_mul_f32 v76, s53, v76
v_mul_f32 v77, s53, v77
v_mul_f32 v92, s53, v92
v_mul_f32 v93, s53, v93
v_mul_f32 v44, v28, v44
v_mul_f32 v45, v28, v45
v_lshlrev_b32 v46, 16, v103
v_and_b32 v47, s97, v103
v_lshlrev_b32 v62, 16, v111
v_and_b32 v63, s97, v111
v_lshlrev_b32 v94, 16, v119
v_and_b32 v95, s97, v119
v_lshlrev_b32 v78, 16, v127
v_and_b32 v79, s97, v127
v_add_f32 v78, 1.0, v78
v_add_f32 v79, 1.0, v79
v_cvt_pk_bf16_f32 v16, v78, v79
v_lshlrev_b32 v78, 16, v16
v_and_b32 v79, s97, v16
v_mul_f32 v78, s53, v78
v_mul_f32 v79, s53, v79
v_mul_f32 v94, s53, v94
v_mul_f32 v95, s53, v95
v_mul_f32 v46, v28, v46
v_mul_f32 v47, v28, v47
s_add_u32 s54, s58, 128
s_mov_b32 s47, 262144
L_sp2%=:
s_buffer_load_dwordx16 s[0:15], s[60:63], s54 glc
s_add_u32 s52, s54, 64
s_buffer_load_dwordx16 s[16:31], s[60:63], s52 glc
s_waitcnt lgkmcnt(0)
s_xor_b64 s[0:1], s[0:1], s[76:77]
s_xor_b64 s[2:3], s[2:3], s[76:77]
s_xor_b64 s[4:5], s[4:5], s[76:77]
s_xor_b64 s[6:7], s[6:7], s[76:77]
s_xor_b64 s[8:9], s[8:9], s[76:77]
s_xor_b64 s[10:11], s[10:11], s[76:77]
s_xor_b64 s[12:13], s[12:13], s[76:77]
s_xor_b64 s[14:15], s[14:15], s[76:77]
s_xor_b64 s[16:17], s[16:17], s[76:77]
s_xor_b64 s[18:19], s[18:19], s[76:77]
s_xor_b64 s[20:21], s[20:21], s[76:77]
s_xor_b64 s[22:23], s[22:23], s[76:77]
s_xor_b64 s[24:25], s[24:25], s[76:77]
s_xor_b64 s[26:27], s[26:27], s[76:77]
s_xor_b64 s[28:29], s[28:29], s[76:77]
s_xor_b64 s[30:31], s[30:31], s[76:77]
s_or_b64 s[0:1], s[0:1], s[16:17]
s_or_b64 s[2:3], s[2:3], s[18:19]
s_or_b64 s[4:5], s[4:5], s[20:21]
s_or_b64 s[6:7], s[6:7], s[22:23]
s_or_b64 s[8:9], s[8:9], s[24:25]
s_or_b64 s[10:11], s[10:11], s[26:27]
s_or_b64 s[12:13], s[12:13], s[28:29]
s_or_b64 s[14:15], s[14:15], s[30:31]
s_or_b64 s[0:1], s[0:1], s[8:9]
s_or_b64 s[2:3], s[2:3], s[10:11]
s_or_b64 s[4:5], s[4:5], s[12:13]
s_or_b64 s[6:7], s[6:7], s[14:15]
s_or_b64 s[0:1], s[0:1], s[4:5]
s_or_b64 s[2:3], s[2:3], s[6:7]
s_or_b64 s[0:1], s[0:1], s[2:3]
s_cbranch_scc0 L_spd2%=
s_sub_u32 s47, s47, 1
s_cbranch_scc0 L_sp2%=
L_spd2%=:
s_add_u32 s53, s59, 33561600
buffer_load_dwordx4 v[128:131], v10, s[60:63], s53 offen offset:0 sc1
s_add_u32 s53, s53, 1835008
buffer_load_dwordx4 v[132:135], v10, s[60:63], s53 offen offset:0 sc1
s_add_u32 s53, s53, 1835008
buffer_load_dwordx4 v[136:139], v10, s[60:63], s53 offen offset:0 sc1
s_add_u32 s53, s53, 1835008
buffer_load_dwordx4 v[140:143], v10, s[60:63], s53 offen offset:0 sc1
s_add_u32 s53, s53, 1835008
buffer_load_dwordx4 v[144:147], v10, s[60:63], s53 offen offset:0 sc1
s_add_u32 s53, s53, 1835008
buffer_load_dwordx4 v[148:151], v10, s[60:63], s53 offen offset:0 sc1
s_add_u32 s53, s53, 1835008
buffer_load_dwordx4 v[152:155], v10, s[60:63], s53 offen offset:0 sc1
s_add_u32 s53, s53, 1835008
buffer_load_dwordx4 v[156:159], v10, s[60:63], s53 offen offset:0 sc1
s_add_u32 s53, s53, 1835008
buffer_load_dwordx4 v[224:227], v10, s[60:63], s53 offen offset:0 sc1
s_add_u32 s53, s53, 1835008
buffer_load_dwordx4 v[228:231], v10, s[60:63], s53 offen offset:0 sc1
s_add_u32 s53, s53, 1835008
buffer_load_dwordx4 v[232:235], v10, s[60:63], s53 offen offset:0 sc1
s_add_u32 s53, s53, 1835008
buffer_load_dwordx4 v[236:239], v10, s[60:63], s53 offen offset:0 sc1
s_add_u32 s53, s53, 1835008
buffer_load_dwordx4 v[240:243], v10, s[60:63], s53 offen offset:0 sc1
s_add_u32 s53, s53, 1835008
buffer_load_dwordx4 v[244:247], v10, s[60:63], s53 offen offset:0 sc1
s_add_u32 s53, s53, 1835008
buffer_load_dwordx4 v[248:251], v10, s[60:63], s53 offen offset:0 sc1
s_add_u32 s53, s53, 1835008
buffer_load_dwordx4 v[252:255], v10, s[60:63], s53 offen offset:0 sc1
s_mul_i32 s48, s80, 25600
s_sub_u32 s49, s49, s45
s_add_u32 s49, s49, s48
s_waitcnt vmcnt(31)
v_lshlrev_b32 v96, 16, v160
v_and_b32 v97, s97, v160
v_lshlrev_b32 v98, 16, v161
v_and_b32 v99, s97, v161
v_lshlrev_b32 v100, 16, v162
v_and_b32 v101, s97, v162
v_lshlrev_b32 v102, 16, v163
v_and_b32 v103, s97, v163
s_waitcnt vmcnt(30)
v_lshlrev_b32 v112, 16, v164
v_and_b32 v113, s97, v164
v_lshlrev_b32 v114, 16, v165
v_and_b32 v115, s97, v165
v_lshlrev_b32 v116, 16, v166
v_and_b32 v117, s97, v166
v_lshlrev_b32 v118, 16, v167
v_and_b32 v119, s97, v167
v_pk_add_f32 v[96:97], v[96:97], v[112:113]
v_pk_add_f32 v[98:99], v[98:99], v[114:115]
v_pk_add_f32 v[100:101], v[100:101], v[116:117]
v_pk_add_f32 v[102:103], v[102:103], v[118:119]
s_waitcnt vmcnt(29)
v_lshlrev_b32 v112, 16, v168
v_and_b32 v113, s97, v168
v_lshlrev_b32 v114, 16, v169
v_and_b32 v115, s97, v169
v_lshlrev_b32 v116, 16, v170
v_and_b32 v117, s97, v170
v_lshlrev_b32 v118, 16, v171
v_and_b32 v119, s97, v171
v_pk_add_f32 v[96:97], v[96:97], v[112:113]
v_pk_add_f32 v[98:99], v[98:99], v[114:115]
v_pk_add_f32 v[100:101], v[100:101], v[116:117]
v_pk_add_f32 v[102:103], v[102:103], v[118:119]
s_waitcnt vmcnt(28)
v_lshlrev_b32 v112, 16, v172
v_and_b32 v113, s97, v172
v_lshlrev_b32 v114, 16, v173
v_and_b32 v115, s97, v173
v_lshlrev_b32 v116, 16, v174
v_and_b32 v117, s97, v174
v_lshlrev_b32 v118, 16, v175
v_and_b32 v119, s97, v175
v_pk_add_f32 v[96:97], v[96:97], v[112:113]
v_pk_add_f32 v[98:99], v[98:99], v[114:115]
v_pk_add_f32 v[100:101], v[100:101], v[116:117]
v_pk_add_f32 v[102:103], v[102:103], v[118:119]
s_waitcnt vmcnt(27)
v_lshlrev_b32 v112, 16, v176
v_and_b32 v113, s97, v176
v_lshlrev_b32 v114, 16, v177
v_and_b32 v115, s97, v177
v_lshlrev_b32 v116, 16, v178
v_and_b32 v117, s97, v178
v_lshlrev_b32 v118, 16, v179
v_and_b32 v119, s97, v179
v_pk_add_f32 v[96:97], v[96:97], v[112:113]
v_pk_add_f32 v[98:99], v[98:99], v[114:115]
v_pk_add_f32 v[100:101], v[100:101], v[116:117]
v_pk_add_f32 v[102:103], v[102:103], v[118:119]
s_waitcnt vmcnt(26)
v_lshlrev_b32 v112, 16, v180
v_and_b32 v113, s97, v180
v_lshlrev_b32 v114, 16, v181
v_and_b32 v115, s97, v181
v_lshlrev_b32 v116, 16, v182
v_and_b32 v117, s97, v182
v_lshlrev_b32 v118, 16, v183
v_and_b32 v119, s97, v183
v_pk_add_f32 v[96:97], v[96:97], v[112:113]
v_pk_add_f32 v[98:99], v[98:99], v[114:115]
v_pk_add_f32 v[100:101], v[100:101], v[116:117]
v_pk_add_f32 v[102:103], v[102:103], v[118:119]
s_waitcnt vmcnt(25)
v_lshlrev_b32 v112, 16, v184
v_and_b32 v113, s97, v184
v_lshlrev_b32 v114, 16, v185
v_and_b32 v115, s97, v185
v_lshlrev_b32 v116, 16, v186
v_and_b32 v117, s97, v186
v_lshlrev_b32 v118, 16, v187
v_and_b32 v119, s97, v187
v_pk_add_f32 v[96:97], v[96:97], v[112:113]
v_pk_add_f32 v[98:99], v[98:99], v[114:115]
v_pk_add_f32 v[100:101], v[100:101], v[116:117]
v_pk_add_f32 v[102:103], v[102:103], v[118:119]
s_waitcnt vmcnt(24)
v_lshlrev_b32 v112, 16, v188
v_and_b32 v113, s97, v188
v_lshlrev_b32 v114, 16, v189
v_and_b32 v115, s97, v189
v_lshlrev_b32 v116, 16, v190
v_and_b32 v117, s97, v190
v_lshlrev_b32 v118, 16, v191
v_and_b32 v119, s97, v191
v_pk_add_f32 v[96:97], v[96:97], v[112:113]
v_pk_add_f32 v[98:99], v[98:99], v[114:115]
v_pk_add_f32 v[100:101], v[100:101], v[116:117]
v_pk_add_f32 v[102:103], v[102:103], v[118:119]
s_waitcnt vmcnt(23)
v_lshlrev_b32 v112, 16, v192
v_and_b32 v113, s97, v192
v_lshlrev_b32 v114, 16, v193
v_and_b32 v115, s97, v193
v_lshlrev_b32 v116, 16, v194
v_and_b32 v117, s97, v194
v_lshlrev_b32 v118, 16, v195
v_and_b32 v119, s97, v195
v_pk_add_f32 v[96:97], v[96:97], v[112:113]
v_pk_add_f32 v[98:99], v[98:99], v[114:115]
v_pk_add_f32 v[100:101], v[100:101], v[116:117]
v_pk_add_f32 v[102:103], v[102:103], v[118:119]
s_waitcnt vmcnt(22)
v_lshlrev_b32 v112, 16, v196
v_and_b32 v113, s97, v196
v_lshlrev_b32 v114, 16, v197
v_and_b32 v115, s97, v197
v_lshlrev_b32 v116, 16, v198
v_and_b32 v117, s97, v198
v_lshlrev_b32 v118, 16, v199
v_and_b32 v119, s97, v199
v_pk_add_f32 v[96:97], v[96:97], v[112:113]
v_pk_add_f32 v[98:99], v[98:99], v[114:115]
v_pk_add_f32 v[100:101], v[100:101], v[116:117]
v_pk_add_f32 v[102:103], v[102:103], v[118:119]
s_waitcnt vmcnt(21)
v_lshlrev_b32 v112, 16, v200
v_and_b32 v113, s97, v200
v_lshlrev_b32 v114, 16, v201
v_and_b32 v115, s97, v201
v_lshlrev_b32 v116, 16, v202
v_and_b32 v117, s97, v202
v_lshlrev_b32 v118, 16, v203
v_and_b32 v119, s97, v203
v_pk_add_f32 v[96:97], v[96:97], v[112:113]
v_pk_add_f32 v[98:99], v[98:99], v[114:115]
v_pk_add_f32 v[100:101], v[100:101], v[116:117]
v_pk_add_f32 v[102:103], v[102:103], v[118:119]
s_waitcnt vmcnt(20)
v_lshlrev_b32 v112, 16, v204
v_and_b32 v113, s97, v204
v_lshlrev_b32 v114, 16, v205
v_and_b32 v115, s97, v205
v_lshlrev_b32 v116, 16, v206
v_and_b32 v117, s97, v206
v_lshlrev_b32 v118, 16, v207
v_and_b32 v119, s97, v207
v_pk_add_f32 v[96:97], v[96:97], v[112:113]
v_pk_add_f32 v[98:99], v[98:99], v[114:115]
v_pk_add_f32 v[100:101], v[100:101], v[116:117]
v_pk_add_f32 v[102:103], v[102:103], v[118:119]
s_waitcnt vmcnt(19)
v_lshlrev_b32 v112, 16, v208
v_and_b32 v113, s97, v208
v_lshlrev_b32 v114, 16, v209
v_and_b32 v115, s97, v209
v_lshlrev_b32 v116, 16, v210
v_and_b32 v117, s97, v210
v_lshlrev_b32 v118, 16, v211
v_and_b32 v119, s97, v211
v_pk_add_f32 v[96:97], v[96:97], v[112:113]
v_pk_add_f32 v[98:99], v[98:99], v[114:115]
v_pk_add_f32 v[100:101], v[100:101], v[116:117]
v_pk_add_f32 v[102:103], v[102:103], v[118:119]
s_waitcnt vmcnt(18)
v_lshlrev_b32 v112, 16, v212
v_and_b32 v113, s97, v212
v_lshlrev_b32 v114, 16, v213
v_and_b32 v115, s97, v213
v_lshlrev_b32 v116, 16, v214
v_and_b32 v117, s97, v214
v_lshlrev_b32 v118, 16, v215
v_and_b32 v119, s97, v215
v_pk_add_f32 v[96:97], v[96:97], v[112:113]
v_pk_add_f32 v[98:99], v[98:99], v[114:115]
v_pk_add_f32 v[100:101], v[100:101], v[116:117]
v_pk_add_f32 v[102:103], v[102:103], v[118:119]
s_waitcnt vmcnt(17)
v_lshlrev_b32 v112, 16, v216
v_and_b32 v113, s97, v216
v_lshlrev_b32 v114, 16, v217
v_and_b32 v115, s97, v217
v_lshlrev_b32 v116, 16, v218
v_and_b32 v117, s97, v218
v_lshlrev_b32 v118, 16, v219
v_and_b32 v119, s97, v219
v_pk_add_f32 v[96:97], v[96:97], v[112:113]
v_pk_add_f32 v[98:99], v[98:99], v[114:115]
v_pk_add_f32 v[100:101], v[100:101], v[116:117]
v_pk_add_f32 v[102:103], v[102:103], v[118:119]
s_waitcnt vmcnt(16)
v_lshlrev_b32 v112, 16, v220
v_and_b32 v113, s97, v220
v_lshlrev_b32 v114, 16, v221
v_and_b32 v115, s97, v221
v_lshlrev_b32 v116, 16, v222
v_and_b32 v117, s97, v222
v_lshlrev_b32 v118, 16, v223
v_and_b32 v119, s97, v223
v_pk_add_f32 v[96:97], v[96:97], v[112:113]
v_pk_add_f32 v[98:99], v[98:99], v[114:115]
v_pk_add_f32 v[100:101], v[100:101], v[116:117]
v_pk_add_f32 v[102:103], v[102:103], v[118:119]
s_waitcnt vmcnt(15)
v_lshlrev_b32 v104, 16, v128
v_and_b32 v105, s97, v128
v_lshlrev_b32 v106, 16, v129
v_and_b32 v107, s97, v129
v_lshlrev_b32 v108, 16, v130
v_and_b32 v109, s97, v130
v_lshlrev_b32 v110, 16, v131
v_and_b32 v111, s97, v131
s_waitcnt vmcnt(14)
v_lshlrev_b32 v112, 16, v132
v_and_b32 v113, s97, v132
v_lshlrev_b32 v114, 16, v133
v_and_b32 v115, s97, v133
v_lshlrev_b32 v116, 16, v134
v_and_b32 v117, s97, v134
v_lshlrev_b32 v118, 16, v135
v_and_b32 v119, s97, v135
v_pk_add_f32 v[104:105], v[104:105], v[112:113]
v_pk_add_f32 v[106:107], v[106:107], v[114:115]
v_pk_add_f32 v[108:109], v[108:109], v[116:117]
v_pk_add_f32 v[110:111], v[110:111], v[118:119]
s_waitcnt vmcnt(13)
v_lshlrev_b32 v112, 16, v136
v_and_b32 v113, s97, v136
v_lshlrev_b32 v114, 16, v137
v_and_b32 v115, s97, v137
v_lshlrev_b32 v116, 16, v138
v_and_b32 v117, s97, v138
v_lshlrev_b32 v118, 16, v139
v_and_b32 v119, s97, v139
v_pk_add_f32 v[104:105], v[104:105], v[112:113]
v_pk_add_f32 v[106:107], v[106:107], v[114:115]
v_pk_add_f32 v[108:109], v[108:109], v[116:117]
v_pk_add_f32 v[110:111], v[110:111], v[118:119]
s_waitcnt vmcnt(12)
v_lshlrev_b32 v112, 16, v140
v_and_b32 v113, s97, v140
v_lshlrev_b32 v114, 16, v141
v_and_b32 v115, s97, v141
v_lshlrev_b32 v116, 16, v142
v_and_b32 v117, s97, v142
v_lshlrev_b32 v118, 16, v143
v_and_b32 v119, s97, v143
v_pk_add_f32 v[104:105], v[104:105], v[112:113]
v_pk_add_f32 v[106:107], v[106:107], v[114:115]
v_pk_add_f32 v[108:109], v[108:109], v[116:117]
v_pk_add_f32 v[110:111], v[110:111], v[118:119]
s_waitcnt vmcnt(11)
v_lshlrev_b32 v112, 16, v144
v_and_b32 v113, s97, v144
v_lshlrev_b32 v114, 16, v145
v_and_b32 v115, s97, v145
v_lshlrev_b32 v116, 16, v146
v_and_b32 v117, s97, v146
v_lshlrev_b32 v118, 16, v147
v_and_b32 v119, s97, v147
v_pk_add_f32 v[104:105], v[104:105], v[112:113]
v_pk_add_f32 v[106:107], v[106:107], v[114:115]
v_pk_add_f32 v[108:109], v[108:109], v[116:117]
v_pk_add_f32 v[110:111], v[110:111], v[118:119]
s_waitcnt vmcnt(10)
v_lshlrev_b32 v112, 16, v148
v_and_b32 v113, s97, v148
v_lshlrev_b32 v114, 16, v149
v_and_b32 v115, s97, v149
v_lshlrev_b32 v116, 16, v150
v_and_b32 v117, s97, v150
v_lshlrev_b32 v118, 16, v151
v_and_b32 v119, s97, v151
v_pk_add_f32 v[104:105], v[104:105], v[112:113]
v_pk_add_f32 v[106:107], v[106:107], v[114:115]
v_pk_add_f32 v[108:109], v[108:109], v[116:117]
v_pk_add_f32 v[110:111], v[110:111], v[118:119]
s_waitcnt vmcnt(9)
v_lshlrev_b32 v112, 16, v152
v_and_b32 v113, s97, v152
v_lshlrev_b32 v114, 16, v153
v_and_b32 v115, s97, v153
v_lshlrev_b32 v116, 16, v154
v_and_b32 v117, s97, v154
v_lshlrev_b32 v118, 16, v155
v_and_b32 v119, s97, v155
v_pk_add_f32 v[104:105], v[104:105], v[112:113]
v_pk_add_f32 v[106:107], v[106:107], v[114:115]
v_pk_add_f32 v[108:109], v[108:109], v[116:117]
v_pk_add_f32 v[110:111], v[110:111], v[118:119]
s_waitcnt vmcnt(8)
v_lshlrev_b32 v112, 16, v156
v_and_b32 v113, s97, v156
v_lshlrev_b32 v114, 16, v157
v_and_b32 v115, s97, v157
v_lshlrev_b32 v116, 16, v158
v_and_b32 v117, s97, v158
v_lshlrev_b32 v118, 16, v159
v_and_b32 v119, s97, v159
v_pk_add_f32 v[104:105], v[104:105], v[112:113]
v_pk_add_f32 v[106:107], v[106:107], v[114:115]
v_pk_add_f32 v[108:109], v[108:109], v[116:117]
v_pk_add_f32 v[110:111], v[110:111], v[118:119]
s_waitcnt vmcnt(7)
v_lshlrev_b32 v112, 16, v224
v_and_b32 v113, s97, v224
v_lshlrev_b32 v114, 16, v225
v_and_b32 v115, s97, v225
v_lshlrev_b32 v116, 16, v226
v_and_b32 v117, s97, v226
v_lshlrev_b32 v118, 16, v227
v_and_b32 v119, s97, v227
v_pk_add_f32 v[104:105], v[104:105], v[112:113]
v_pk_add_f32 v[106:107], v[106:107], v[114:115]
v_pk_add_f32 v[108:109], v[108:109], v[116:117]
v_pk_add_f32 v[110:111], v[110:111], v[118:119]
s_waitcnt vmcnt(6)
v_lshlrev_b32 v112, 16, v228
v_and_b32 v113, s97, v228
v_lshlrev_b32 v114, 16, v229
v_and_b32 v115, s97, v229
v_lshlrev_b32 v116, 16, v230
v_and_b32 v117, s97, v230
v_lshlrev_b32 v118, 16, v231
v_and_b32 v119, s97, v231
v_pk_add_f32 v[104:105], v[104:105], v[112:113]
v_pk_add_f32 v[106:107], v[106:107], v[114:115]
v_pk_add_f32 v[108:109], v[108:109], v[116:117]
v_pk_add_f32 v[110:111], v[110:111], v[118:119]
s_waitcnt vmcnt(5)
v_lshlrev_b32 v112, 16, v232
v_and_b32 v113, s97, v232
v_lshlrev_b32 v114, 16, v233
v_and_b32 v115, s97, v233
v_lshlrev_b32 v116, 16, v234
v_and_b32 v117, s97, v234
v_lshlrev_b32 v118, 16, v235
v_and_b32 v119, s97, v235
v_pk_add_f32 v[104:105], v[104:105], v[112:113]
v_pk_add_f32 v[106:107], v[106:107], v[114:115]
v_pk_add_f32 v[108:109], v[108:109], v[116:117]
v_pk_add_f32 v[110:111], v[110:111], v[118:119]
s_waitcnt vmcnt(4)
v_lshlrev_b32 v112, 16, v236
v_and_b32 v113, s97, v236
v_lshlrev_b32 v114, 16, v237
v_and_b32 v115, s97, v237
v_lshlrev_b32 v116, 16, v238
v_and_b32 v117, s97, v238
v_lshlrev_b32 v118, 16, v239
v_and_b32 v119, s97, v239
v_pk_add_f32 v[104:105], v[104:105], v[112:113]
v_pk_add_f32 v[106:107], v[106:107], v[114:115]
v_pk_add_f32 v[108:109], v[108:109], v[116:117]
v_pk_add_f32 v[110:111], v[110:111], v[118:119]
s_waitcnt vmcnt(3)
v_lshlrev_b32 v112, 16, v240
v_and_b32 v113, s97, v240
v_lshlrev_b32 v114, 16, v241
v_and_b32 v115, s97, v241
v_lshlrev_b32 v116, 16, v242
v_and_b32 v117, s97, v242
v_lshlrev_b32 v118, 16, v243
v_and_b32 v119, s97, v243
v_pk_add_f32 v[104:105], v[104:105], v[112:113]
v_pk_add_f32 v[106:107], v[106:107], v[114:115]
v_pk_add_f32 v[108:109], v[108:109], v[116:117]
v_pk_add_f32 v[110:111], v[110:111], v[118:119]
s_waitcnt vmcnt(2)
v_lshlrev_b32 v112, 16, v244
v_and_b32 v113, s97, v244
v_lshlrev_b32 v114, 16, v245
v_and_b32 v115, s97, v245
v_lshlrev_b32 v116, 16, v246
v_and_b32 v117, s97, v246
v_lshlrev_b32 v118, 16, v247
v_and_b32 v119, s97, v247
v_pk_add_f32 v[104:105], v[104:105], v[112:113]
v_pk_add_f32 v[106:107], v[106:107], v[114:115]
v_pk_add_f32 v[108:109], v[108:109], v[116:117]
v_pk_add_f32 v[110:111], v[110:111], v[118:119]
s_waitcnt vmcnt(1)
v_lshlrev_b32 v112, 16, v248
v_and_b32 v113, s97, v248
v_lshlrev_b32 v114, 16, v249
v_and_b32 v115, s97, v249
v_lshlrev_b32 v116, 16, v250
v_and_b32 v117, s97, v250
v_lshlrev_b32 v118, 16, v251
v_and_b32 v119, s97, v251
v_pk_add_f32 v[104:105], v[104:105], v[112:113]
v_pk_add_f32 v[106:107], v[106:107], v[114:115]
v_pk_add_f32 v[108:109], v[108:109], v[116:117]
v_pk_add_f32 v[110:111], v[110:111], v[118:119]
s_waitcnt vmcnt(0)
v_lshlrev_b32 v112, 16, v252
v_and_b32 v113, s97, v252
v_lshlrev_b32 v114, 16, v253
v_and_b32 v115, s97, v253
v_lshlrev_b32 v116, 16, v254
v_and_b32 v117, s97, v254
v_lshlrev_b32 v118, 16, v255
v_and_b32 v119, s97, v255
v_pk_add_f32 v[104:105], v[104:105], v[112:113]
v_pk_add_f32 v[106:107], v[106:107], v[114:115]
v_pk_add_f32 v[108:109], v[108:109], v[116:117]
v_pk_add_f32 v[110:111], v[110:111], v[118:119]
L_cp5%=:
s_add_u32 m0, s48, 0
s_add_u32 s55, s49, 0
buffer_load_dwordx4 v1, s[60:63], s55 offen offset:0 sc1 lds
v_pk_fma_f32 v[96:97], v[96:97], v[48:49], v[32:33]
v_pk_fma_f32 v[98:99], v[98:99], v[50:51], v[34:35]
v_pk_fma_f32 v[100:101], v[100:101], v[52:53], v[36:37]
v_pk_fma_f32 v[102:103], v[102:103], v[54:55], v[38:39]
v_pk_fma_f32 v[104:105], v[104:105], v[56:57], v[40:41]
v_pk_fma_f32 v[106:107], v[106:107], v[58:59], v[42:43]
v_pk_fma_f32 v[108:109], v[108:109], v[60:61], v[44:45]
v_pk_fma_f32 v[110:111], v[110:111], v[62:63], v[46:47]
v_cvt_pk_bf16_f32 v120, v96, v97
v_cvt_pk_bf16_f32 v121, v98, v99
buffer_load_dwordx4 v1, s[60:63], s55 offen offset:1024 sc1 lds
v_cvt_pk_bf16_f32 v122, v100, v101
v_cvt_pk_bf16_f32 v123, v102, v103
v_cvt_pk_bf16_f32 v124, v104, v105
v_cvt_pk_bf16_f32 v125, v106, v107
v_cvt_pk_bf16_f32 v126, v108, v109
v_cvt_pk_bf16_f32 v127, v110, v111
ds_write_b64 v15, v[120:121] offset:0
ds_write_b64 v15, v[122:123] offset:32
ds_write_b64 v15, v[124:125] offset:448
ds_write_b64 v15, v[126:127] offset:480
buffer_load_dwordx4 v1, s[60:63], s55 offen offset:2048 sc1 lds
v_lshlrev_b32 v96, 16, v120
v_and_b32 v97, s97, v120
v_pk_mul_f32 v[18:19], v[96:97], v[96:97]
v_lshlrev_b32 v98, 16, v121
v_and_b32 v99, s97, v121
v_pk_fma_f32 v[18:19], v[98:99], v[98:99], v[18:19]
v_pk_add_f32 v[16:17], v[98:99], v[96:97]
v_lshlrev_b32 v100, 16, v122
v_and_b32 v101, s97, v122
buffer_load_dwordx4 v1, s[60:63], s55 offen offset:3072 sc1 lds
v_pk_fma_f32 v[18:19], v[100:101], v[100:101], v[18:19]
v_pk_add_f32 v[16:17], v[100:101], v[16:17]
v_lshlrev_b32 v102, 16, v123
v_and_b32 v103, s97, v123
v_pk_fma_f32 v[18:19], v[102:103], v[102:103], v[18:19]
v_pk_add_f32 v[16:17], v[102:103], v[16:17]
v_lshlrev_b32 v104, 16, v124
v_and_b32 v105, s97, v124
v_pk_fma_f32 v[18:19], v[104:105], v[104:105], v[18:19]
v_pk_add_f32 v[16:17], v[104:105], v[16:17]
s_add_u32 m0, s48, 4096
s_add_u32 s55, s49, 4096
buffer_load_dwordx4 v1, s[60:63], s55 offen offset:0 sc1 lds
v_lshlrev_b32 v106, 16, v125
v_and_b32 v107, s97, v125
v_pk_fma_f32 v[18:19], v[106:107], v[106:107], v[18:19]
v_pk_add_f32 v[16:17], v[106:107], v[16:17]
v_lshlrev_b32 v108, 16, v126
v_and_b32 v109, s97, v126
v_pk_fma_f32 v[18:19], v[108:109], v[108:109], v[18:19]
v_pk_add_f32 v[16:17], v[108:109], v[16:17]
v_lshlrev_b32 v110, 16, v127
v_and_b32 v111, s97, v127
buffer_load_dwordx4 v1, s[60:63], s55 offen offset:1024 sc1 lds
v_pk_fma_f32 v[18:19], v[110:111], v[110:111], v[18:19]
v_pk_add_f32 v[16:17], v[110:111], v[16:17]
v_add_f32 v16, v16, v17
v_add_f32 v17, v18, v19
s_nop 0
v_add_f32_dpp v16, v16, v16 row_ror:8 row_mask:0xf bank_mask:0xf
v_add_f32_dpp v17, v17, v17 row_ror:8 row_mask:0xf bank_mask:0xf
v_mov_b32 v18, v16
v_mov_b32 v19, v17
buffer_load_dwordx4 v1, s[60:63], s55 offen offset:2048 sc1 lds
s_nop 0
v_permlane32_swap_b32 v16, v18
v_permlane32_swap_b32 v17, v19
s_nop 0
v_pk_add_f32 v[16:17], v[16:17], v[18:19]
v_mov_b32 v18, v16
v_mov_b32 v19, v17
s_nop 0
v_permlane16_swap_b32 v16, v18
v_permlane16_swap_b32 v17, v19
buffer_load_dwordx4 v1, s[60:63], s55 offen offset:3072 sc1 lds
s_nop 0
v_pk_add_f32 v[16:17], v[16:17], v[18:19]
ds_write_b64 v13, v[16:17] offset:0
s_waitcnt lgkmcnt(0)
s_barrier
ds_read_b128 v[112:115], v14 offset:0
ds_read_b128 v[116:119], v14 offset:16
s_lshl_b32 s52, s80, 11
v_add_u32 v9, s52, v1
v_add_u32 v8, 107520, v9
s_add_u32 m0, s48, 8192
s_add_u32 s55, s49, 8192
buffer_load_dwordx4 v1, s[60:63], s55 offen offset:0 sc1 lds
ds_read_b128 v[120:123], v8 offset:0
ds_read_b128 v[124:127], v8 offset:1024
s_waitcnt lgkmcnt(2)
v_pk_add_f32 v[112:113], v[112:113], v[114:115]
v_pk_add_f32 v[116:117], v[116:117], v[118:119]
v_pk_add_f32 v[16:17], v[112:113], v[116:117]
v_mul_f32 v16, 0x3b124925, v16
v_mul_f32 v17, 0x3b124925, v17
v_fma_f32 v17, -v16, v16, v17
buffer_load_dwordx4 v1, s[60:63], s55 offen offset:1024 sc1 lds
v_max_f32 v17, 0, v17
v_add_f32 v17, 0x3727c5ac, v17
v_rsq_f32 v18, v17
s_nop 0
v_mul_f32 v19, v16, v18
v_pk_fma_f32 v[96:97], v[96:97], v[18:19], v[18:19] op_sel:[0,0,1] op_sel_hi:[1,0,1] neg_lo:[0,0,1] neg_hi:[0,0,1]
v_pk_fma_f32 v[98:99], v[98:99], v[18:19], v[18:19] op_sel:[0,0,1] op_sel_hi:[1,0,1] neg_lo:[0,0,1] neg_hi:[0,0,1]
v_pk_fma_f32 v[100:101], v[100:101], v[18:19], v[18:19] op_sel:[0,0,1] op_sel_hi:[1,0,1] neg_lo:[0,0,1] neg_hi:[0,0,1]
v_pk_fma_f32 v[102:103], v[102:103], v[18:19], v[18:19] op_sel:[0,0,1] op_sel_hi:[1,0,1] neg_lo:[0,0,1] neg_hi:[0,0,1]
v_pk_fma_f32 v[104:105], v[104:105], v[18:19], v[18:19] op_sel:[0,0,1] op_sel_hi:[1,0,1] neg_lo:[0,0,1] neg_hi:[0,0,1]
buffer_load_dwordx4 v1, s[60:63], s55 offen offset:2048 sc1 lds
v_pk_fma_f32 v[106:107], v[106:107], v[18:19], v[18:19] op_sel:[0,0,1] op_sel_hi:[1,0,1] neg_lo:[0,0,1] neg_hi:[0,0,1]
v_pk_fma_f32 v[108:109], v[108:109], v[18:19], v[18:19] op_sel:[0,0,1] op_sel_hi:[1,0,1] neg_lo:[0,0,1] neg_hi:[0,0,1]
v_pk_fma_f32 v[110:111], v[110:111], v[18:19], v[18:19] op_sel:[0,0,1] op_sel_hi:[1,0,1] neg_lo:[0,0,1] neg_hi:[0,0,1]
v_pk_fma_f32 v[96:97], v[96:97], v[64:65], v[80:81]
v_pk_fma_f32 v[98:99], v[98:99], v[66:67], v[82:83]
v_pk_fma_f32 v[100:101], v[100:101], v[68:69], v[84:85]
v_pk_fma_f32 v[102:103], v[102:103], v[70:71], v[86:87]
v_pk_fma_f32 v[104:105], v[104:105], v[72:73], v[88:89]
v_pk_fma_f32 v[106:107], v[106:107], v[74:75], v[90:91]
v_pk_fma_f32 v[108:109], v[108:109], v[76:77], v[92:93]
buffer_load_dwordx4 v1, s[60:63], s55 offen offset:3072 sc1 lds
v_pk_fma_f32 v[110:111], v[110:111], v[78:79], v[94:95]
v_cvt_pk_bf16_f32 v112, v96, v97
v_cvt_pk_bf16_f32 v113, v98, v99
v_cvt_pk_bf16_f32 v114, v100, v101
v_cvt_pk_bf16_f32 v115, v102, v103
v_cvt_pk_bf16_f32 v116, v104, v105
v_cvt_pk_bf16_f32 v117, v106, v107
v_cvt_pk_bf16_f32 v118, v108, v109
v_cvt_pk_bf16_f32 v119, v110, v111
s_add_u32 s53, s59, 1048576
s_add_u32 s54, s59, 1055744
buffer_store_dwordx4 v[112:115], v10, s[60:63], s53 offen offset:0 sc1
buffer_store_dwordx4 v[116:119], v10, s[60:63], s54 offen offset:0 sc1
s_add_u32 s53, s78, 4194304
s_waitcnt lgkmcnt(0)
buffer_store_dwordx4 v[120:123], v9, s[60:63], s53 offen offset:0 sc1
s_cmp_lt_u32 s80, 3
s_cbranch_scc0 L_nox1%=
buffer_store_dwordx4 v[124:127], v9, s[60:63], s53 offen offset:1024 sc1
L_nox1%=:
s_waitcnt vmcnt(0)
buffer_store_dwordx2 v[30:31], v29, s[60:63], s79 offen offset:0 sc1
s_add_u32 m0, s48, 12288
s_add_u32 s55, s49, 12288
buffer_load_dwordx4 v1, s[60:63], s55 offen offset:0 sc1 lds
buffer_load_dwordx4 v1, s[60:63], s55 offen offset:1024 sc1 lds
buffer_load_dwordx4 v1, s[60:63], s55 offen offset:2048 sc1 lds
buffer_load_dwordx4 v1, s[60:63], s55 offen offset:3072 sc1 lds
s_add_u32 m0, s48, 16384
s_add_u32 s55, s49, 16384
buffer_load_dwordx4 v1, s[60:63], s55 offen offset:0 sc1 lds
buffer_load_dwordx4 v1, s[60:63], s55 offen offset:1024 sc1 lds
buffer_load_dwordx4 v1, s[60:63], s55 offen offset:2048 sc1 lds
buffer_load_dwordx4 v1, s[60:63], s55 offen offset:3072 sc1 lds
s_add_u32 m0, s48, 20480
s_add_u32 s55, s49, 20480
buffer_load_dwordx4 v1, s[60:63], s55 offen offset:0 sc1 lds
buffer_load_dwordx4 v1, s[60:63], s55 offen offset:1024 sc1 lds
buffer_load_dwordx4 v1, s[60:63], s55 offen offset:2048 sc1 lds
buffer_load_dwordx4 v1, s[60:63], s55 offen offset:3072 sc1 lds
s_add_u32 m0, s48, 24576
s_add_u32 s55, s49, 24576
buffer_load_dwordx4 v1, s[60:63], s55 offen offset:0 sc1 lds
s_waitcnt vmcnt(0)
s_barrier
s_mov_b32 s47, 262144
L_sp3%=:
s_buffer_load_dwordx16 s[0:15], s[60:63], s88 glc
s_add_u32 s52, s88, 64
s_buffer_load_dwordx16 s[16:31], s[60:63], s52 glc
s_waitcnt lgkmcnt(0)
s_xor_b64 s[0:1], s[0:1], s[76:77]
s_xor_b64 s[2:3], s[2:3], s[76:77]
s_xor_b64 s[4:5], s[4:5], s[76:77]
s_xor_b64 s[6:7], s[6:7], s[76:77]
s_xor_b64 s[8:9], s[8:9], s[76:77]
s_xor_b64 s[10:11], s[10:11], s[76:77]
s_xor_b64 s[12:13], s[12:13], s[76:77]
s_xor_b64 s[14:15], s[14:15], s[76:77]
s_xor_b64 s[16:17], s[16:17], s[76:77]
s_xor_b64 s[18:19], s[18:19], s[76:77]
s_xor_b64 s[20:21], s[20:21], s[76:77]
s_xor_b64 s[22:23], s[22:23], s[76:77]
s_xor_b64 s[24:25], s[24:25], s[76:77]
s_xor_b64 s[26:27], s[26:27], s[76:77]
s_xor_b64 s[28:29], s[28:29], s[76:77]
s_xor_b64 s[30:31], s[30:31], s[76:77]
s_or_b64 s[0:1], s[0:1], s[16:17]
s_or_b64 s[2:3], s[2:3], s[18:19]
s_or_b64 s[4:5], s[4:5], s[20:21]
s_or_b64 s[6:7], s[6:7], s[22:23]
s_or_b64 s[8:9], s[8:9], s[24:25]
s_or_b64 s[10:11], s[10:11], s[26:27]
s_or_b64 s[12:13], s[12:13], s[28:29]
s_or_b64 s[14:15], s[14:15], s[30:31]
s_or_b64 s[0:1], s[0:1], s[8:9]
s_or_b64 s[2:3], s[2:3], s[10:11]
s_or_b64 s[4:5], s[4:5], s[12:13]
s_or_b64 s[6:7], s[6:7], s[14:15]
s_or_b64 s[0:1], s[0:1], s[4:5]
s_or_b64 s[2:3], s[2:3], s[6:7]
s_or_b64 s[0:1], s[0:1], s[2:3]
s_cbranch_scc0 L_spd3%=
s_sub_u32 s47, s47, 1
s_cbranch_scc0 L_sp3%=
L_spd3%=:
s_add_u32 s52, s91, 0
buffer_load_dwordx4 v[128:131], v1, s[60:63], s52 offen offset:0 sc1
s_add_u32 s54, s91, 14336
buffer_load_dwordx4 v[132:135], v1, s[60:63], s54 offen offset:0 sc1
buffer_load_dwordx4 v[136:139], v1, s[60:63], s52 offen offset:1024 sc1
buffer_load_dwordx4 v[140:143], v1, s[60:63], s54 offen offset:1024 sc1
buffer_load_dwordx4 v[144:147], v1, s[60:63], s52 offen offset:2048 sc1
buffer_load_dwordx4 v[148:151], v1, s[60:63], s54 offen offset:2048 sc1
buffer_load_dwordx4 v[152:155], v1, s[60:63], s52 offen offset:3072 sc1
buffer_load_dwordx4 v[156:159], v1, s[60:63], s54 offen offset:3072 sc1
s_add_u32 s52, s91, 4096
buffer_load_dwordx4 v[160:163], v1, s[60:63], s52 offen offset:0 sc1
s_add_u32 s54, s91, 18432
buffer_load_dwordx4 v[164:167], v1, s[60:63], s54 offen offset:0 sc1
buffer_load_dwordx4 v[168:171], v1, s[60:63], s52 offen offset:1024 sc1
buffer_load_dwordx4 v[172:175], v1, s[60:63], s54 offen offset:1024 sc1
ds_read_b128 v[96:99], v1 offset:0
ds_read_b128 v[100:103], v1 offset:7168
ds_read_b128 v[104:107], v1 offset:1024
ds_read_b128 v[108:111], v1 offset:8192
ds_read_b128 v[112:115], v1 offset:2048
ds_read_b128 v[116:119], v1 offset:9216
s_waitcnt vmcnt(8)
s_waitcnt lgkmcnt(4)
v_mfma_f32_16x16x32_bf16 v[32:35], v[96:99], v[128:131], 0
ds_read_b128 v[120:123], v1 offset:3072
ds_read_b128 v[124:127], v1 offset:10240
v_mfma_f32_16x16x32_bf16 v[32:35], v[100:103], v[136:139], v[32:35]
buffer_load_dwordx4 v[176:179], v1, s[60:63], s52 offen offset:2048 sc1
v_mfma_f32_16x16x32_bf16 v[36:39], v[96:99], v[132:135], 0
v_mfma_f32_16x16x32_bf16 v[36:39], v[100:103], v[140:143], v[36:39]
buffer_load_dwordx4 v[180:183], v1, s[60:63], s54 offen offset:2048 sc1
s_waitcnt lgkmcnt(4)
v_mfma_f32_16x16x32_bf16 v[40:43], v[104:107], v[128:131], 0
ds_read_b128 v[96:99], v1 offset:4096
ds_read_b128 v[100:103], v1 offset:11264
v_mfma_f32_16x16x32_bf16 v[40:43], v[108:111], v[136:139], v[40:43]
buffer_load_dwordx4 v[184:187], v1, s[60:63], s52 offen offset:3072 sc1
v_mfma_f32_16x16x32_bf16 v[44:47], v[104:107], v[132:135], 0
v_mfma_f32_16x16x32_bf16 v[44:47], v[108:111], v[140:143], v[44:47]
buffer_load_dwordx4 v[188:191], v1, s[60:63], s54 offen offset:3072 sc1
s_waitcnt lgkmcnt(4)
v_mfma_f32_16x16x32_bf16 v[48:51], v[112:115], v[128:131], 0
ds_read_b128 v[104:107], v1 offset:5120
ds_read_b128 v[108:111], v1 offset:12288
v_mfma_f32_16x16x32_bf16 v[48:51], v[116:119], v[136:139], v[48:51]
s_add_u32 s52, s91, 8192
buffer_load_dwordx4 v[192:195], v1, s[60:63], s52 offen offset:0 sc1
v_mfma_f32_16x16x32_bf16 v[52:55], v[112:115], v[132:135], 0
v_mfma_f32_16x16x32_bf16 v[52:55], v[116:119], v[140:143], v[52:55]
s_add_u32 s54, s91, 22528
buffer_load_dwordx4 v[196:199], v1, s[60:63], s54 offen offset:0 sc1
s_waitcnt lgkmcnt(4)
v_mfma_f32_16x16x32_bf16 v[56:59], v[120:123], v[128:131], 0
ds_read_b128 v[112:115], v1 offset:6144
ds_read_b128 v[116:119], v1 offset:13312
v_mfma_f32_16x16x32_bf16 v[56:59], v[124:127], v[136:139], v[56:59]
buffer_load_dwordx4 v[200:203], v1, s[60:63], s52 offen offset:1024 sc1
v_mfma_f32_16x16x32_bf16 v[60:63], v[120:123], v[132:135], 0
v_mfma_f32_16x16x32_bf16 v[60:63], v[124:127], v[140:143], v[60:63]
buffer_load_dwordx4 v[204:207], v1, s[60:63], s54 offen offset:1024 sc1
s_waitcnt lgkmcnt(4)
v_mfma_f32_16x16x32_bf16 v[64:67], v[96:99], v[128:131], 0
ds_read_b128 v[120:123], v1 offset:14336
ds_read_b128 v[124:127], v1 offset:21504
v_mfma_f32_16x16x32_bf16 v[64:67], v[100:103], v[136:139], v[64:67]
buffer_load_dwordx4 v[208:211], v1, s[60:63], s52 offen offset:2048 sc1
v_mfma_f32_16x16x32_bf16 v[68:71], v[96:99], v[132:135], 0
v_mfma_f32_16x16x32_bf16 v[68:71], v[100:103], v[140:143], v[68:71]
buffer_load_dwordx4 v[212:215], v1, s[60:63], s54 offen offset:2048 sc1
s_waitcnt lgkmcnt(4)
v_mfma_f32_16x16x32_bf16 v[72:75], v[104:107], v[128:131], 0
ds_read_b128 v[96:99], v1 offset:15360
ds_read_b128 v[100:103], v1 offset:22528
v_mfma_f32_16x16x32_bf16 v[72:75], v[108:111], v[136:139], v[72:75]
buffer_load_dwordx4 v[216:219], v1, s[60:63], s52 offen offset:3072 sc1
v_mfma_f32_16x16x32_bf16 v[76:79], v[104:107], v[132:135], 0
v_mfma_f32_16x16x32_bf16 v[76:79], v[108:111], v[140:143], v[76:79]
buffer_load_dwordx4 v[220:223], v1, s[60:63], s54 offen offset:3072 sc1
s_waitcnt lgkmcnt(4)
v_mfma_f32_16x16x32_bf16 v[80:83], v[112:115], v[128:131], 0
ds_read_b128 v[104:107], v1 offset:16384
ds_read_b128 v[108:111], v1 offset:23552
v_mfma_f32_16x16x32_bf16 v[80:83], v[116:119], v[136:139], v[80:83]
s_add_u32 s52, s91, 12288
buffer_load_dwordx4 v[224:227], v1, s[60:63], s52 offen offset:0 sc1
v_mfma_f32_16x16x32_bf16 v[84:87], v[112:115], v[132:135], 0
v_mfma_f32_16x16x32_bf16 v[84:87], v[116:119], v[140:143], v[84:87]
s_add_u32 s54, s91, 26624
buffer_load_dwordx4 v[228:231], v1, s[60:63], s54 offen offset:0 sc1
s_waitcnt vmcnt(18)
s_waitcnt lgkmcnt(4)
v_mfma_f32_16x16x32_bf16 v[32:35], v[120:123], v[144:147], v[32:35]
ds_read_b128 v[112:115], v1 offset:17408
ds_read_b128 v[116:119], v1 offset:24576
v_mfma_f32_16x16x32_bf16 v[32:35], v[124:127], v[152:155], v[32:35]
buffer_load_dwordx4 v[232:235], v1, s[60:63], s52 offen offset:1024 sc1
v_mfma_f32_16x16x32_bf16 v[36:39], v[120:123], v[148:151], v[36:39]
v_mfma_f32_16x16x32_bf16 v[36:39], v[124:127], v[156:159], v[36:39]
buffer_load_dwordx4 v[236:239], v1, s[60:63], s54 offen offset:1024 sc1
s_waitcnt lgkmcnt(4)
v_mfma_f32_16x16x32_bf16 v[40:43], v[96:99], v[144:147], v[40:43]
ds_read_b128 v[120:123], v1 offset:18432
ds_read_b128 v[124:127], v1 offset:25600
v_mfma_f32_16x16x32_bf16 v[40:43], v[100:103], v[152:155], v[40:43]
s_add_u32 s55, s44, 45056
buffer_load_dwordx4 a[96:99], v1, s[60:63], s55 offen offset:0 sc1
v_mfma_f32_16x16x32_bf16 v[44:47], v[96:99], v[148:151], v[44:47]
v_mfma_f32_16x16x32_bf16 v[44:47], v[100:103], v[156:159], v[44:47]
buffer_load_dwordx4 a[100:103], v1, s[60:63], s55 offen offset:1024 sc1
s_waitcnt lgkmcnt(4)
v_mfma_f32_16x16x32_bf16 v[48:51], v[104:107], v[144:147], v[48:51]
ds_read_b128 v[96:99], v1 offset:19456
ds_read_b128 v[100:103], v1 offset:26624
v_mfma_f32_16x16x32_bf16 v[48:51], v[108:111], v[152:155], v[48:51]
buffer_load_dwordx4 a[112:115], v1, s[60:63], s55 offen offset:2048 sc1
v_mfma_f32_16x16x32_bf16 v[52:55], v[104:107], v[148:151], v[52:55]
v_mfma_f32_16x16x32_bf16 v[52:55], v[108:111], v[156:159], v[52:55]
buffer_load_dwordx4 a[116:119], v1, s[60:63], s55 offen offset:3072 sc1
s_waitcnt lgkmcnt(4)
v_mfma_f32_16x16x32_bf16 v[56:59], v[112:115], v[144:147], v[56:59]
ds_read_b128 v[104:107], v1 offset:20480
ds_read_b128 v[108:111], v1 offset:27648
v_mfma_f32_16x16x32_bf16 v[56:59], v[116:119], v[152:155], v[56:59]
s_add_u32 s55, s44, 49152
buffer_load_dwordx4 a[128:131], v1, s[60:63], s55 offen offset:0 sc1
v_mfma_f32_16x16x32_bf16 v[60:63], v[112:115], v[148:151], v[60:63]
v_mfma_f32_16x16x32_bf16 v[60:63], v[116:119], v[156:159], v[60:63]
buffer_load_dwordx4 a[132:135], v1, s[60:63], s55 offen offset:1024 sc1
s_waitcnt lgkmcnt(4)
v_mfma_f32_16x16x32_bf16 v[64:67], v[120:123], v[144:147], v[64:67]
ds_read_b128 v[112:115], v1 offset:28672
ds_read_b128 v[116:119], v1 offset:35840
v_mfma_f32_16x16x32_bf16 v[64:67], v[124:127], v[152:155], v[64:67]
buffer_load_dwordx4 a[144:147], v1, s[60:63], s55 offen offset:2048 sc1
v_mfma_f32_16x16x32_bf16 v[68:71], v[120:123], v[148:151], v[68:71]
v_mfma_f32_16x16x32_bf16 v[68:71], v[124:127], v[156:159], v[68:71]
buffer_load_dwordx4 a[148:151], v1, s[60:63], s55 offen offset:3072 sc1
s_waitcnt lgkmcnt(4)
v_mfma_f32_16x16x32_bf16 v[72:75], v[96:99], v[144:147], v[72:75]
ds_read_b128 v[120:123], v1 offset:29696
ds_read_b128 v[124:127], v1 offset:36864
v_mfma_f32_16x16x32_bf16 v[72:75], v[100:103], v[152:155], v[72:75]
s_add_u32 s55, s44, 53248
buffer_load_dwordx4 a[160:163], v1, s[60:63], s55 offen offset:0 sc1
v_mfma_f32_16x16x32_bf16 v[76:79], v[96:99], v[148:151], v[76:79]
v_mfma_f32_16x16x32_bf16 v[76:79], v[100:103], v[156:159], v[76:79]
buffer_load_dwordx4 a[164:167], v1, s[60:63], s55 offen offset:1024 sc1
s_waitcnt lgkmcnt(4)
v_mfma_f32_16x16x32_bf16 v[80:83], v[104:107], v[144:147], v[80:83]
ds_read_b128 v[96:99], v1 offset:30720
ds_read_b128 v[100:103], v1 offset:37888
v_mfma_f32_16x16x32_bf16 v[80:83], v[108:111], v[152:155], v[80:83]
buffer_load_dwordx4 a[176:179], v1, s[60:63], s55 offen offset:2048 sc1
v_mfma_f32_16x16x32_bf16 v[84:87], v[104:107], v[148:151], v[84:87]
v_mfma_f32_16x16x32_bf16 v[84:87], v[108:111], v[156:159], v[84:87]
buffer_load_dwordx4 a[180:183], v1, s[60:63], s55 offen offset:3072 sc1
s_waitcnt vmcnt(28)
s_waitcnt lgkmcnt(4)
v_mfma_f32_16x16x32_bf16 v[32:35], v[112:115], v[160:163], v[32:35]
ds_read_b128 v[104:107], v1 offset:31744
ds_read_b128 v[108:111], v1 offset:38912
v_mfma_f32_16x16x32_bf16 v[32:35], v[116:119], v[168:171], v[32:35]
s_add_u32 s55, s44, 57344
buffer_load_dwordx4 a[192:195], v1, s[60:63], s55 offen offset:0 sc1
v_mfma_f32_16x16x32_bf16 v[36:39], v[112:115], v[164:167], v[36:39]
v_mfma_f32_16x16x32_bf16 v[36:39], v[116:119], v[172:175], v[36:39]
buffer_load_dwordx4 a[196:199], v1, s[60:63], s55 offen offset:1024 sc1
s_waitcnt lgkmcnt(4)
v_mfma_f32_16x16x32_bf16 v[40:43], v[120:123], v[160:163], v[40:43]
ds_read_b128 v[112:115], v1 offset:32768
ds_read_b128 v[116:119], v1 offset:39936
v_mfma_f32_16x16x32_bf16 v[40:43], v[124:127], v[168:171], v[40:43]
buffer_load_dwordx4 a[208:211], v1, s[60:63], s55 offen offset:2048 sc1
v_mfma_f32_16x16x32_bf16 v[44:47], v[120:123], v[164:167], v[44:47]
v_mfma_f32_16x16x32_bf16 v[44:47], v[124:127], v[172:175], v[44:47]
buffer_load_dwordx4 a[212:215], v1, s[60:63], s55 offen offset:3072 sc1
s_waitcnt lgkmcnt(4)
v_mfma_f32_16x16x32_bf16 v[48:51], v[96:99], v[160:163], v[48:51]
ds_read_b128 v[120:123], v1 offset:33792
ds_read_b128 v[124:127], v1 offset:40960
v_mfma_f32_16x16x32_bf16 v[48:51], v[100:103], v[168:171], v[48:51]
v_mfma_f32_16x16x32_bf16 v[52:55], v[96:99], v[164:167], v[52:55]
v_mfma_f32_16x16x32_bf16 v[52:55], v[100:103], v[172:175], v[52:55]
s_add_u32 s55, s44, 61440
buffer_load_dwordx4 a[224:227], v1, s[60:63], s55 offen offset:0 sc1
s_waitcnt lgkmcnt(4)
v_mfma_f32_16x16x32_bf16 v[56:59], v[104:107], v[160:163], v[56:59]
ds_read_b128 v[96:99], v1 offset:34816
ds_read_b128 v[100:103], v1 offset:41984
v_mfma_f32_16x16x32_bf16 v[56:59], v[108:111], v[168:171], v[56:59]
v_mfma_f32_16x16x32_bf16 v[60:63], v[104:107], v[164:167], v[60:63]
v_mfma_f32_16x16x32_bf16 v[60:63], v[108:111], v[172:175], v[60:63]
buffer_load_dwordx4 a[228:231], v1, s[60:63], s55 offen offset:1024 sc1
s_waitcnt lgkmcnt(4)
v_mfma_f32_16x16x32_bf16 v[64:67], v[112:115], v[160:163], v[64:67]
ds_read_b128 v[104:107], v1 offset:43008
ds_read_b128 v[108:111], v1 offset:44032
v_mfma_f32_16x16x32_bf16 v[64:67], v[116:119], v[168:171], v[64:67]
v_mfma_f32_16x16x32_bf16 v[68:71], v[112:115], v[164:167], v[68:71]
v_mfma_f32_16x16x32_bf16 v[68:71], v[116:119], v[172:175], v[68:71]
buffer_load_dwordx4 a[240:243], v1, s[60:63], s55 offen offset:2048 sc1
s_waitcnt lgkmcnt(4)
v_mfma_f32_16x16x32_bf16 v[72:75], v[120:123], v[160:163], v[72:75]
ds_read_b128 v[112:115], v1 offset:45056
ds_read_b128 v[116:119], v1 offset:46080
v_mfma_f32_16x16x32_bf16 v[72:75], v[124:127], v[168:171], v[72:75]
v_mfma_f32_16x16x32_bf16 v[76:79], v[120:123], v[164:167], v[76:79]
v_mfma_f32_16x16x32_bf16 v[76:79], v[124:127], v[172:175], v[76:79]
s_add_u32 m0, s46, 0
s_add_u32 s55, s44, 64512
buffer_load_dwordx4 v1, s[60:63], s55 offen offset:0 sc1 lds
s_waitcnt lgkmcnt(4)
v_mfma_f32_16x16x32_bf16 v[80:83], v[96:99], v[160:163], v[80:83]
ds_read_b128 v[120:123], v1 offset:47104
ds_read_b128 v[124:127], v1 offset:48128
v_mfma_f32_16x16x32_bf16 v[80:83], v[100:103], v[168:171], v[80:83]
v_mfma_f32_16x16x32_bf16 v[84:87], v[96:99], v[164:167], v[84:87]
v_mfma_f32_16x16x32_bf16 v[84:87], v[100:103], v[172:175], v[84:87]
buffer_load_dwordx4 v1, s[60:63], s55 offen offset:1024 sc1 lds
s_waitcnt vmcnt(33)
s_waitcnt lgkmcnt(4)
v_mfma_f32_16x16x32_bf16 v[32:35], v[104:107], v[176:179], v[32:35]
ds_read_b128 v[96:99], v1 offset:49152
ds_read_b128 v[100:103], v1 offset:50176
v_mfma_f32_16x16x32_bf16 v[32:35], v[108:111], v[184:187], v[32:35]
v_mfma_f32_16x16x32_bf16 v[36:39], v[104:107], v[180:183], v[36:39]
v_mfma_f32_16x16x32_bf16 v[36:39], v[108:111], v[188:191], v[36:39]
buffer_load_dwordx4 v1, s[60:63], s55 offen offset:2048 sc1 lds
s_waitcnt vmcnt(30)
s_barrier
s_waitcnt lgkmcnt(4)
v_mfma_f32_16x16x32_bf16 v[32:35], v[112:115], v[192:195], v[32:35]
ds_read_b128 v[104:107], v1 offset:51200
ds_read_b128 v[108:111], v1 offset:52224
v_mfma_f32_16x16x32_bf16 v[32:35], v[116:119], v[200:203], v[32:35]
v_mfma_f32_16x16x32_bf16 v[36:39], v[112:115], v[196:199], v[36:39]
v_mfma_f32_16x16x32_bf16 v[36:39], v[116:119], v[204:207], v[36:39]
buffer_load_dwordx4 v1, s[60:63], s55 offen offset:3072 sc1 lds
s_waitcnt vmcnt(27)
s_waitcnt lgkmcnt(4)
v_mfma_f32_16x16x32_bf16 v[32:35], v[120:123], v[208:211], v[32:35]
ds_read_b128 v[112:115], v1 offset:53248
ds_read_b128 v[116:119], v1 offset:54272
v_mfma_f32_16x16x32_bf16 v[32:35], v[124:127], v[216:219], v[32:35]
v_mfma_f32_16x16x32_bf16 v[36:39], v[120:123], v[212:215], v[36:39]
v_mfma_f32_16x16x32_bf16 v[36:39], v[124:127], v[220:223], v[36:39]
s_add_u32 m0, s46, 4096
s_add_u32 s55, s44, 68608
buffer_load_dwordx4 v1, s[60:63], s55 offen offset:0 sc1 lds
s_waitcnt vmcnt(24)
s_waitcnt lgkmcnt(4)
v_mfma_f32_16x16x32_bf16 v[32:35], v[96:99], v[224:227], v[32:35]
ds_read_b128 v[120:123], v1 offset:55296
ds_read_b128 v[124:127], v1 offset:56320
v_mfma_f32_16x16x32_bf16 v[32:35], v[100:103], v[232:235], v[32:35]
v_mfma_f32_16x16x32_bf16 v[36:39], v[96:99], v[228:231], v[36:39]
v_mfma_f32_16x16x32_bf16 v[36:39], v[100:103], v[236:239], v[36:39]
buffer_load_dwordx4 v1, s[60:63], s55 offen offset:1024 sc1 lds
s_waitcnt lgkmcnt(4)
v_mfma_f32_16x16x32_bf16 v[40:43], v[104:107], v[176:179], v[40:43]
ds_read_b128 v[96:99], v27 offset:0
ds_read_b128 v[100:103], v27 offset:1024
v_mfma_f32_16x16x32_bf16 v[40:43], v[108:111], v[184:187], v[40:43]
v_mfma_f32_16x16x32_bf16 v[44:47], v[104:107], v[180:183], v[44:47]
v_mfma_f32_16x16x32_bf16 v[44:47], v[108:111], v[188:191], v[44:47]
buffer_load_dwordx4 v1, s[60:63], s55 offen offset:2048 sc1 lds
s_waitcnt lgkmcnt(4)
v_mfma_f32_16x16x32_bf16 v[40:43], v[112:115], v[192:195], v[40:43]
ds_read_b128 v[104:107], v27 offset:2048
ds_read_b128 v[108:111], v27 offset:3072
v_exp_f32 v88, -v32
v_mfma_f32_16x16x32_bf16 v[40:43], v[116:119], v[200:203], v[40:43]
v_exp_f32 v89, -v33
v_exp_f32 v90, -v34
v_mfma_f32_16x16x32_bf16 v[44:47], v[112:115], v[196:199], v[44:47]
v_exp_f32 v91, -v35
v_exp_f32 v92, -v36
v_mfma_f32_16x16x32_bf16 v[44:47], v[116:119], v[204:207], v[44:47]
s_add_u32 m0, s46, 7168
s_add_u32 s55, s44, 71680
buffer_load_dwordx4 v1, s[60:63], s55 offen offset:0 sc1 lds
v_exp_f32 v93, -v37
v_exp_f32 v94, -v38
s_waitcnt lgkmcnt(4)
v_mfma_f32_16x16x32_bf16 v[40:43], v[120:123], v[208:211], v[40:43]
ds_read_b128 v[112:115], v27 offset:4096
ds_read_b128 v[116:119], v27 offset:5120
v_exp_f32 v95, -v39
v_mfma_f32_16x16x32_bf16 v[40:43], v[124:127], v[216:219], v[40:43]
v_add_f32 v88, 1.0, v88
v_add_f32 v89, 1.0, v89
v_add_f32 v90, 1.0, v90
v_add_f32 v91, 1.0, v91
v_mfma_f32_16x16x32_bf16 v[44:47], v[120:123], v[212:215], v[44:47]
v_add_f32 v92, 1.0, v92
v_add_f32 v93, 1.0, v93
v_add_f32 v94, 1.0, v94
v_add_f32 v95, 1.0, v95
v_mfma_f32_16x16x32_bf16 v[44:47], v[124:127], v[220:223], v[44:47]
buffer_load_dwordx4 v1, s[60:63], s55 offen offset:1024 sc1 lds
v_rcp_f32 v88, v88
v_rcp_f32 v89, v89
s_waitcnt lgkmcnt(4)
v_mfma_f32_16x16x32_bf16 v[40:43], v[96:99], v[224:227], v[40:43]
ds_read_b128 v[120:123], v27 offset:6144
ds_read_b128 v[124:127], v27 offset:7168
v_rcp_f32 v90, v90
v_mfma_f32_16x16x32_bf16 v[40:43], v[100:103], v[232:235], v[40:43]
v_rcp_f32 v91, v91
v_rcp_f32 v92, v92
v_mfma_f32_16x16x32_bf16 v[44:47], v[96:99], v[228:231], v[44:47]
v_rcp_f32 v93, v93
v_rcp_f32 v94, v94
v_mfma_f32_16x16x32_bf16 v[44:47], v[100:103], v[236:239], v[44:47]
buffer_load_dwordx4 v1, s[60:63], s55 offen offset:2048 sc1 lds
v_rcp_f32 v95, v95
s_waitcnt lgkmcnt(4)
v_mfma_f32_16x16x32_bf16 v[48:51], v[104:107], v[176:179], v[48:51]
ds_read_b128 v[96:99], v27 offset:8192
ds_read_b128 v[100:103], v27 offset:9216
v_mul_f32 v32, v32, v88
v_mul_f32 v33, v33, v89
v_mfma_f32_16x16x32_bf16 v[48:51], v[108:111], v[184:187], v[48:51]
v_mul_f32 v34, v34, v90
v_mul_f32 v35, v35, v91
v_mul_f32 v36, v36, v92
v_mul_f32 v37, v37, v93
v_mfma_f32_16x16x32_bf16 v[52:55], v[104:107], v[180:183], v[52:55]
v_mul_f32 v38, v38, v94
v_mul_f32 v39, v39, v95
v_cvt_pk_bf16_f32 v240, v32, v33
v_cvt_pk_bf16_f32 v241, v34, v35
v_mfma_f32_16x16x32_bf16 v[52:55], v[108:111], v[188:191], v[52:55]
buffer_load_dwordx4 v1, s[60:63], s55 offen offset:3072 sc1 lds
v_cvt_pk_bf16_f32 v244, v36, v37
v_cvt_pk_bf16_f32 v245, v38, v39
s_barrier
s_waitcnt lgkmcnt(4)
v_mfma_f32_16x16x32_bf16 v[48:51], v[112:115], v[192:195], v[48:51]
ds_read_b128 v[104:107], v27 offset:10240
ds_read_b128 v[108:111], v27 offset:11264
v_exp_f32 v88, -v40
v_mfma_f32_16x16x32_bf16 v[48:51], v[116:119], v[200:203], v[48:51]
v_exp_f32 v89, -v41
v_exp_f32 v90, -v42
v_mfma_f32_16x16x32_bf16 v[52:55], v[112:115], v[196:199], v[52:55]
v_exp_f32 v91, -v43
v_exp_f32 v92, -v44
v_mfma_f32_16x16x32_bf16 v[52:55], v[116:119], v[204:207], v[52:55]
s_add_u32 m0, s46, 11264
s_add_u32 s55, s44, 75776
buffer_load_dwordx4 v1, s[60:63], s55 offen offset:0 sc1 lds
v_exp_f32 v93, -v45
v_exp_f32 v94, -v46
s_waitcnt lgkmcnt(4)
v_mfma_f32_16x16x32_bf16 v[48:51], v[120:123], v[208:211], v[48:51]
ds_read_b128 v[112:115], v27 offset:12288
ds_read_b128 v[116:119], v27 offset:13312
v_exp_f32 v95, -v47
v_mfma_f32_16x16x32_bf16 v[48:51], v[124:127], v[216:219], v[48:51]
v_add_f32 v88, 1.0, v88
v_add_f32 v89, 1.0, v89
v_add_f32 v90, 1.0, v90
v_add_f32 v91, 1.0, v91
v_mfma_f32_16x16x32_bf16 v[52:55], v[120:123], v[212:215], v[52:55]
v_add_f32 v92, 1.0, v92
v_add_f32 v93, 1.0, v93
v_add_f32 v94, 1.0, v94
v_add_f32 v95, 1.0, v95
v_mfma_f32_16x16x32_bf16 v[52:55], v[124:127], v[220:223], v[52:55]
buffer_load_dwordx4 v1, s[60:63], s55 offen offset:1024 sc1 lds
v_rcp_f32 v88, v88
v_rcp_f32 v89, v89
s_waitcnt lgkmcnt(4)
v_mfma_f32_16x16x32_bf16 v[48:51], v[96:99], v[224:227], v[48:51]
ds_read_b128 v[120:123], v27 offset:14336
ds_read_b128 v[124:127], v27 offset:15360
v_rcp_f32 v90, v90
v_mfma_f32_16x16x32_bf16 v[48:51], v[100:103], v[232:235], v[48:51]
v_rcp_f32 v91, v91
v_rcp_f32 v92, v92
v_mfma_f32_16x16x32_bf16 v[52:55], v[96:99], v[228:231], v[52:55]
v_rcp_f32 v93, v93
v_rcp_f32 v94, v94
v_mfma_f32_16x16x32_bf16 v[52:55], v[100:103], v[236:239], v[52:55]
buffer_load_dwordx4 v1, s[60:63], s55 offen offset:2048 sc1 lds
v_rcp_f32 v95, v95
v_mul_f32 v40, v40, v88
v_mul_f32 v41, v41, v89
s_waitcnt lgkmcnt(4)
v_mfma_f32_16x16x32_bf16 v[56:59], v[104:107], v[176:179], v[56:59]
ds_read_b128 v[96:99], v27 offset:16384
ds_read_b128 v[100:103], v27 offset:17408
v_mul_f32 v42, v42, v90
v_mul_f32 v43, v43, v91
v_mfma_f32_16x16x32_bf16 v[56:59], v[108:111], v[184:187], v[56:59]
v_mul_f32 v44, v44, v92
v_mul_f32 v45, v45, v93
v_mul_f32 v46, v46, v94
v_mul_f32 v47, v47, v95
v_mfma_f32_16x16x32_bf16 v[60:63], v[104:107], v[180:183], v[60:63]
v_cvt_pk_bf16_f32 v242, v40, v41
v_cvt_pk_bf16_f32 v243, v42, v43
v_cvt_pk_bf16_f32 v246, v44, v45
v_cvt_pk_bf16_f32 v247, v46, v47
v_mfma_f32_16x16x32_bf16 v[60:63], v[108:111], v[188:191], v[60:63]
s_add_u32 m0, s45, 0
s_add_u32 s55, s44, 78848
buffer_load_dwordx4 v1, s[60:63], s55 offen offset:0 sc1 lds
s_add_u32 s53, s83, 0
buffer_store_dwordx4 v[240:243], v1, s[60:63], s53 offen offset:0 sc1
s_add_u32 s53, s83, 0
buffer_store_dwordx4 v[244:247], v1, s[60:63], s53 offen offset:1024 sc1
s_waitcnt lgkmcnt(4)
v_mfma_f32_16x16x32_bf16 v[56:59], v[112:115], v[192:195], v[56:59]
ds_read_b128 v[104:107], v27 offset:18432
ds_read_b128 v[108:111], v27 offset:19456
v_exp_f32 v88, -v48
v_mfma_f32_16x16x32_bf16 v[56:59], v[116:119], v[200:203], v[56:59]
v_exp_f32 v89, -v49
v_exp_f32 v90, -v50
v_mfma_f32_16x16x32_bf16 v[60:63], v[112:115], v[196:199], v[60:63]
v_exp_f32 v91, -v51
v_exp_f32 v92, -v52
v_mfma_f32_16x16x32_bf16 v[60:63], v[116:119], v[204:207], v[60:63]
buffer_load_dwordx4 v1, s[60:63], s55 offen offset:1024 sc1 lds
v_exp_f32 v93, -v53
v_exp_f32 v94, -v54
s_waitcnt lgkmcnt(4)
v_mfma_f32_16x16x32_bf16 v[56:59], v[120:123], v[208:211], v[56:59]
ds_read_b128 v[112:115], v27 offset:20480
ds_read_b128 v[116:119], v27 offset:21504
v_exp_f32 v95, -v55
v_mfma_f32_16x16x32_bf16 v[56:59], v[124:127], v[216:219], v[56:59]
v_add_f32 v88, 1.0, v88
v_add_f32 v89, 1.0, v89
v_add_f32 v90, 1.0, v90
v_add_f32 v91, 1.0, v91
v_mfma_f32_16x16x32_bf16 v[60:63], v[120:123], v[212:215], v[60:63]
v_add_f32 v92, 1.0, v92
v_add_f32 v93, 1.0, v93
v_add_f32 v94, 1.0, v94
v_add_f32 v95, 1.0, v95
v_mfma_f32_16x16x32_bf16 v[60:63], v[124:127], v[220:223], v[60:63]
buffer_load_dwordx4 v1, s[60:63], s55 offen offset:2048 sc1 lds
v_rcp_f32 v88, v88
v_rcp_f32 v89, v89
s_waitcnt lgkmcnt(4)
v_mfma_f32_16x16x32_bf16 v[56:59], v[96:99], v[224:227], v[56:59]
ds_read_b128 v[120:123], v27 offset:22528
ds_read_b128 v[124:127], v27 offset:23552
v_rcp_f32 v90, v90
v_mfma_f32_16x16x32_bf16 v[56:59], v[100:103], v[232:235], v[56:59]
v_rcp_f32 v91, v91
v_rcp_f32 v92, v92
v_mfma_f32_16x16x32_bf16 v[60:63], v[96:99], v[228:231], v[60:63]
v_rcp_f32 v93, v93
v_rcp_f32 v94, v94
v_mfma_f32_16x16x32_bf16 v[60:63], v[100:103], v[236:239], v[60:63]
buffer_load_dwordx4 v1, s[60:63], s55 offen offset:3072 sc1 lds
v_rcp_f32 v95, v95
s_waitcnt lgkmcnt(4)
v_mfma_f32_16x16x32_bf16 v[64:67], v[104:107], v[176:179], v[64:67]
ds_read_b128 v[96:99], v27 offset:24576
ds_read_b128 v[100:103], v27 offset:25600
v_mul_f32 v48, v48, v88
v_mul_f32 v49, v49, v89
v_mfma_f32_16x16x32_bf16 v[64:67], v[108:111], v[184:187], v[64:67]
v_mul_f32 v50, v50, v90
v_mul_f32 v51, v51, v91
v_mul_f32 v52, v52, v92
v_mul_f32 v53, v53, v93
v_mfma_f32_16x16x32_bf16 v[68:71], v[104:107], v[180:183], v[68:71]
v_mul_f32 v54, v54, v94
v_mul_f32 v55, v55, v95
v_cvt_pk_bf16_f32 v248, v48, v49
v_cvt_pk_bf16_f32 v249, v50, v51
v_mfma_f32_16x16x32_bf16 v[68:71], v[108:111], v[188:191], v[68:71]
s_add_u32 m0, s45, 4096
s_add_u32 s55, s44, 82944
buffer_load_dwordx4 v1, s[60:63], s55 offen offset:0 sc1 lds
v_cvt_pk_bf16_f32 v252, v52, v53
v_cvt_pk_bf16_f32 v253, v54, v55
s_waitcnt lgkmcnt(4)
v_mfma_f32_16x16x32_bf16 v[64:67], v[112:115], v[192:195], v[64:67]
ds_read_b128 v[104:107], v27 offset:26624
ds_read_b128 v[108:111], v27 offset:27648
v_exp_f32 v88, -v56
v_mfma_f32_16x16x32_bf16 v[64:67], v[116:119], v[200:203], v[64:67]
v_exp_f32 v89, -v57
v_exp_f32 v90, -v58
v_mfma_f32_16x16x32_bf16 v[68:71], v[112:115], v[196:199], v[68:71]
v_exp_f32 v91, -v59
v_exp_f32 v92, -v60
v_mfma_f32_16x16x32_bf16 v[68:71], v[116:119], v[204:207], v[68:71]
buffer_load_dwordx4 v1, s[60:63], s55 offen offset:1024 sc1 lds
v_exp_f32 v93, -v61
v_exp_f32 v94, -v62
s_waitcnt lgkmcnt(4)
v_mfma_f32_16x16x32_bf16 v[64:67], v[120:123], v[208:211], v[64:67]
ds_read_b128 v[112:115], v27 offset:28672
ds_read_b128 v[116:119], v27 offset:29696
v_exp_f32 v95, -v63
v_mfma_f32_16x16x32_bf16 v[64:67], v[124:127], v[216:219], v[64:67]
v_add_f32 v88, 1.0, v88
v_add_f32 v89, 1.0, v89
v_add_f32 v90, 1.0, v90
v_add_f32 v91, 1.0, v91
v_mfma_f32_16x16x32_bf16 v[68:71], v[120:123], v[212:215], v[68:71]
v_add_f32 v92, 1.0, v92
v_add_f32 v93, 1.0, v93
v_add_f32 v94, 1.0, v94
v_add_f32 v95, 1.0, v95
v_mfma_f32_16x16x32_bf16 v[68:71], v[124:127], v[220:223], v[68:71]
buffer_load_dwordx4 v1, s[60:63], s55 offen offset:2048 sc1 lds
v_rcp_f32 v88, v88
v_rcp_f32 v89, v89
s_waitcnt lgkmcnt(4)
v_mfma_f32_16x16x32_bf16 v[64:67], v[96:99], v[224:227], v[64:67]
ds_read_b128 v[120:123], v27 offset:30720
ds_read_b128 v[124:127], v27 offset:31744
v_rcp_f32 v90, v90
v_mfma_f32_16x16x32_bf16 v[64:67], v[100:103], v[232:235], v[64:67]
v_rcp_f32 v91, v91
v_rcp_f32 v92, v92
v_mfma_f32_16x16x32_bf16 v[68:71], v[96:99], v[228:231], v[68:71]
v_rcp_f32 v93, v93
v_rcp_f32 v94, v94
v_mfma_f32_16x16x32_bf16 v[68:71], v[100:103], v[236:239], v[68:71]
s_add_u32 m0, s45, 28672
s_add_u32 s55, s44, 86016
buffer_load_dwordx4 v1, s[60:63], s55 offen offset:0 sc1 lds
v_rcp_f32 v95, v95
v_mul_f32 v56, v56, v88
v_mul_f32 v57, v57, v89
s_waitcnt lgkmcnt(4)
v_mfma_f32_16x16x32_bf16 v[72:75], v[104:107], v[176:179], v[72:75]
ds_read_b128 v[96:99], v27 offset:32768
ds_read_b128 v[100:103], v27 offset:33792
v_mul_f32 v58, v58, v90
v_mul_f32 v59, v59, v91
v_mfma_f32_16x16x32_bf16 v[72:75], v[108:111], v[184:187], v[72:75]
v_mul_f32 v60, v60, v92
v_mul_f32 v61, v61, v93
v_mul_f32 v62, v62, v94
v_mul_f32 v63, v63, v95
v_mfma_f32_16x16x32_bf16 v[76:79], v[104:107], v[180:183], v[76:79]
v_cvt_pk_bf16_f32 v250, v56, v57
v_cvt_pk_bf16_f32 v251, v58, v59
v_cvt_pk_bf16_f32 v254, v60, v61
v_cvt_pk_bf16_f32 v255, v62, v63
v_mfma_f32_16x16x32_bf16 v[76:79], v[108:111], v[188:191], v[76:79]
buffer_load_dwordx4 v1, s[60:63], s55 offen offset:1024 sc1 lds
s_add_u32 s53, s83, 0
buffer_store_dwordx4 v[248:251], v1, s[60:63], s53 offen offset:2048 sc1
s_add_u32 s53, s83, 0
buffer_store_dwordx4 v[252:255], v1, s[60:63], s53 offen offset:3072 sc1
s_waitcnt lgkmcnt(4)
v_mfma_f32_16x16x32_bf16 v[72:75], v[112:115], v[192:195], v[72:75]
ds_read_b128 v[104:107], v27 offset:34816
ds_read_b128 v[108:111], v27 offset:35840
v_exp_f32 v88, -v64
v_mfma_f32_16x16x32_bf16 v[72:75], v[116:119], v[200:203], v[72:75]
v_exp_f32 v89, -v65
v_exp_f32 v90, -v66
v_mfma_f32_16x16x32_bf16 v[76:79], v[112:115], v[196:199], v[76:79]
v_exp_f32 v91, -v67
v_exp_f32 v92, -v68
v_mfma_f32_16x16x32_bf16 v[76:79], v[116:119], v[204:207], v[76:79]
buffer_load_dwordx4 v1, s[60:63], s55 offen offset:2048 sc1 lds
v_exp_f32 v93, -v69
v_exp_f32 v94, -v70
s_waitcnt lgkmcnt(4)
v_mfma_f32_16x16x32_bf16 v[72:75], v[120:123], v[208:211], v[72:75]
ds_read_b128 v[112:115], v27 offset:36864
ds_read_b128 v[116:119], v27 offset:37888
v_exp_f32 v95, -v71
v_mfma_f32_16x16x32_bf16 v[72:75], v[124:127], v[216:219], v[72:75]
v_add_f32 v88, 1.0, v88
v_add_f32 v89, 1.0, v89
v_add_f32 v90, 1.0, v90
v_add_f32 v91, 1.0, v91
s_waitcnt vmcnt(1)
buffer_store_dwordx2 v[30:31], v29, s[60:63], s89 offen offset:0 sc1
v_mfma_f32_16x16x32_bf16 v[76:79], v[120:123], v[212:215], v[76:79]
v_add_f32 v92, 1.0, v92
v_add_f32 v93, 1.0, v93
v_add_f32 v94, 1.0, v94
v_add_f32 v95, 1.0, v95
v_mfma_f32_16x16x32_bf16 v[76:79], v[124:127], v[220:223], v[76:79]
buffer_load_dwordx4 v1, s[60:63], s55 offen offset:3072 sc1 lds
v_rcp_f32 v88, v88
v_rcp_f32 v89, v89
s_waitcnt lgkmcnt(4)
v_mfma_f32_16x16x32_bf16 v[72:75], v[96:99], v[224:227], v[72:75]
ds_read_b128 v[120:123], v27 offset:38912
ds_read_b128 v[124:127], v27 offset:39936
v_rcp_f32 v90, v90
v_mfma_f32_16x16x32_bf16 v[72:75], v[100:103], v[232:235], v[72:75]
v_rcp_f32 v91, v91
v_rcp_f32 v92, v92
v_mfma_f32_16x16x32_bf16 v[76:79], v[96:99], v[228:231], v[76:79]
v_rcp_f32 v93, v93
v_rcp_f32 v94, v94
v_mfma_f32_16x16x32_bf16 v[76:79], v[100:103], v[236:239], v[76:79]
s_add_u32 m0, s45, 32768
s_add_u32 s55, s44, 90112
buffer_load_dwordx4 v1, s[60:63], s55 offen offset:0 sc1 lds
v_rcp_f32 v95, v95
s_waitcnt lgkmcnt(4)
v_mfma_f32_16x16x32_bf16 v[80:83], v[104:107], v[176:179], v[80:83]
ds_read_b128 v[96:99], v27 offset:40960
ds_read_b128 v[100:103], v27 offset:41984
v_mul_f32 v64, v64, v88
v_mul_f32 v65, v65, v89
v_mfma_f32_16x16x32_bf16 v[80:83], v[108:111], v[184:187], v[80:83]
v_mul_f32 v66, v66, v90
v_mul_f32 v67, v67, v91
v_mul_f32 v68, v68, v92
v_mul_f32 v69, v69, v93
v_mfma_f32_16x16x32_bf16 v[84:87], v[104:107], v[180:183], v[84:87]
v_mul_f32 v70, v70, v94
v_mul_f32 v71, v71, v95
v_cvt_pk_bf16_f32 v240, v64, v65
v_cvt_pk_bf16_f32 v241, v66, v67
v_mfma_f32_16x16x32_bf16 v[84:87], v[108:111], v[188:191], v[84:87]
buffer_load_dwordx4 v1, s[60:63], s55 offen offset:1024 sc1 lds
v_cvt_pk_bf16_f32 v244, v68, v69
v_cvt_pk_bf16_f32 v245, v70, v71
s_waitcnt lgkmcnt(4)
v_mfma_f32_16x16x32_bf16 v[80:83], v[112:115], v[192:195], v[80:83]
v_exp_f32 v88, -v72
v_mfma_f32_16x16x32_bf16 v[80:83], v[116:119], v[200:203], v[80:83]
v_exp_f32 v89, -v73
v_exp_f32 v90, -v74
v_mfma_f32_16x16x32_bf16 v[84:87], v[112:115], v[196:199], v[84:87]
v_exp_f32 v91, -v75
v_exp_f32 v92, -v76
v_mfma_f32_16x16x32_bf16 v[84:87], v[116:119], v[204:207], v[84:87]
buffer_load_dwordx4 v1, s[60:63], s55 offen offset:2048 sc1 lds
v_exp_f32 v93, -v77
v_exp_f32 v94, -v78
s_waitcnt lgkmcnt(2)
v_mfma_f32_16x16x32_bf16 v[80:83], v[120:123], v[208:211], v[80:83]
v_exp_f32 v95, -v79
v_mfma_f32_16x16x32_bf16 v[80:83], v[124:127], v[216:219], v[80:83]
v_add_f32 v88, 1.0, v88
v_add_f32 v89, 1.0, v89
v_add_f32 v90, 1.0, v90
v_add_f32 v91, 1.0, v91
v_mfma_f32_16x16x32_bf16 v[84:87], v[120:123], v[212:215], v[84:87]
v_add_f32 v92, 1.0, v92
v_add_f32 v93, 1.0, v93
v_add_f32 v94, 1.0, v94
v_add_f32 v95, 1.0, v95
v_mfma_f32_16x16x32_bf16 v[84:87], v[124:127], v[220:223], v[84:87]
v_rcp_f32 v88, v88
v_rcp_f32 v89, v89
s_waitcnt lgkmcnt(0)
v_mfma_f32_16x16x32_bf16 v[80:83], v[96:99], v[224:227], v[80:83]
v_rcp_f32 v90, v90
v_mfma_f32_16x16x32_bf16 v[80:83], v[100:103], v[232:235], v[80:83]
v_rcp_f32 v91, v91
v_rcp_f32 v92, v92
v_mfma_f32_16x16x32_bf16 v[84:87], v[96:99], v[228:231], v[84:87]
v_rcp_f32 v93, v93
v_rcp_f32 v94, v94
v_mfma_f32_16x16x32_bf16 v[84:87], v[100:103], v[236:239], v[84:87]
v_rcp_f32 v95, v95
v_mul_f32 v72, v72, v88
v_mul_f32 v73, v73, v89
s_buffer_load_dwordx8 s[0:7], s[60:63], s90 glc
v_mul_f32 v74, v74, v90
v_mul_f32 v75, v75, v91
v_mul_f32 v76, v76, v92
v_mul_f32 v77, v77, v93
v_mul_f32 v78, v78, v94
v_mul_f32 v79, v79, v95
v_cvt_pk_bf16_f32 v242, v72, v73
v_cvt_pk_bf16_f32 v243, v74, v75
v_cvt_pk_bf16_f32 v246, v76, v77
v_cvt_pk_bf16_f32 v247, v78, v79
s_add_u32 s53, s83, 4096
buffer_store_dwordx4 v[240:243], v1, s[60:63], s53 offen offset:0 sc1
s_add_u32 s53, s83, 4096
buffer_store_dwordx4 v[244:247], v1, s[60:63], s53 offen offset:1024 sc1
s_nop 7
v_exp_f32 v88, -v80
v_exp_f32 v89, -v81
v_exp_f32 v90, -v82
v_exp_f32 v91, -v83
v_exp_f32 v92, -v84
v_exp_f32 v93, -v85
v_exp_f32 v94, -v86
v_exp_f32 v95, -v87
v_add_f32 v88, 1.0, v88
v_add_f32 v89, 1.0, v89
v_add_f32 v90, 1.0, v90
v_add_f32 v91, 1.0, v91
v_add_f32 v92, 1.0, v92
v_add_f32 v93, 1.0, v93
v_add_f32 v94, 1.0, v94
v_add_f32 v95, 1.0, v95
v_rcp_f32 v88, v88
v_rcp_f32 v89, v89
v_rcp_f32 v90, v90
v_rcp_f32 v91, v91
v_rcp_f32 v92, v92
v_rcp_f32 v93, v93
v_rcp_f32 v94, v94
v_rcp_f32 v95, v95
v_mul_f32 v80, v80, v88
v_mul_f32 v81, v81, v89
v_mul_f32 v82, v82, v90
v_mul_f32 v83, v83, v91
v_mul_f32 v84, v84, v92
v_mul_f32 v85, v85, v93
v_mul_f32 v86, v86, v94
v_mul_f32 v87, v87, v95
v_cvt_pk_bf16_f32 v20, v80, v81
v_cvt_pk_bf16_f32 v21, v82, v83
v_cvt_pk_bf16_f32 v22, v84, v85
v_cvt_pk_bf16_f32 v23, v86, v87
s_add_u32 s53, s83, 4096
buffer_store_dwordx4 v[20:23], v1, s[60:63], s53 offen offset:2048 sc1
s_waitcnt lgkmcnt(0)
s_xor_b64 s[0:1], s[0:1], s[76:77]
s_xor_b64 s[2:3], s[2:3], s[76:77]
s_xor_b64 s[4:5], s[4:5], s[76:77]
s_xor_b64 s[6:7], s[6:7], s[76:77]
s_or_b64 s[0:1], s[0:1], s[2:3]
s_or_b64 s[4:5], s[4:5], s[6:7]
s_or_b64 s[0:1], s[0:1], s[4:5]
s_cbranch_scc0 L_f3a%=
s_mov_b32 s47, 262144
L_sp4%=:
s_buffer_load_dwordx8 s[0:7], s[60:63], s90 glc
s_waitcnt lgkmcnt(0)
s_xor_b64 s[0:1], s[0:1], s[76:77]
s_xor_b64 s[2:3], s[2:3], s[76:77]
s_xor_b64 s[4:5], s[4:5], s[76:77]
s_xor_b64 s[6:7], s[6:7], s[76:77]
s_or_b64 s[0:1], s[0:1], s[4:5]
s_or_b64 s[2:3], s[2:3], s[6:7]
s_or_b64 s[0:1], s[0:1], s[2:3]
s_cbranch_scc0 L_spd4%=
s_sub_u32 s47, s47, 1
s_cbranch_scc0 L_sp4%=
L_spd4%=:
L_f3a%=:
s_add_u32 s54, s96, 0
buffer_load_dwordx4 v[128:131], v1, s[60:63], s54 offen offset:0 sc1
s_add_u32 s54, s96, 0
buffer_load_dwordx4 v[132:135], v1, s[60:63], s54 offen offset:1024 sc1
s_add_u32 s54, s96, 4096
buffer_load_dwordx4 v[156:159], v1, s[60:63], s54 offen offset:3072 sc1
s_add_u32 s54, s96, 8192
buffer_load_dwordx4 v[160:163], v1, s[60:63], s54 offen offset:0 sc1
s_add_u32 s54, s96, 12288
buffer_load_dwordx4 v[184:187], v1, s[60:63], s54 offen offset:2048 sc1
s_add_u32 s54, s96, 12288
buffer_load_dwordx4 v[188:191], v1, s[60:63], s54 offen offset:3072 sc1
s_add_u32 s54, s96, 20480
buffer_load_dwordx4 v[212:215], v1, s[60:63], s54 offen offset:1024 sc1
s_add_u32 s54, s96, 20480
buffer_load_dwordx4 v[216:219], v1, s[60:63], s54 offen offset:2048 sc1
s_add_u32 s54, s96, 0
buffer_load_dwordx4 v[136:139], v1, s[60:63], s54 offen offset:2048 sc1
s_add_u32 s54, s96, 0
buffer_load_dwordx4 v[140:143], v1, s[60:63], s54 offen offset:3072 sc1
s_add_u32 s54, s96, 8192
buffer_load_dwordx4 v[164:167], v1, s[60:63], s54 offen offset:1024 sc1
s_add_u32 s54, s96, 8192
buffer_load_dwordx4 v[168:171], v1, s[60:63], s54 offen offset:2048 sc1
s_add_u32 s89, s89, 32768
s_add_u32 s90, s90, 32768
s_waitcnt vmcnt(12)
buffer_store_dwordx2 v[30:31], v29, s[60:63], s89 offen offset:0 sc1
s_barrier
s_mov_b32 s53, 0x3f317218
s_waitcnt vmcnt(11)
v_mfma_f32_16x16x32_bf16 v[32:35], a[8:11], v[128:131], 0
s_add_u32 s54, s96, 16384
buffer_load_dwordx4 v[192:195], v1, s[60:63], s54 offen offset:0 sc1
v_mfma_f32_16x16x32_bf16 v[36:39], a[8:11], v[132:135], 0
v_mfma_f32_16x16x32_bf16 v[40:43], a[12:15], v[128:131], 0
v_mfma_f32_16x16x32_bf16 v[44:47], a[12:15], v[132:135], 0
v_mfma_f32_16x16x32_bf16 v[48:51], a[24:27], v[128:131], 0
s_add_u32 s54, s96, 16384
buffer_load_dwordx4 v[196:199], v1, s[60:63], s54 offen offset:1024 sc1
v_mfma_f32_16x16x32_bf16 v[52:55], a[24:27], v[132:135], 0
v_mfma_f32_16x16x32_bf16 v[56:59], a[28:31], v[128:131], 0
v_mfma_f32_16x16x32_bf16 v[60:63], a[28:31], v[132:135], 0
v_mfma_f32_16x16x32_bf16 v[64:67], a[40:43], v[128:131], 0
s_add_u32 s54, s96, 20480
buffer_load_dwordx4 v[220:223], v1, s[60:63], s54 offen offset:3072 sc1
v_mfma_f32_16x16x32_bf16 v[68:71], a[40:43], v[132:135], 0
v_mfma_f32_16x16x32_bf16 v[72:75], a[44:47], v[128:131], 0
v_mfma_f32_16x16x32_bf16 v[76:79], a[44:47], v[132:135], 0
v_mfma_f32_16x16x32_bf16 v[80:83], a[56:59], v[128:131], 0
s_add_u32 s54, s96, 24576
buffer_load_dwordx4 v[224:227], v1, s[60:63], s54 offen offset:0 sc1
v_mfma_f32_16x16x32_bf16 v[84:87], a[56:59], v[132:135], 0
s_add_u32 m0, s45, 57344
s_add_u32 s55, s44, 93184
buffer_load_dwordx4 v1, s[60:63], s55 offen offset:0 sc1 lds
s_waitcnt vmcnt(14)
v_mfma_f32_16x16x32_bf16 v[32:35], a[60:63], v[156:159], v[32:35]
v_mfma_f32_16x16x32_bf16 v[36:39], a[60:63], v[160:163], v[36:39]
buffer_load_dwordx4 v1, s[60:63], s55 offen offset:1024 sc1 lds
v_mfma_f32_16x16x32_bf16 v[40:43], a[72:75], v[156:159], v[40:43]
v_mfma_f32_16x16x32_bf16 v[44:47], a[72:75], v[160:163], v[44:47]
v_mfma_f32_16x16x32_bf16 v[48:51], a[76:79], v[156:159], v[48:51]
buffer_load_dwordx4 v1, s[60:63], s55 offen offset:2048 sc1 lds
v_mfma_f32_16x16x32_bf16 v[52:55], a[76:79], v[160:163], v[52:55]
v_mfma_f32_16x16x32_bf16 v[56:59], a[88:91], v[156:159], v[56:59]
v_mfma_f32_16x16x32_bf16 v[60:63], a[88:91], v[160:163], v[60:63]
buffer_load_dwordx4 v1, s[60:63], s55 offen offset:3072 sc1 lds
v_mfma_f32_16x16x32_bf16 v[64:67], a[92:95], v[156:159], v[64:67]
v_mfma_f32_16x16x32_bf16 v[68:71], a[92:95], v[160:163], v[68:71]
v_mfma_f32_16x16x32_bf16 v[72:75], a[104:107], v[156:159], v[72:75]
s_add_u32 m0, s45, 61440
s_add_u32 s55, s44, 97280
buffer_load_dwordx4 v1, s[60:63], s55 offen offset:0 sc1 lds
v_mfma_f32_16x16x32_bf16 v[76:79], a[104:107], v[160:163], v[76:79]
v_mfma_f32_16x16x32_bf16 v[80:83], a[108:111], v[156:159], v[80:83]
v_mfma_f32_16x16x32_bf16 v[84:87], a[108:111], v[160:163], v[84:87]
buffer_load_dwordx4 v1, s[60:63], s55 offen offset:1024 sc1 lds
s_waitcnt vmcnt(17)
v_mfma_f32_16x16x32_bf16 v[32:35], a[120:123], v[184:187], v[32:35]
v_mfma_f32_16x16x32_bf16 v[36:39], a[120:123], v[188:191], v[36:39]
buffer_load_dwordx4 v1, s[60:63], s55 offen offset:2048 sc1 lds
v_mfma_f32_16x16x32_bf16 v[40:43], a[124:127], v[184:187], v[40:43]
v_mfma_f32_16x16x32_bf16 v[44:47], a[124:127], v[188:191], v[44:47]
v_mfma_f32_16x16x32_bf16 v[48:51], a[136:139], v[184:187], v[48:51]
v_mfma_f32_16x16x32_bf16 v[52:55], a[136:139], v[188:191], v[52:55]
v_mfma_f32_16x16x32_bf16 v[56:59], a[140:143], v[184:187], v[56:59]
v_mfma_f32_16x16x32_bf16 v[60:63], a[140:143], v[188:191], v[60:63]
v_mfma_f32_16x16x32_bf16 v[64:67], a[152:155], v[184:187], v[64:67]
v_mfma_f32_16x16x32_bf16 v[68:71], a[152:155], v[188:191], v[68:71]
v_mfma_f32_16x16x32_bf16 v[72:75], a[156:159], v[184:187], v[72:75]
v_mfma_f32_16x16x32_bf16 v[76:79], a[156:159], v[188:191], v[76:79]
v_mfma_f32_16x16x32_bf16 v[80:83], a[168:171], v[184:187], v[80:83]
v_mfma_f32_16x16x32_bf16 v[84:87], a[168:171], v[188:191], v[84:87]
s_waitcnt vmcnt(16)
v_mfma_f32_16x16x32_bf16 v[32:35], a[172:175], v[212:215], v[32:35]
v_mfma_f32_16x16x32_bf16 v[36:39], a[172:175], v[216:219], v[36:39]
v_mfma_f32_16x16x32_bf16 v[40:43], a[184:187], v[212:215], v[40:43]
s_buffer_load_dwordx8 s[0:7], s[60:63], s90 glc
v_mfma_f32_16x16x32_bf16 v[44:47], a[184:187], v[216:219], v[44:47]
v_mfma_f32_16x16x32_bf16 v[48:51], a[188:191], v[212:215], v[48:51]
v_mfma_f32_16x16x32_bf16 v[52:55], a[188:191], v[216:219], v[52:55]
v_mfma_f32_16x16x32_bf16 v[56:59], a[200:203], v[212:215], v[56:59]
v_mfma_f32_16x16x32_bf16 v[60:63], a[200:203], v[216:219], v[60:63]
v_mfma_f32_16x16x32_bf16 v[64:67], a[204:207], v[212:215], v[64:67]
v_mfma_f32_16x16x32_bf16 v[68:71], a[204:207], v[216:219], v[68:71]
v_mfma_f32_16x16x32_bf16 v[72:75], a[216:219], v[212:215], v[72:75]
v_mfma_f32_16x16x32_bf16 v[76:79], a[216:219], v[216:219], v[76:79]
v_mfma_f32_16x16x32_bf16 v[80:83], a[220:223], v[212:215], v[80:83]
v_mfma_f32_16x16x32_bf16 v[84:87], a[220:223], v[216:219], v[84:87]
s_waitcnt vmcnt(14)
v_mfma_f32_16x16x32_bf16 v[32:35], a[232:235], v[136:139], v[32:35]
s_waitcnt lgkmcnt(0)
s_xor_b64 s[0:1], s[0:1], s[76:77]
s_xor_b64 s[2:3], s[2:3], s[76:77]
s_xor_b64 s[4:5], s[4:5], s[76:77]
s_xor_b64 s[6:7], s[6:7], s[76:77]
v_mfma_f32_16x16x32_bf16 v[36:39], a[232:235], v[140:143], v[36:39]
s_or_b64 s[0:1], s[0:1], s[2:3]
s_or_b64 s[4:5], s[4:5], s[6:7]
s_or_b64 s[0:1], s[0:1], s[4:5]
s_cbranch_scc0 L_f3b%=
s_mov_b32 s47, 262144
L_sp5%=:
s_buffer_load_dwordx8 s[0:7], s[60:63], s90 glc
s_waitcnt lgkmcnt(0)
s_xor_b64 s[0:1], s[0:1], s[76:77]
s_xor_b64 s[2:3], s[2:3], s[76:77]
s_xor_b64 s[4:5], s[4:5], s[76:77]
s_xor_b64 s[6:7], s[6:7], s[76:77]
s_or_b64 s[0:1], s[0:1], s[4:5]
s_or_b64 s[2:3], s[2:3], s[6:7]
s_or_b64 s[0:1], s[0:1], s[2:3]
s_cbranch_scc0 L_spd5%=
s_sub_u32 s47, s47, 1
s_cbranch_scc0 L_sp5%=
L_spd5%=:
L_f3b%=:
v_mfma_f32_16x16x32_bf16 v[40:43], a[236:239], v[136:139], v[40:43]
s_add_u32 s54, s96, 4096
buffer_load_dwordx4 v[144:147], v1, s[60:63], s54 offen offset:0 sc1
v_mfma_f32_16x16x32_bf16 v[44:47], a[236:239], v[140:143], v[44:47]
v_mfma_f32_16x16x32_bf16 v[48:51], a[248:251], v[136:139], v[48:51]
v_mfma_f32_16x16x32_bf16 v[52:55], a[248:251], v[140:143], v[52:55]
s_add_u32 s54, s96, 4096
buffer_load_dwordx4 v[148:151], v1, s[60:63], s54 offen offset:1024 sc1
v_mfma_f32_16x16x32_bf16 v[56:59], a[252:255], v[136:139], v[56:59]
v_mfma_f32_16x16x32_bf16 v[60:63], a[252:255], v[140:143], v[60:63]
v_mfma_f32_16x16x32_bf16 v[64:67], a[0:3], v[136:139], v[64:67]
s_add_u32 s54, s96, 8192
buffer_load_dwordx4 v[172:175], v1, s[60:63], s54 offen offset:3072 sc1
v_mfma_f32_16x16x32_bf16 v[68:71], a[0:3], v[140:143], v[68:71]
v_mfma_f32_16x16x32_bf16 v[72:75], a[4:7], v[136:139], v[72:75]
v_mfma_f32_16x16x32_bf16 v[76:79], a[4:7], v[140:143], v[76:79]
s_add_u32 s54, s96, 12288
buffer_load_dwordx4 v[176:179], v1, s[60:63], s54 offen offset:0 sc1
v_mfma_f32_16x16x32_bf16 v[80:83], a[16:19], v[136:139], v[80:83]
v_mfma_f32_16x16x32_bf16 v[84:87], a[16:19], v[140:143], v[84:87]
s_waitcnt vmcnt(16)
v_mfma_f32_16x16x32_bf16 v[32:35], a[20:23], v[164:167], v[32:35]
v_mfma_f32_16x16x32_bf16 v[36:39], a[20:23], v[168:171], v[36:39]
v_mfma_f32_16x16x32_bf16 v[40:43], a[32:35], v[164:167], v[40:43]
s_add_u32 s54, s96, 16384
buffer_load_dwordx4 v[200:203], v1, s[60:63], s54 offen offset:2048 sc1
v_mfma_f32_16x16x32_bf16 v[44:47], a[32:35], v[168:171], v[44:47]
v_mfma_f32_16x16x32_bf16 v[48:51], a[36:39], v[164:167], v[48:51]
v_mfma_f32_16x16x32_bf16 v[52:55], a[36:39], v[168:171], v[52:55]
s_add_u32 s54, s96, 16384
buffer_load_dwordx4 v[204:207], v1, s[60:63], s54 offen offset:3072 sc1
v_mfma_f32_16x16x32_bf16 v[56:59], a[48:51], v[164:167], v[56:59]
v_mfma_f32_16x16x32_bf16 v[60:63], a[48:51], v[168:171], v[60:63]
v_mfma_f32_16x16x32_bf16 v[64:67], a[52:55], v[164:167], v[64:67]
s_add_u32 s54, s96, 24576
buffer_load_dwordx4 v[228:231], v1, s[60:63], s54 offen offset:1024 sc1
v_mfma_f32_16x16x32_bf16 v[68:71], a[52:55], v[168:171], v[68:71]
v_mfma_f32_16x16x32_bf16 v[72:75], a[64:67], v[164:167], v[72:75]
v_mfma_f32_16x16x32_bf16 v[76:79], a[64:67], v[168:171], v[76:79]
s_add_u32 s54, s96, 24576
buffer_load_dwordx4 v[232:235], v1, s[60:63], s54 offen offset:2048 sc1
v_mfma_f32_16x16x32_bf16 v[80:83], a[68:71], v[164:167], v[80:83]
v_mfma_f32_16x16x32_bf16 v[84:87], a[68:71], v[168:171], v[84:87]
s_waitcnt vmcnt(17)
v_mfma_f32_16x16x32_bf16 v[32:35], a[80:83], v[192:195], v[32:35]
v_mfma_f32_16x16x32_bf16 v[36:39], a[80:83], v[196:199], v[36:39]
v_mfma_f32_16x16x32_bf16 v[40:43], a[84:87], v[192:195], v[40:43]
s_add_u32 s54, s96, 4096
buffer_load_dwordx4 v[240:243], v1, s[60:63], s54 offen offset:2048 sc1
v_mfma_f32_16x16x32_bf16 v[44:47], a[84:87], v[196:199], v[44:47]
v_mfma_f32_16x16x32_bf16 v[48:51], a[96:99], v[192:195], v[48:51]
v_mfma_f32_16x16x32_bf16 v[52:55], a[96:99], v[196:199], v[52:55]
s_add_u32 s54, s96, 12288
buffer_load_dwordx4 v[244:247], v1, s[60:63], s54 offen offset:1024 sc1
v_mfma_f32_16x16x32_bf16 v[56:59], a[100:103], v[192:195], v[56:59]
v_mfma_f32_16x16x32_bf16 v[60:63], a[100:103], v[196:199], v[60:63]
v_mfma_f32_16x16x32_bf16 v[64:67], a[112:115], v[192:195], v[64:67]
s_add_u32 s54, s96, 20480
buffer_load_dwordx4 v[248:251], v1, s[60:63], s54 offen offset:0 sc1
v_mfma_f32_16x16x32_bf16 v[68:71], a[112:115], v[196:199], v[68:71]
v_mfma_f32_16x16x32_bf16 v[72:75], a[116:119], v[192:195], v[72:75]
v_mfma_f32_16x16x32_bf16 v[76:79], a[116:119], v[196:199], v[76:79]
s_add_u32 s54, s96, 24576
buffer_load_dwordx4 v[252:255], v1, s[60:63], s54 offen offset:3072 sc1
v_mfma_f32_16x16x32_bf16 v[80:83], a[128:131], v[192:195], v[80:83]
v_mfma_f32_16x16x32_bf16 v[84:87], a[128:131], v[196:199], v[84:87]
s_waitcnt vmcnt(19)
v_mfma_f32_16x16x32_bf16 v[32:35], a[132:135], v[220:223], v[32:35]
v_mfma_f32_16x16x32_bf16 v[36:39], a[132:135], v[224:227], v[36:39]
v_mfma_f32_16x16x32_bf16 v[40:43], a[144:147], v[220:223], v[40:43]
s_add_u32 s54, s98, 4194304
buffer_load_dwordx4 v[152:155], v2, s[60:63], s54 offen offset:0 sc1
v_mfma_f32_16x16x32_bf16 v[44:47], a[144:147], v[224:227], v[44:47]
v_mfma_f32_16x16x32_bf16 v[48:51], a[148:151], v[220:223], v[48:51]
v_mfma_f32_16x16x32_bf16 v[52:55], a[148:151], v[224:227], v[52:55]
s_add_u32 s54, s98, 4208640
buffer_load_dwordx4 v[180:183], v2, s[60:63], s54 offen offset:0 sc1
v_mfma_f32_16x16x32_bf16 v[56:59], a[160:163], v[220:223], v[56:59]
v_mfma_f32_16x16x32_bf16 v[60:63], a[160:163], v[224:227], v[60:63]
v_mfma_f32_16x16x32_bf16 v[64:67], a[164:167], v[220:223], v[64:67]
buffer_load_dwordx4 v[16:19], v3, s[60:63], s99 offen offset:0 sc1
v_mfma_f32_16x16x32_bf16 v[68:71], a[164:167], v[224:227], v[68:71]
v_mfma_f32_16x16x32_bf16 v[72:75], a[176:179], v[220:223], v[72:75]
v_mfma_f32_16x16x32_bf16 v[76:79], a[176:179], v[224:227], v[76:79]
v_mfma_f32_16x16x32_bf16 v[80:83], a[180:183], v[220:223], v[80:83]
v_mfma_f32_16x16x32_bf16 v[84:87], a[180:183], v[224:227], v[84:87]
s_waitcnt vmcnt(13)
v_mfma_f32_16x16x32_bf16 v[32:35], a[192:195], v[144:147], v[32:35]
s_waitcnt vmcnt(0)
v_lshlrev_b32 v88, 16, v16
v_and_b32 v89, s97, v16
v_mfma_f32_16x16x32_bf16 v[36:39], a[192:195], v[148:151], v[36:39]
v_lshlrev_b32 v90, 16, v17
v_and_b32 v91, s97, v17
v_mfma_f32_16x16x32_bf16 v[40:43], a[196:199], v[144:147], v[40:43]
v_lshlrev_b32 v92, 16, v18
v_and_b32 v93, s97, v18
v_mfma_f32_16x16x32_bf16 v[44:47], a[196:199], v[148:151], v[44:47]
v_lshlrev_b32 v94, 16, v19
v_and_b32 v95, s97, v19
v_mfma_f32_16x16x32_bf16 v[48:51], a[208:211], v[144:147], v[48:51]
v_mul_f32 v88, s53, v88
v_mul_f32 v89, s53, v89
ds_read_b128 v[124:127], v26 offset:0
v_mfma_f32_16x16x32_bf16 v[52:55], a[208:211], v[148:151], v[52:55]
v_mul_f32 v90, s53, v90
v_mul_f32 v91, s53, v91
v_mfma_f32_16x16x32_bf16 v[56:59], a[212:215], v[144:147], v[56:59]
v_mul_f32 v92, s53, v92
v_mul_f32 v93, s53, v93
ds_read_b128 v[96:99], v26 offset:1024
v_mfma_f32_16x16x32_bf16 v[60:63], a[212:215], v[148:151], v[60:63]
v_mul_f32 v94, s53, v94
v_mul_f32 v95, s53, v95
v_mfma_f32_16x16x32_bf16 v[64:67], a[224:227], v[144:147], v[64:67]
ds_read_b128 v[100:103], v26 offset:2048
v_mfma_f32_16x16x32_bf16 v[68:71], a[224:227], v[148:151], v[68:71]
v_mfma_f32_16x16x32_bf16 v[72:75], a[228:231], v[144:147], v[72:75]
ds_read_b128 v[104:107], v26 offset:3072
v_mfma_f32_16x16x32_bf16 v[76:79], a[228:231], v[148:151], v[76:79]
v_mfma_f32_16x16x32_bf16 v[80:83], a[240:243], v[144:147], v[80:83]
ds_read_b128 v[108:111], v26 offset:4096
v_mfma_f32_16x16x32_bf16 v[84:87], a[240:243], v[148:151], v[84:87]
s_waitcnt vmcnt(11)
s_waitcnt lgkmcnt(4)
v_mfma_f32_16x16x32_bf16 v[32:35], v[124:127], v[172:175], v[32:35]
ds_read_b128 v[112:115], v26 offset:5120
v_mfma_f32_16x16x32_bf16 v[36:39], v[124:127], v[176:179], v[36:39]
s_waitcnt lgkmcnt(4)
v_mfma_f32_16x16x32_bf16 v[40:43], v[96:99], v[172:175], v[40:43]
ds_read_b128 v[116:119], v26 offset:6144
v_mfma_f32_16x16x32_bf16 v[44:47], v[96:99], v[176:179], v[44:47]
s_waitcnt lgkmcnt(4)
v_mfma_f32_16x16x32_bf16 v[48:51], v[100:103], v[172:175], v[48:51]
ds_read_b128 v[120:123], v26 offset:7168
v_mfma_f32_16x16x32_bf16 v[52:55], v[100:103], v[176:179], v[52:55]
s_waitcnt lgkmcnt(4)
v_mfma_f32_16x16x32_bf16 v[56:59], v[104:107], v[172:175], v[56:59]
ds_read_b128 v[124:127], v26 offset:8192
v_mfma_f32_16x16x32_bf16 v[60:63], v[104:107], v[176:179], v[60:63]
s_waitcnt lgkmcnt(4)
v_mfma_f32_16x16x32_bf16 v[64:67], v[108:111], v[172:175], v[64:67]
ds_read_b128 v[96:99], v26 offset:9216
v_mfma_f32_16x16x32_bf16 v[68:71], v[108:111], v[176:179], v[68:71]
s_waitcnt lgkmcnt(4)
v_mfma_f32_16x16x32_bf16 v[72:75], v[112:115], v[172:175], v[72:75]
ds_read_b128 v[100:103], v26 offset:10240
v_mfma_f32_16x16x32_bf16 v[76:79], v[112:115], v[176:179], v[76:79]
s_waitcnt lgkmcnt(4)
v_mfma_f32_16x16x32_bf16 v[80:83], v[116:119], v[172:175], v[80:83]
ds_read_b128 v[104:107], v26 offset:11264
v_mfma_f32_16x16x32_bf16 v[84:87], v[116:119], v[176:179], v[84:87]
s_waitcnt vmcnt(9)
s_waitcnt lgkmcnt(4)
v_mfma_f32_16x16x32_bf16 v[32:35], v[120:123], v[200:203], v[32:35]
ds_read_b128 v[108:111], v26 offset:12288
v_mfma_f32_16x16x32_bf16 v[36:39], v[120:123], v[204:207], v[36:39]
s_waitcnt lgkmcnt(4)
v_mfma_f32_16x16x32_bf16 v[40:43], v[124:127], v[200:203], v[40:43]
ds_read_b128 v[112:115], v26 offset:13312
v_mfma_f32_16x16x32_bf16 v[44:47], v[124:127], v[204:207], v[44:47]
s_waitcnt lgkmcnt(4)
v_mfma_f32_16x16x32_bf16 v[48:51], v[96:99], v[200:203], v[48:51]
s_waitcnt vmcnt(0)
ds_read_b128 v[116:119], v24 offset:0
v_mfma_f32_16x16x32_bf16 v[52:55], v[96:99], v[204:207], v[52:55]
s_waitcnt lgkmcnt(4)
v_mfma_f32_16x16x32_bf16 v[56:59], v[100:103], v[200:203], v[56:59]
ds_read_b128 v[120:123], v24 offset:28672
v_mfma_f32_16x16x32_bf16 v[60:63], v[100:103], v[204:207], v[60:63]
s_waitcnt lgkmcnt(4)
v_mfma_f32_16x16x32_bf16 v[64:67], v[104:107], v[200:203], v[64:67]
ds_read_b128 v[124:127], v24 offset:57344
v_mfma_f32_16x16x32_bf16 v[68:71], v[104:107], v[204:207], v[68:71]
s_waitcnt lgkmcnt(4)
v_mfma_f32_16x16x32_bf16 v[72:75], v[108:111], v[200:203], v[72:75]
ds_read_b128 v[96:99], v24 offset:1024
v_mfma_f32_16x16x32_bf16 v[76:79], v[108:111], v[204:207], v[76:79]
s_waitcnt lgkmcnt(4)
v_mfma_f32_16x16x32_bf16 v[80:83], v[112:115], v[200:203], v[80:83]
ds_read_b128 v[100:103], v24 offset:29696
v_mfma_f32_16x16x32_bf16 v[84:87], v[112:115], v[204:207], v[84:87]
s_waitcnt vmcnt(7)
s_waitcnt lgkmcnt(4)
v_mfma_f32_16x16x32_bf16 v[32:35], v[116:119], v[228:231], v[32:35]
ds_read_b128 v[104:107], v24 offset:58368
v_mfma_f32_16x16x32_bf16 v[36:39], v[116:119], v[232:235], v[36:39]
s_waitcnt vmcnt(5)
v_swap_b32 v242, v244
v_swap_b32 v243, v245
s_nop 1
s_waitcnt lgkmcnt(4)
v_mfma_f32_16x16x32_bf16 v[32:35], v[120:123], v[240:243], v[32:35]
ds_read_b128 v[108:111], v24 offset:2048
v_mfma_f32_16x16x32_bf16 v[36:39], v[120:123], v[244:247], v[36:39]
s_waitcnt vmcnt(3)
v_swap_b32 v250, v252
v_swap_b32 v251, v253
s_nop 1
s_waitcnt lgkmcnt(4)
v_mfma_f32_16x16x32_bf16 v[32:35], v[124:127], v[248:251], v[32:35]
ds_read_b128 v[112:115], v24 offset:30720
v_mfma_f32_16x16x32_bf16 v[36:39], v[124:127], v[252:255], v[36:39]
s_waitcnt lgkmcnt(4)
v_mfma_f32_16x16x32_bf16 v[40:43], v[96:99], v[228:231], v[40:43]
ds_read_b128 v[116:119], v24 offset:59392
v_mfma_f32_16x16x32_bf16 v[44:47], v[96:99], v[232:235], v[44:47]
s_waitcnt lgkmcnt(4)
v_mfma_f32_16x16x32_bf16 v[40:43], v[100:103], v[240:243], v[40:43]
ds_read_b128 v[120:123], v24 offset:3072
v_mfma_f32_16x16x32_bf16 v[44:47], v[100:103], v[244:247], v[44:47]
ds_write_b128 v4, v[32:35] offset:0
s_waitcnt lgkmcnt(5)
v_mfma_f32_16x16x32_bf16 v[40:43], v[104:107], v[248:251], v[40:43]
ds_read_b128 v[124:127], v24 offset:31744
v_mfma_f32_16x16x32_bf16 v[44:47], v[104:107], v[252:255], v[44:47]
ds_write_b128 v4, v[36:39] offset:7168
s_waitcnt lgkmcnt(6)
v_mfma_f32_16x16x32_bf16 v[48:51], v[108:111], v[228:231], v[48:51]
ds_read_b128 v[96:99], v24 offset:60416
v_mfma_f32_16x16x32_bf16 v[52:55], v[108:111], v[232:235], v[52:55]
s_waitcnt lgkmcnt(6)
v_mfma_f32_16x16x32_bf16 v[48:51], v[112:115], v[240:243], v[48:51]
ds_read_b128 v[100:103], v24 offset:4096
v_mfma_f32_16x16x32_bf16 v[52:55], v[112:115], v[244:247], v[52:55]
ds_write_b128 v4, v[40:43] offset:64
s_waitcnt lgkmcnt(7)
v_mfma_f32_16x16x32_bf16 v[48:51], v[116:119], v[248:251], v[48:51]
ds_read_b128 v[104:107], v24 offset:32768
v_mfma_f32_16x16x32_bf16 v[52:55], v[116:119], v[252:255], v[52:55]
ds_write_b128 v4, v[44:47] offset:7232
s_waitcnt lgkmcnt(8)
v_mfma_f32_16x16x32_bf16 v[56:59], v[120:123], v[228:231], v[56:59]
ds_read_b128 v[108:111], v24 offset:61440
v_mfma_f32_16x16x32_bf16 v[60:63], v[120:123], v[232:235], v[60:63]
s_waitcnt lgkmcnt(7)
v_mfma_f32_16x16x32_bf16 v[56:59], v[124:127], v[240:243], v[56:59]
ds_read_b128 v[112:115], v24 offset:5120
v_mfma_f32_16x16x32_bf16 v[60:63], v[124:127], v[244:247], v[60:63]
ds_write_b128 v4, v[48:51] offset:128
s_waitcnt lgkmcnt(7)
v_mfma_f32_16x16x32_bf16 v[56:59], v[96:99], v[248:251], v[56:59]
ds_read_b128 v[116:119], v24 offset:33792
v_mfma_f32_16x16x32_bf16 v[60:63], v[96:99], v[252:255], v[60:63]
ds_write_b128 v4, v[52:55] offset:7296
s_waitcnt lgkmcnt(8)
v_mfma_f32_16x16x32_bf16 v[64:67], v[100:103], v[228:231], v[64:67]
ds_read_b128 v[120:123], v24 offset:62464
v_mfma_f32_16x16x32_bf16 v[68:71], v[100:103], v[232:235], v[68:71]
s_waitcnt lgkmcnt(7)
v_mfma_f32_16x16x32_bf16 v[64:67], v[104:107], v[240:243], v[64:67]
ds_read_b128 v[124:127], v24 offset:6144
v_mfma_f32_16x16x32_bf16 v[68:71], v[104:107], v[244:247], v[68:71]
ds_write_b128 v4, v[56:59] offset:192
s_waitcnt lgkmcnt(7)
v_mfma_f32_16x16x32_bf16 v[64:67], v[108:111], v[248:251], v[64:67]
ds_read_b128 v[96:99], v24 offset:34816
v_mfma_f32_16x16x32_bf16 v[68:71], v[108:111], v[252:255], v[68:71]
ds_write_b128 v4, v[60:63] offset:7360
s_waitcnt lgkmcnt(8)
v_mfma_f32_16x16x32_bf16 v[72:75], v[112:115], v[228:231], v[72:75]
ds_read_b128 v[100:103], v24 offset:63488
v_mfma_f32_16x16x32_bf16 v[76:79], v[112:115], v[232:235], v[76:79]
s_waitcnt lgkmcnt(7)
v_mfma_f32_16x16x32_bf16 v[72:75], v[116:119], v[240:243], v[72:75]
v_mfma_f32_16x16x32_bf16 v[76:79], v[116:119], v[244:247], v[76:79]
ds_write_b128 v4, v[64:67] offset:256
s_waitcnt lgkmcnt(6)
v_mfma_f32_16x16x32_bf16 v[72:75], v[120:123], v[248:251], v[72:75]
v_mfma_f32_16x16x32_bf16 v[76:79], v[120:123], v[252:255], v[76:79]
ds_write_b128 v4, v[68:71] offset:7424
s_waitcnt lgkmcnt(6)
v_mfma_f32_16x16x32_bf16 v[80:83], v[124:127], v[228:231], v[80:83]
v_mfma_f32_16x16x32_bf16 v[84:87], v[124:127], v[232:235], v[84:87]
s_waitcnt lgkmcnt(4)
v_mfma_f32_16x16x32_bf16 v[80:83], v[96:99], v[240:243], v[80:83]
v_mfma_f32_16x16x32_bf16 v[84:87], v[96:99], v[244:247], v[84:87]
ds_write_b128 v4, v[72:75] offset:320
s_waitcnt lgkmcnt(3)
v_mfma_f32_16x16x32_bf16 v[80:83], v[100:103], v[248:251], v[80:83]
v_mfma_f32_16x16x32_bf16 v[84:87], v[100:103], v[252:255], v[84:87]
ds_write_b128 v4, v[76:79] offset:7488
v_lshlrev_b32 v96, 16, v152
v_and_b32 v97, s97, v152
v_lshlrev_b32 v98, 16, v153
v_and_b32 v99, s97, v153
v_lshlrev_b32 v100, 16, v154
v_and_b32 v101, s97, v154
v_lshlrev_b32 v102, 16, v155
v_and_b32 v103, s97, v155
v_lshlrev_b32 v104, 16, v180
v_and_b32 v105, s97, v180
v_lshlrev_b32 v106, 16, v181
v_and_b32 v107, s97, v181
v_lshlrev_b32 v108, 16, v182
v_and_b32 v109, s97, v182
v_lshlrev_b32 v110, 16, v183
v_and_b32 v111, s97, v183
v_xor_b32 v6, 16, v5
ds_write_b128 v4, v[80:83] offset:384
ds_write_b128 v4, v[84:87] offset:7552
s_waitcnt lgkmcnt(0)
s_barrier
ds_read_b128 v[128:131], v5 offset:0
ds_read_b128 v[132:135], v6 offset:0
ds_read_b128 v[136:139], v5 offset:14336
ds_read_b128 v[140:143], v6 offset:14336
ds_read_b128 v[144:147], v5 offset:28672
ds_read_b128 v[148:151], v6 offset:28672
ds_read_b128 v[152:155], v5 offset:43008
ds_read_b128 v[156:159], v6 offset:43008
ds_read_b128 v[160:163], v5 offset:7168
ds_read_b128 v[164:167], v6 offset:7168
ds_read_b128 v[168:171], v5 offset:21504
ds_read_b128 v[172:175], v6 offset:21504
ds_read_b128 v[176:179], v5 offset:35840
ds_read_b128 v[180:183], v6 offset:35840
ds_read_b128 v[184:187], v5 offset:50176
ds_read_b128 v[188:191], v6 offset:50176
s_add_u32 s54, s98, 14336
s_waitcnt lgkmcnt(13)
v_pk_add_f32 v[128:129], v[128:129], v[136:137]
v_pk_add_f32 v[130:131], v[130:131], v[138:139]
s_waitcnt lgkmcnt(12)
v_pk_add_f32 v[132:133], v[132:133], v[140:141]
v_pk_add_f32 v[134:135], v[134:135], v[142:143]
s_waitcnt lgkmcnt(9)
v_pk_add_f32 v[144:145], v[144:145], v[152:153]
v_pk_add_f32 v[146:147], v[146:147], v[154:155]
s_waitcnt lgkmcnt(8)
v_pk_add_f32 v[148:149], v[148:149], v[156:157]
v_pk_add_f32 v[150:151], v[150:151], v[158:159]
v_pk_add_f32 v[128:129], v[128:129], v[144:145]
v_pk_add_f32 v[130:131], v[130:131], v[146:147]
v_pk_add_f32 v[132:133], v[132:133], v[148:149]
v_pk_add_f32 v[134:135], v[134:135], v[150:151]
v_pk_fma_f32 v[128:129], v[128:129], v[88:89], v[96:97]
v_pk_fma_f32 v[130:131], v[130:131], v[90:91], v[98:99]
v_pk_fma_f32 v[132:133], v[132:133], v[92:93], v[100:101]
v_pk_fma_f32 v[134:135], v[134:135], v[94:95], v[102:103]
v_cvt_pk_bf16_f32 v8, v128, v129
v_cvt_pk_bf16_f32 v9, v130, v131
v_cvt_pk_bf16_f32 v10, v132, v133
v_cvt_pk_bf16_f32 v11, v134, v135
buffer_store_dwordx4 v[8:11], v2, s[92:95], s98 offen offset:0
s_waitcnt lgkmcnt(5)
v_pk_add_f32 v[160:161], v[160:161], v[168:169]
v_pk_add_f32 v[162:163], v[162:163], v[170:171]
s_waitcnt lgkmcnt(4)
v_pk_add_f32 v[164:165], v[164:165], v[172:173]
v_pk_add_f32 v[166:167], v[166:167], v[174:175]
s_waitcnt lgkmcnt(1)
v_pk_add_f32 v[176:177], v[176:177], v[184:185]
v_pk_add_f32 v[178:179], v[178:179], v[186:187]
s_waitcnt lgkmcnt(0)
v_pk_add_f32 v[180:181], v[180:181], v[188:189]
v_pk_add_f32 v[182:183], v[182:183], v[190:191]
v_pk_add_f32 v[160:161], v[160:161], v[176:177]
v_pk_add_f32 v[162:163], v[162:163], v[178:179]
v_pk_add_f32 v[164:165], v[164:165], v[180:181]
v_pk_add_f32 v[166:167], v[166:167], v[182:183]
v_pk_fma_f32 v[160:161], v[160:161], v[88:89], v[104:105]
v_pk_fma_f32 v[162:163], v[162:163], v[90:91], v[106:107]
v_pk_fma_f32 v[164:165], v[164:165], v[92:93], v[108:109]
v_pk_fma_f32 v[166:167], v[166:167], v[94:95], v[110:111]
v_cvt_pk_bf16_f32 v12, v160, v161
v_cvt_pk_bf16_f32 v13, v162, v163
v_cvt_pk_bf16_f32 v14, v164, v165
v_cvt_pk_bf16_f32 v15, v166, v167
buffer_store_dwordx4 v[12:15], v2, s[92:95], s54 offen offset:0
s_or_b32 s53, s34, s35
s_or_b32 s53, s53, s80
s_cbranch_scc1 L_noep%=
buffer_store_dwordx2 v[30:31], v29, s[60:63], 0 offen offset:0 sc1
L_noep%=:
s_branch L_end%=
L_modsub%=:
s_mov_b32 s53, 0xbfb8aa3b
v_lshlrev_b32 v192, 16, v192
v_lshlrev_b32 v193, 16, v193
v_lshlrev_b32 v194, 16, v194
v_lshlrev_b32 v195, 16, v195
v_lshlrev_b32 v196, 16, v196
v_lshlrev_b32 v197, 16, v197
v_lshlrev_b32 v198, 16, v198
v_mul_f32 v200, s53, v192
v_mul_f32 v201, s53, v193
v_mul_f32 v202, s53, v194
v_mul_f32 v203, s53, v195
v_mul_f32 v204, s53, v196
v_mul_f32 v205, s53, v197
v_mul_f32 v206, s53, v198
v_exp_f32 v200, v200
v_exp_f32 v201, v201
v_exp_f32 v202, v202
v_exp_f32 v203, v203
v_exp_f32 v204, v204
v_exp_f32 v205, v205
v_exp_f32 v206, v206
v_add_f32 v200, 1.0, v200
v_add_f32 v201, 1.0, v201
v_add_f32 v202, 1.0, v202
v_add_f32 v203, 1.0, v203
v_add_f32 v204, 1.0, v204
v_add_f32 v205, 1.0, v205
v_add_f32 v206, 1.0, v206
v_rcp_f32 v200, v200
v_rcp_f32 v201, v201
v_rcp_f32 v202, v202
v_rcp_f32 v203, v203
v_rcp_f32 v204, v204
v_rcp_f32 v205, v205
v_rcp_f32 v206, v206
v_mul_f32 v192, v192, v200
v_mul_f32 v193, v193, v201
v_mul_f32 v194, v194, v202
v_mul_f32 v195, v195, v203
v_mul_f32 v196, v196, v204
v_mul_f32 v197, v197, v205
v_mul_f32 v198, v198, v206
v_cvt_pk_bf16_f32 v192, v192, v192
v_cvt_pk_bf16_f32 v193, v193, v193
v_cvt_pk_bf16_f32 v194, v194, v194
v_cvt_pk_bf16_f32 v195, v195, v195
v_cvt_pk_bf16_f32 v196, v196, v196
v_cvt_pk_bf16_f32 v197, v197, v197
v_cvt_pk_bf16_f32 v198, v198, v198
v_and_b32 v192, s97, v192
v_and_b32 v193, s97, v193
v_and_b32 v194, s97, v194
v_and_b32 v195, s97, v195
v_and_b32 v196, s97, v196
v_and_b32 v197, s97, v197
v_and_b32 v198, s97, v198
v_lshlrev_b32 v208, 16, v128
v_and_b32 v209, s97, v128
v_pk_mul_f32 v[200:201], v[208:209], v[192:193] op_sel:[0,0] op_sel_hi:[1,0]
v_lshlrev_b32 v208, 16, v129
v_and_b32 v209, s97, v129
v_pk_mul_f32 v[202:203], v[208:209], v[192:193] op_sel:[0,0] op_sel_hi:[1,0]
v_lshlrev_b32 v208, 16, v130
v_and_b32 v209, s97, v130
v_pk_mul_f32 v[204:205], v[208:209], v[192:193] op_sel:[0,0] op_sel_hi:[1,0]
v_lshlrev_b32 v208, 16, v131
v_and_b32 v209, s97, v131
v_pk_mul_f32 v[206:207], v[208:209], v[192:193] op_sel:[0,0] op_sel_hi:[1,0]
v_lshlrev_b32 v208, 16, v132
v_and_b32 v209, s97, v132
v_pk_fma_f32 v[200:201], v[208:209], v[192:193], v[200:201] op_sel:[0,1,0] op_sel_hi:[1,1,1]
v_lshlrev_b32 v208, 16, v133
v_and_b32 v209, s97, v133
v_pk_fma_f32 v[202:203], v[208:209], v[192:193], v[202:203] op_sel:[0,1,0] op_sel_hi:[1,1,1]
v_lshlrev_b32 v208, 16, v134
v_and_b32 v209, s97, v134
v_pk_fma_f32 v[204:205], v[208:209], v[192:193], v[204:205] op_sel:[0,1,0] op_sel_hi:[1,1,1]
v_lshlrev_b32 v208, 16, v135
v_and_b32 v209, s97, v135
v_pk_fma_f32 v[206:207], v[208:209], v[192:193], v[206:207] op_sel:[0,1,0] op_sel_hi:[1,1,1]
v_lshlrev_b32 v208, 16, v136
v_and_b32 v209, s97, v136
v_pk_fma_f32 v[200:201], v[208:209], v[194:195], v[200:201] op_sel:[0,0,0] op_sel_hi:[1,0,1]
v_lshlrev_b32 v208, 16, v137
v_and_b32 v209, s97, v137
v_pk_fma_f32 v[202:203], v[208:209], v[194:195], v[202:203] op_sel:[0,0,0] op_sel_hi:[1,0,1]
v_lshlrev_b32 v208, 16, v138
v_and_b32 v209, s97, v138
v_pk_fma_f32 v[204:205], v[208:209], v[194:195], v[204:205] op_sel:[0,0,0] op_sel_hi:[1,0,1]
v_lshlrev_b32 v208, 16, v139
v_and_b32 v209, s97, v139
v_pk_fma_f32 v[206:207], v[208:209], v[194:195], v[206:207] op_sel:[0,0,0] op_sel_hi:[1,0,1]
v_lshlrev_b32 v208, 16, v140
v_and_b32 v209, s97, v140
v_pk_fma_f32 v[200:201], v[208:209], v[194:195], v[200:201] op_sel:[0,1,0] op_sel_hi:[1,1,1]
v_lshlrev_b32 v208, 16, v141
v_and_b32 v209, s97, v141
v_pk_fma_f32 v[202:203], v[208:209], v[194:195], v[202:203] op_sel:[0,1,0] op_sel_hi:[1,1,1]
v_lshlrev_b32 v208, 16, v142
v_and_b32 v209, s97, v142
v_pk_fma_f32 v[204:205], v[208:209], v[194:195], v[204:205] op_sel:[0,1,0] op_sel_hi:[1,1,1]
v_lshlrev_b32 v208, 16, v143
v_and_b32 v209, s97, v143
v_pk_fma_f32 v[206:207], v[208:209], v[194:195], v[206:207] op_sel:[0,1,0] op_sel_hi:[1,1,1]
v_lshlrev_b32 v208, 16, v144
v_and_b32 v209, s97, v144
v_pk_fma_f32 v[200:201], v[208:209], v[196:197], v[200:201] op_sel:[0,0,0] op_sel_hi:[1,0,1]
v_lshlrev_b32 v208, 16, v145
v_and_b32 v209, s97, v145
v_pk_fma_f32 v[202:203], v[208:209], v[196:197], v[202:203] op_sel:[0,0,0] op_sel_hi:[1,0,1]
v_lshlrev_b32 v208, 16, v146
v_and_b32 v209, s97, v146
v_pk_fma_f32 v[204:205], v[208:209], v[196:197], v[204:205] op_sel:[0,0,0] op_sel_hi:[1,0,1]
v_lshlrev_b32 v208, 16, v147
v_and_b32 v209, s97, v147
v_pk_fma_f32 v[206:207], v[208:209], v[196:197], v[206:207] op_sel:[0,0,0] op_sel_hi:[1,0,1]
v_lshlrev_b32 v208, 16, v148
v_and_b32 v209, s97, v148
v_pk_fma_f32 v[200:201], v[208:209], v[196:197], v[200:201] op_sel:[0,1,0] op_sel_hi:[1,1,1]
v_lshlrev_b32 v208, 16, v149
v_and_b32 v209, s97, v149
v_pk_fma_f32 v[202:203], v[208:209], v[196:197], v[202:203] op_sel:[0,1,0] op_sel_hi:[1,1,1]
v_lshlrev_b32 v208, 16, v150
v_and_b32 v209, s97, v150
v_pk_fma_f32 v[204:205], v[208:209], v[196:197], v[204:205] op_sel:[0,1,0] op_sel_hi:[1,1,1]
v_lshlrev_b32 v208, 16, v151
v_and_b32 v209, s97, v151
v_pk_fma_f32 v[206:207], v[208:209], v[196:197], v[206:207] op_sel:[0,1,0] op_sel_hi:[1,1,1]
v_lshlrev_b32 v208, 16, v152
v_and_b32 v209, s97, v152
v_pk_fma_f32 v[200:201], v[208:209], v[198:199], v[200:201] op_sel:[0,0,0] op_sel_hi:[1,0,1]
v_lshlrev_b32 v208, 16, v153
v_and_b32 v209, s97, v153
v_pk_fma_f32 v[202:203], v[208:209], v[198:199], v[202:203] op_sel:[0,0,0] op_sel_hi:[1,0,1]
v_lshlrev_b32 v208, 16, v154
v_and_b32 v209, s97, v154
v_pk_fma_f32 v[204:205], v[208:209], v[198:199], v[204:205] op_sel:[0,0,0] op_sel_hi:[1,0,1]
v_lshlrev_b32 v208, 16, v155
v_and_b32 v209, s97, v155
v_pk_fma_f32 v[206:207], v[208:209], v[198:199], v[206:207] op_sel:[0,0,0] op_sel_hi:[1,0,1]
s_nop 0
v_add_f32_dpp v200, v200, v200 row_ror:8 row_mask:0xf bank_mask:0xf
v_add_f32_dpp v201, v201, v201 row_ror:8 row_mask:0xf bank_mask:0xf
v_add_f32_dpp v202, v202, v202 row_ror:8 row_mask:0xf bank_mask:0xf
v_add_f32_dpp v203, v203, v203 row_ror:8 row_mask:0xf bank_mask:0xf
v_add_f32_dpp v204, v204, v204 row_ror:8 row_mask:0xf bank_mask:0xf
v_add_f32_dpp v205, v205, v205 row_ror:8 row_mask:0xf bank_mask:0xf
v_add_f32_dpp v206, v206, v206 row_ror:8 row_mask:0xf bank_mask:0xf
v_add_f32_dpp v207, v207, v207 row_ror:8 row_mask:0xf bank_mask:0xf
v_add_f32_dpp v200, v200, v200 row_ror:4 row_mask:0xf bank_mask:0xf
v_add_f32_dpp v201, v201, v201 row_ror:4 row_mask:0xf bank_mask:0xf
v_add_f32_dpp v202, v202, v202 row_ror:4 row_mask:0xf bank_mask:0xf
v_add_f32_dpp v203, v203, v203 row_ror:4 row_mask:0xf bank_mask:0xf
v_add_f32_dpp v204, v204, v204 row_ror:4 row_mask:0xf bank_mask:0xf
v_add_f32_dpp v205, v205, v205 row_ror:4 row_mask:0xf bank_mask:0xf
v_add_f32_dpp v206, v206, v206 row_ror:4 row_mask:0xf bank_mask:0xf
v_add_f32_dpp v207, v207, v207 row_ror:4 row_mask:0xf bank_mask:0xf
ds_write_b32 v212, v200 offset:0
ds_write_b32 v212, v201 offset:64
ds_write_b32 v212, v202 offset:128
ds_write_b32 v212, v203 offset:192
ds_write_b32 v212, v204 offset:256
ds_write_b32 v212, v205 offset:320
ds_write_b32 v212, v206 offset:384
ds_write_b32 v212, v207 offset:448
s_waitcnt lgkmcnt(0)
s_barrier
s_cmp_eq_u32 s80, 0
s_cbranch_scc0 L_nofin%=
v_and_b32 v16, 31, v15
v_lshlrev_b32 v17, 6, v16
v_add_u32 v17, 114688, v17
ds_read_b128 v[128:131], v17 offset:0
ds_read_b128 v[132:135], v17 offset:16
ds_read_b128 v[136:139], v17 offset:32
ds_read_b128 v[140:143], v17 offset:48
s_waitcnt lgkmcnt(0)
v_pk_add_f32 v[128:129], v[128:129], v[136:137]
v_pk_add_f32 v[130:131], v[130:131], v[138:139]
v_pk_add_f32 v[132:133], v[132:133], v[140:141]
v_pk_add_f32 v[134:135], v[134:135], v[142:143]
v_pk_add_f32 v[128:129], v[128:129], v[132:133]
v_pk_add_f32 v[130:131], v[130:131], v[134:135]
v_pk_add_f32 v[128:129], v[128:129], v[130:131]
v_add_f32 v128, v128, v129
v_cvt_pk_bf16_f32 v128, v128, v128
v_and_b32 v128, 0xffff, v128
s_cmp_lt_u32 s99, 28
s_cbranch_scc0 L_fin1%=
v_or_b32 v128, s78, v128
v_mov_b32 v129, s79
v_lshlrev_b32 v16, 3, v16
s_lshl_b32 s53, s99, 8
s_add_u32 s53, s53, 4096
buffer_store_dwordx2 v[128:129], v16, s[60:63], s53 offen offset:0 sc1
s_branch L_nofin%=
L_fin1%=:
v_lshlrev_b32 v16, 1, v16
s_lshl_b32 s53, s99, 6
s_add_u32 s53, s53, 135168
buffer_store_short v128, v16, s[60:63], s53 offen offset:0 sc1
L_nofin%=:
s_setpc_b64 s[30:31]
L_alr%=:
s_mov_b32 s47, 131072
L_all%=:
s_mov_b32 s54, 131072
buffer_load_dwordx4 v[88:91], v1, s[60:63], s54 offen offset:0 sc1
buffer_load_dwordx4 v[92:95], v1, s[60:63], s54 offen offset:1024 sc1
s_waitcnt vmcnt(0)
v_xor_b32 v88, s76, v88
v_xor_b32 v89, s77, v89
v_xor_b32 v90, s76, v90
v_xor_b32 v91, s77, v91
v_xor_b32 v92, s76, v92
v_xor_b32 v93, s77, v93
v_xor_b32 v94, s76, v94
v_xor_b32 v95, s77, v95
v_or3_b32 v88, v88, v89, v90
v_or3_b32 v92, v92, v93, v94
v_or3_b32 v88, v88, v91, v95
v_or_b32 v88, v88, v92
v_cmp_ne_u32 vcc, 0, v88
s_cbranch_vccz L_ald%=
s_sleep 1
s_sub_u32 s47, s47, 1
s_cbranch_scc0 L_all%=
L_ald%=:
s_setpc_b64 s[30:31]
L_p1%=_rare:
v_max3_f32 v48, v64, v65, v66
v_max3_f32 v48, v48, v67, v80
v_max3_f32 v48, v48, v81, v82
v_max_f32 v48, v48, v83
v_mov_b32 v15, v48
s_nop 1
v_permlane32_swap_b32 v48, v15
s_nop 0
v_max_f32 v48, v48, v15
v_mov_b32 v15, v48
s_nop 1
v_permlane16_swap_b32 v48, v15
s_nop 0
v_max_f32 v48, v48, v15
v_max3_f32 v49, v68, v69, v70
v_max3_f32 v49, v49, v71, v84
v_max3_f32 v49, v49, v85, v86
v_max_f32 v49, v49, v87
v_mov_b32 v15, v49
s_nop 1
v_permlane32_swap_b32 v49, v15
s_nop 0
v_max_f32 v49, v49, v15
v_mov_b32 v15, v49
s_nop 1
v_permlane16_swap_b32 v49, v15
s_nop 0
v_max_f32 v49, v49, v15
v_max3_f32 v50, v72, v73, v74
v_max3_f32 v50, v50, v75, v88
v_max3_f32 v50, v50, v89, v90
v_max_f32 v50, v50, v91
v_mov_b32 v15, v50
s_nop 1
v_permlane32_swap_b32 v50, v15
s_nop 0
v_max_f32 v50, v50, v15
v_mov_b32 v15, v50
s_nop 1
v_permlane16_swap_b32 v50, v15
s_nop 0
v_max_f32 v50, v50, v15
v_max3_f32 v51, v76, v77, v78
v_max3_f32 v51, v51, v79, v92
v_max3_f32 v51, v51, v93, v94
v_max_f32 v51, v51, v95
v_mov_b32 v15, v51
s_nop 1
v_permlane32_swap_b32 v51, v15
s_nop 0
v_max_f32 v51, v51, v15
v_mov_b32 v15, v51
s_nop 1
v_permlane16_swap_b32 v51, v15
s_nop 0
v_max_f32 v51, v51, v15
v_max_f32 v48, 0, v48
v_sub_f32 v15, 0, v48
v_exp_f32 v52, v15
v_sub_f32 v64, v64, v48
v_sub_f32 v65, v65, v48
v_sub_f32 v66, v66, v48
v_sub_f32 v67, v67, v48
v_sub_f32 v80, v80, v48
v_sub_f32 v81, v81, v48
v_sub_f32 v82, v82, v48
v_sub_f32 v83, v83, v48
v_sub_f32 v16, v16, v48
v_sub_f32 v17, v17, v48
v_sub_f32 v18, v18, v48
v_sub_f32 v19, v19, v48
v_mul_f32 v8, v8, v52
v_max_f32 v49, 0, v49
v_sub_f32 v15, 0, v49
v_exp_f32 v53, v15
v_sub_f32 v68, v68, v49
v_sub_f32 v69, v69, v49
v_sub_f32 v70, v70, v49
v_sub_f32 v71, v71, v49
v_sub_f32 v84, v84, v49
v_sub_f32 v85, v85, v49
v_sub_f32 v86, v86, v49
v_sub_f32 v87, v87, v49
v_sub_f32 v20, v20, v49
v_sub_f32 v21, v21, v49
v_sub_f32 v22, v22, v49
v_sub_f32 v23, v23, v49
v_mul_f32 v9, v9, v53
v_max_f32 v50, 0, v50
v_sub_f32 v15, 0, v50
v_exp_f32 v54, v15
v_sub_f32 v72, v72, v50
v_sub_f32 v73, v73, v50
v_sub_f32 v74, v74, v50
v_sub_f32 v75, v75, v50
v_sub_f32 v88, v88, v50
v_sub_f32 v89, v89, v50
v_sub_f32 v90, v90, v50
v_sub_f32 v91, v91, v50
v_sub_f32 v24, v24, v50
v_sub_f32 v25, v25, v50
v_sub_f32 v26, v26, v50
v_sub_f32 v27, v27, v50
v_mul_f32 v10, v10, v54
v_max_f32 v51, 0, v51
v_sub_f32 v15, 0, v51
v_exp_f32 v55, v15
v_sub_f32 v76, v76, v51
v_sub_f32 v77, v77, v51
v_sub_f32 v78, v78, v51
v_sub_f32 v79, v79, v51
v_sub_f32 v92, v92, v51
v_sub_f32 v93, v93, v51
v_sub_f32 v94, v94, v51
v_sub_f32 v95, v95, v51
v_sub_f32 v28, v28, v51
v_sub_f32 v29, v29, v51
v_sub_f32 v30, v30, v51
v_sub_f32 v31, v31, v51
v_mul_f32 v11, v11, v55
ds_read_b64_tr_b16 v[102:103], v3 offset:8960
v_mfma_f32_16x16x32_bf16 a[56:59], v[40:43], v[108:111], 0
v_exp_f32 v64, v64
v_mfma_f32_16x16x32_bf16 a[36:39], v[36:39], v[104:107], 0
buffer_load_dwordx4 v7, s[36:39], s44 offen offset:3264 lds
v_mfma_f32_16x16x32_bf16 a[52:55], v[36:39], v[108:111], 0
v_exp_f32 v65, v65
v_mfma_f32_16x16x32_bf16 a[32:35], v[32:35], v[104:107], 0
v_exp_f32 v66, v66
v_mfma_f32_16x16x32_bf16 a[48:51], v[32:35], v[108:111], 0
v_add_f32 v8, v8, v64
s_waitcnt lgkmcnt(8)
v_mfma_f32_16x16x32_bf16 a[64:67], v[32:35], v[112:115], 0
v_exp_f32 v67, v67
v_mfma_f32_16x16x32_bf16 a[80:83], v[32:35], v[116:119], 0
s_add_u32 m0, s45, 39168
v_add_f32 v8, v8, v65
v_mfma_f32_16x16x32_bf16 a[68:71], v[36:39], v[112:115], 0
v_exp_f32 v68, v68
v_mfma_f32_16x16x32_bf16 a[84:87], v[36:39], v[116:119], 0
v_add_f32 v8, v8, v66
v_add_f32 v8, v8, v67
v_mfma_f32_16x16x32_bf16 a[72:75], v[40:43], v[112:115], 0
buffer_load_dwordx4 v4, s[36:39], s49 offen offset:0 lds
v_mfma_f32_16x16x32_bf16 a[88:91], v[40:43], v[116:119], 0
v_exp_f32 v69, v69
v_mfma_f32_16x16x32_bf16 a[76:79], v[44:47], v[112:115], 0
v_exp_f32 v70, v70
v_mfma_f32_16x16x32_bf16 a[92:95], v[44:47], v[116:119], 0
ds_read_b64_tr_b16 v[104:105], v2 offset:320
s_waitcnt lgkmcnt(5)
v_mfma_f32_16x16x32_bf16 a[108:111], v[44:47], v[120:123], 0
ds_read_b64_tr_b16 v[106:107], v2 offset:9024
ds_read_b64_tr_b16 v[108:109], v3 offset:320
v_mfma_f32_16x16x32_bf16 a[124:127], v[44:47], v[124:127], 0
ds_read_b64_tr_b16 v[110:111], v3 offset:9024
v_add_f32 v9, v9, v68
v_mfma_f32_16x16x32_bf16 a[104:107], v[40:43], v[120:123], 0
v_exp_f32 v71, v71
v_mfma_f32_16x16x32_bf16 a[120:123], v[40:43], v[124:127], 0
v_add_f32 v9, v9, v69
v_add_f32 v9, v9, v70
v_mfma_f32_16x16x32_bf16 a[100:103], v[36:39], v[120:123], 0
buffer_load_dwordx4 v5, s[36:39], s49 offen offset:1088 lds
v_mfma_f32_16x16x32_bf16 a[116:119], v[36:39], v[124:127], 0
v_exp_f32 v72, v72
v_mfma_f32_16x16x32_bf16 a[96:99], v[32:35], v[120:123], 0
v_exp_f32 v73, v73
v_mfma_f32_16x16x32_bf16 a[112:115], v[32:35], v[124:127], 0
ds_read_b64_tr_b16 v[112:113], v2 offset:384
s_waitcnt lgkmcnt(5)
v_mfma_f32_16x16x32_bf16 a[128:131], v[32:35], v[96:99], 0
ds_read_b64_tr_b16 v[114:115], v2 offset:9088
ds_read_b64_tr_b16 v[116:117], v3 offset:384
v_mfma_f32_16x16x32_bf16 a[144:147], v[32:35], v[100:103], 0
ds_read_b64_tr_b16 v[118:119], v3 offset:9088
v_add_f32 v9, v9, v71
v_mfma_f32_16x16x32_bf16 a[132:135], v[36:39], v[96:99], 0
ds_read_b64_tr_b16 v[120:121], v2 offset:448
ds_read_b64_tr_b16 v[122:123], v2 offset:9152
v_mfma_f32_16x16x32_bf16 a[148:151], v[36:39], v[100:103], 0
ds_read_b64_tr_b16 v[124:125], v3 offset:448
v_add_f32 v10, v10, v72
v_mfma_f32_16x16x32_bf16 a[136:139], v[40:43], v[96:99], 0
buffer_load_dwordx4 v6, s[36:39], s49 offen offset:2176 lds
v_mfma_f32_16x16x32_bf16 a[152:155], v[40:43], v[100:103], 0
ds_read_b64_tr_b16 v[126:127], v3 offset:9152
v_add_f32 v10, v10, v73
v_mfma_f32_16x16x32_bf16 a[140:143], v[44:47], v[96:99], 0
v_exp_f32 v74, v74
v_mfma_f32_16x16x32_bf16 a[156:159], v[44:47], v[100:103], 0
v_add_f32 v10, v10, v74
s_waitcnt lgkmcnt(8)
v_mfma_f32_16x16x32_bf16 a[172:175], v[44:47], v[104:107], 0
v_exp_f32 v75, v75
v_mfma_f32_16x16x32_bf16 a[188:191], v[44:47], v[108:111], 0
v_exp_f32 v76, v76
v_mfma_f32_16x16x32_bf16 a[168:171], v[40:43], v[104:107], 0
buffer_load_dwordx4 v7, s[36:39], s49 offen offset:3264 lds
v_mfma_f32_16x16x32_bf16 a[184:187], v[40:43], v[108:111], 0
v_exp_f32 v77, v77
v_mfma_f32_16x16x32_bf16 a[164:167], v[36:39], v[104:107], 0
v_add_f32 v10, v10, v75
v_add_f32 v11, v11, v76
v_mfma_f32_16x16x32_bf16 a[180:183], v[36:39], v[108:111], 0
v_exp_f32 v78, v78
v_mfma_f32_16x16x32_bf16 a[160:163], v[32:35], v[104:107], 0
v_exp_f32 v79, v79
v_mfma_f32_16x16x32_bf16 a[176:179], v[32:35], v[108:111], 0
s_waitcnt vmcnt(16) lgkmcnt(0)
s_barrier
v_mfma_f32_16x16x32_bf16 a[192:195], v[32:35], v[112:115], 0
ds_read_b128 v[96:99], v12 offset:0
v_add_f32 v11, v11, v77
v_mfma_f32_16x16x32_bf16 a[208:211], v[32:35], v[116:119], 0
ds_read_b128 v[100:103], v12 offset:64
v_add_f32 v11, v11, v78
v_mfma_f32_16x16x32_bf16 a[196:199], v[36:39], v[112:115], 0
v_exp_f32 v80, v80
v_mfma_f32_16x16x32_bf16 a[212:215], v[36:39], v[116:119], 0
v_exp_f32 v81, v81
v_mfma_f32_16x16x32_bf16 a[200:203], v[40:43], v[112:115], 0
v_add_f32 v11, v11, v79
s_add_u32 s44, s44, 32768
v_mfma_f32_16x16x32_bf16 a[216:219], v[40:43], v[116:119], 0
s_add_u32 s49, s49, 32768
v_add_f32 v8, v8, v80
v_mfma_f32_16x16x32_bf16 a[204:207], v[44:47], v[112:115], 0
ds_read_b128 v[104:107], v12 offset:128
ds_read_b128 v[108:111], v12 offset:192
v_mfma_f32_16x16x32_bf16 a[220:223], v[44:47], v[116:119], 0
v_add_f32 v8, v8, v81
v_mfma_f32_16x16x32_bf16 a[236:239], v[44:47], v[120:123], 0
v_mfma_f32_16x16x32_bf16 a[252:255], v[44:47], v[124:127], 0
v_mfma_f32_16x16x32_bf16 a[232:235], v[40:43], v[120:123], 0
v_mfma_f32_16x16x32_bf16 a[248:251], v[40:43], v[124:127], 0
v_mfma_f32_16x16x32_bf16 a[228:231], v[36:39], v[120:123], 0
v_mfma_f32_16x16x32_bf16 a[244:247], v[36:39], v[124:127], 0
v_mfma_f32_16x16x32_bf16 a[224:227], v[32:35], v[120:123], 0
v_mfma_f32_16x16x32_bf16 a[240:243], v[32:35], v[124:127], 0
s_call_b64 s[50:51], L_resc1%=
s_branch L_p1%=_next
L_l2%=_rare:
v_max3_f32 v80, v32, v33, v34
v_max3_f32 v80, v80, v35, v48
v_max3_f32 v80, v80, v49, v50
v_max_f32 v80, v80, v51
v_mov_b32 v15, v80
s_nop 1
v_permlane32_swap_b32 v80, v15
s_nop 0
v_max_f32 v80, v80, v15
v_mov_b32 v15, v80
s_nop 1
v_permlane16_swap_b32 v80, v15
s_nop 0
v_max_f32 v80, v80, v15
v_max3_f32 v81, v36, v37, v38
v_max3_f32 v81, v81, v39, v52
v_max3_f32 v81, v81, v53, v54
v_max_f32 v81, v81, v55
v_mov_b32 v15, v81
s_nop 1
v_permlane32_swap_b32 v81, v15
s_nop 0
v_max_f32 v81, v81, v15
v_mov_b32 v15, v81
s_nop 1
v_permlane16_swap_b32 v81, v15
s_nop 0
v_max_f32 v81, v81, v15
v_max3_f32 v82, v40, v41, v42
v_max3_f32 v82, v82, v43, v56
v_max3_f32 v82, v82, v57, v58
v_max_f32 v82, v82, v59
v_mov_b32 v15, v82
s_nop 1
v_permlane32_swap_b32 v82, v15
s_nop 0
v_max_f32 v82, v82, v15
v_mov_b32 v15, v82
s_nop 1
v_permlane16_swap_b32 v82, v15
s_nop 0
v_max_f32 v82, v82, v15
v_max3_f32 v83, v44, v45, v46
v_max3_f32 v83, v83, v47, v60
v_max3_f32 v83, v83, v61, v62
v_max_f32 v83, v83, v63
v_mov_b32 v15, v83
s_nop 1
v_permlane32_swap_b32 v83, v15
s_nop 0
v_max_f32 v83, v83, v15
v_mov_b32 v15, v83
s_nop 1
v_permlane16_swap_b32 v83, v15
s_nop 0
v_max_f32 v83, v83, v15
v_max_f32 v80, 0, v80
v_sub_f32 v15, 0, v80
v_exp_f32 v84, v15
v_sub_f32 v32, v32, v80
v_sub_f32 v33, v33, v80
v_sub_f32 v34, v34, v80
v_sub_f32 v35, v35, v80
v_sub_f32 v48, v48, v80
v_sub_f32 v49, v49, v80
v_sub_f32 v50, v50, v80
v_sub_f32 v51, v51, v80
v_sub_f32 v16, v16, v80
v_sub_f32 v17, v17, v80
v_sub_f32 v18, v18, v80
v_sub_f32 v19, v19, v80
v_mul_f32 v8, v8, v84
v_max_f32 v81, 0, v81
v_sub_f32 v15, 0, v81
v_exp_f32 v85, v15
v_sub_f32 v36, v36, v81
v_sub_f32 v37, v37, v81
v_sub_f32 v38, v38, v81
v_sub_f32 v39, v39, v81
v_sub_f32 v52, v52, v81
v_sub_f32 v53, v53, v81
v_sub_f32 v54, v54, v81
v_sub_f32 v55, v55, v81
v_sub_f32 v20, v20, v81
v_sub_f32 v21, v21, v81
v_sub_f32 v22, v22, v81
v_sub_f32 v23, v23, v81
v_mul_f32 v9, v9, v85
v_max_f32 v82, 0, v82
v_sub_f32 v15, 0, v82
v_exp_f32 v86, v15
v_sub_f32 v40, v40, v82
v_sub_f32 v41, v41, v82
v_sub_f32 v42, v42, v82
v_sub_f32 v43, v43, v82
v_sub_f32 v56, v56, v82
v_sub_f32 v57, v57, v82
v_sub_f32 v58, v58, v82
v_sub_f32 v59, v59, v82
v_sub_f32 v24, v24, v82
v_sub_f32 v25, v25, v82
v_sub_f32 v26, v26, v82
v_sub_f32 v27, v27, v82
v_mul_f32 v10, v10, v86
v_max_f32 v83, 0, v83
v_sub_f32 v15, 0, v83
v_exp_f32 v87, v15
v_sub_f32 v44, v44, v83
v_sub_f32 v45, v45, v83
v_sub_f32 v46, v46, v83
v_sub_f32 v47, v47, v83
v_sub_f32 v60, v60, v83
v_sub_f32 v61, v61, v83
v_sub_f32 v62, v62, v83
v_sub_f32 v63, v63, v83
v_sub_f32 v28, v28, v83
v_sub_f32 v29, v29, v83
v_sub_f32 v30, v30, v83
v_sub_f32 v31, v31, v83
v_mul_f32 v11, v11, v87
ds_read_b64_tr_b16 v[102:103], v3 offset:43776
v_mfma_f32_16x16x32_bf16 a[56:59], v[72:75], v[108:111], a[56:59]
v_exp_f32 v32, v32
v_mfma_f32_16x16x32_bf16 a[36:39], v[68:71], v[104:107], a[36:39]
buffer_load_dwordx4 v7, s[36:39], s44 offen offset:3264 lds
v_mfma_f32_16x16x32_bf16 a[52:55], v[68:71], v[108:111], a[52:55]
v_exp_f32 v33, v33
v_mfma_f32_16x16x32_bf16 a[32:35], v[64:67], v[104:107], a[32:35]
v_exp_f32 v34, v34
v_mfma_f32_16x16x32_bf16 a[48:51], v[64:67], v[108:111], a[48:51]
v_add_f32 v8, v8, v32
s_waitcnt lgkmcnt(8)
v_mfma_f32_16x16x32_bf16 a[64:67], v[64:67], v[112:115], a[64:67]
v_exp_f32 v35, v35
v_mfma_f32_16x16x32_bf16 a[80:83], v[64:67], v[116:119], a[80:83]
s_add_u32 m0, s45, 4352
v_add_f32 v8, v8, v33
v_mfma_f32_16x16x32_bf16 a[68:71], v[68:71], v[112:115], a[68:71]
v_exp_f32 v36, v36
v_mfma_f32_16x16x32_bf16 a[84:87], v[68:71], v[116:119], a[84:87]
v_add_f32 v8, v8, v34
v_add_f32 v8, v8, v35
v_mfma_f32_16x16x32_bf16 a[72:75], v[72:75], v[112:115], a[72:75]
buffer_load_dwordx4 v4, s[36:39], s49 offen offset:0 lds
v_mfma_f32_16x16x32_bf16 a[88:91], v[72:75], v[116:119], a[88:91]
v_exp_f32 v37, v37
v_mfma_f32_16x16x32_bf16 a[76:79], v[76:79], v[112:115], a[76:79]
v_exp_f32 v38, v38
v_mfma_f32_16x16x32_bf16 a[92:95], v[76:79], v[116:119], a[92:95]
ds_read_b64_tr_b16 v[104:105], v2 offset:35136
s_waitcnt lgkmcnt(5)
v_mfma_f32_16x16x32_bf16 a[108:111], v[76:79], v[120:123], a[108:111]
ds_read_b64_tr_b16 v[106:107], v2 offset:43840
ds_read_b64_tr_b16 v[108:109], v3 offset:35136
v_mfma_f32_16x16x32_bf16 a[124:127], v[76:79], v[124:127], a[124:127]
ds_read_b64_tr_b16 v[110:111], v3 offset:43840
v_add_f32 v9, v9, v36
v_mfma_f32_16x16x32_bf16 a[104:107], v[72:75], v[120:123], a[104:107]
v_exp_f32 v39, v39
v_mfma_f32_16x16x32_bf16 a[120:123], v[72:75], v[124:127], a[120:123]
v_add_f32 v9, v9, v37
v_add_f32 v9, v9, v38
v_mfma_f32_16x16x32_bf16 a[100:103], v[68:71], v[120:123], a[100:103]
buffer_load_dwordx4 v5, s[36:39], s49 offen offset:1088 lds
v_mfma_f32_16x16x32_bf16 a[116:119], v[68:71], v[124:127], a[116:119]
v_exp_f32 v40, v40
v_mfma_f32_16x16x32_bf16 a[96:99], v[64:67], v[120:123], a[96:99]
v_exp_f32 v41, v41
v_mfma_f32_16x16x32_bf16 a[112:115], v[64:67], v[124:127], a[112:115]
ds_read_b64_tr_b16 v[112:113], v2 offset:35200
s_waitcnt lgkmcnt(5)
v_mfma_f32_16x16x32_bf16 a[128:131], v[64:67], v[96:99], a[128:131]
ds_read_b64_tr_b16 v[114:115], v2 offset:43904
ds_read_b64_tr_b16 v[116:117], v3 offset:35200
v_mfma_f32_16x16x32_bf16 a[144:147], v[64:67], v[100:103], a[144:147]
ds_read_b64_tr_b16 v[118:119], v3 offset:43904
v_add_f32 v9, v9, v39
v_mfma_f32_16x16x32_bf16 a[132:135], v[68:71], v[96:99], a[132:135]
ds_read_b64_tr_b16 v[120:121], v2 offset:35264
ds_read_b64_tr_b16 v[122:123], v2 offset:43968
v_mfma_f32_16x16x32_bf16 a[148:151], v[68:71], v[100:103], a[148:151]
ds_read_b64_tr_b16 v[124:125], v3 offset:35264
v_add_f32 v10, v10, v40
v_mfma_f32_16x16x32_bf16 a[136:139], v[72:75], v[96:99], a[136:139]
buffer_load_dwordx4 v6, s[36:39], s49 offen offset:2176 lds
v_mfma_f32_16x16x32_bf16 a[152:155], v[72:75], v[100:103], a[152:155]
ds_read_b64_tr_b16 v[126:127], v3 offset:43968
v_add_f32 v10, v10, v41
v_mfma_f32_16x16x32_bf16 a[140:143], v[76:79], v[96:99], a[140:143]
v_exp_f32 v42, v42
v_mfma_f32_16x16x32_bf16 a[156:159], v[76:79], v[100:103], a[156:159]
v_add_f32 v10, v10, v42
s_waitcnt lgkmcnt(8)
v_mfma_f32_16x16x32_bf16 a[172:175], v[76:79], v[104:107], a[172:175]
v_exp_f32 v43, v43
v_mfma_f32_16x16x32_bf16 a[188:191], v[76:79], v[108:111], a[188:191]
v_exp_f32 v44, v44
v_mfma_f32_16x16x32_bf16 a[168:171], v[72:75], v[104:107], a[168:171]
buffer_load_dwordx4 v7, s[36:39], s49 offen offset:3264 lds
v_mfma_f32_16x16x32_bf16 a[184:187], v[72:75], v[108:111], a[184:187]
v_exp_f32 v45, v45
v_mfma_f32_16x16x32_bf16 a[164:167], v[68:71], v[104:107], a[164:167]
v_add_f32 v10, v10, v43
v_add_f32 v11, v11, v44
v_mfma_f32_16x16x32_bf16 a[180:183], v[68:71], v[108:111], a[180:183]
v_exp_f32 v46, v46
v_mfma_f32_16x16x32_bf16 a[160:163], v[64:67], v[104:107], a[160:163]
v_exp_f32 v47, v47
v_mfma_f32_16x16x32_bf16 a[176:179], v[64:67], v[108:111], a[176:179]
s_waitcnt vmcnt(16) lgkmcnt(0)
s_barrier
v_mfma_f32_16x16x32_bf16 a[192:195], v[64:67], v[112:115], a[192:195]
ds_read_b128 v[96:99], v12 offset:34816
v_add_f32 v11, v11, v45
v_mfma_f32_16x16x32_bf16 a[208:211], v[64:67], v[116:119], a[208:211]
ds_read_b128 v[100:103], v12 offset:34880
v_add_f32 v11, v11, v46
v_mfma_f32_16x16x32_bf16 a[196:199], v[68:71], v[112:115], a[196:199]
v_exp_f32 v48, v48
v_mfma_f32_16x16x32_bf16 a[212:215], v[68:71], v[116:119], a[212:215]
v_exp_f32 v49, v49
v_mfma_f32_16x16x32_bf16 a[200:203], v[72:75], v[112:115], a[200:203]
v_add_f32 v11, v11, v47
s_add_u32 s44, s44, 32768
v_mfma_f32_16x16x32_bf16 a[216:219], v[72:75], v[116:119], a[216:219]
s_add_u32 s49, s49, 32768
v_add_f32 v8, v8, v48
v_mfma_f32_16x16x32_bf16 a[204:207], v[76:79], v[112:115], a[204:207]
ds_read_b128 v[104:107], v12 offset:34944
ds_read_b128 v[108:111], v12 offset:35008
v_mfma_f32_16x16x32_bf16 a[220:223], v[76:79], v[116:119], a[220:223]
v_add_f32 v8, v8, v49
v_mfma_f32_16x16x32_bf16 a[236:239], v[76:79], v[120:123], a[236:239]
v_mfma_f32_16x16x32_bf16 a[252:255], v[76:79], v[124:127], a[252:255]
v_mfma_f32_16x16x32_bf16 a[232:235], v[72:75], v[120:123], a[232:235]
v_mfma_f32_16x16x32_bf16 a[248:251], v[72:75], v[124:127], a[248:251]
v_mfma_f32_16x16x32_bf16 a[228:231], v[68:71], v[120:123], a[228:231]
v_mfma_f32_16x16x32_bf16 a[244:247], v[68:71], v[124:127], a[244:247]
v_mfma_f32_16x16x32_bf16 a[224:227], v[64:67], v[120:123], a[224:227]
v_mfma_f32_16x16x32_bf16 a[240:243], v[64:67], v[124:127], a[240:243]
s_call_b64 s[50:51], L_resc0%=
s_branch L_l2%=_next
L_l3%=_rare:
v_max3_f32 v48, v64, v65, v66
v_max3_f32 v48, v48, v67, v80
v_max3_f32 v48, v48, v81, v82
v_max_f32 v48, v48, v83
v_mov_b32 v15, v48
s_nop 1
v_permlane32_swap_b32 v48, v15
s_nop 0
v_max_f32 v48, v48, v15
v_mov_b32 v15, v48
s_nop 1
v_permlane16_swap_b32 v48, v15
s_nop 0
v_max_f32 v48, v48, v15
v_max3_f32 v49, v68, v69, v70
v_max3_f32 v49, v49, v71, v84
v_max3_f32 v49, v49, v85, v86
v_max_f32 v49, v49, v87
v_mov_b32 v15, v49
s_nop 1
v_permlane32_swap_b32 v49, v15
s_nop 0
v_max_f32 v49, v49, v15
v_mov_b32 v15, v49
s_nop 1
v_permlane16_swap_b32 v49, v15
s_nop 0
v_max_f32 v49, v49, v15
v_max3_f32 v50, v72, v73, v74
v_max3_f32 v50, v50, v75, v88
v_max3_f32 v50, v50, v89, v90
v_max_f32 v50, v50, v91
v_mov_b32 v15, v50
s_nop 1
v_permlane32_swap_b32 v50, v15
s_nop 0
v_max_f32 v50, v50, v15
v_mov_b32 v15, v50
s_nop 1
v_permlane16_swap_b32 v50, v15
s_nop 0
v_max_f32 v50, v50, v15
v_max3_f32 v51, v76, v77, v78
v_max3_f32 v51, v51, v79, v92
v_max3_f32 v51, v51, v93, v94
v_max_f32 v51, v51, v95
v_mov_b32 v15, v51
s_nop 1
v_permlane32_swap_b32 v51, v15
s_nop 0
v_max_f32 v51, v51, v15
v_mov_b32 v15, v51
s_nop 1
v_permlane16_swap_b32 v51, v15
s_nop 0
v_max_f32 v51, v51, v15
v_max_f32 v48, 0, v48
v_sub_f32 v15, 0, v48
v_exp_f32 v52, v15
v_sub_f32 v64, v64, v48
v_sub_f32 v65, v65, v48
v_sub_f32 v66, v66, v48
v_sub_f32 v67, v67, v48
v_sub_f32 v80, v80, v48
v_sub_f32 v81, v81, v48
v_sub_f32 v82, v82, v48
v_sub_f32 v83, v83, v48
v_sub_f32 v16, v16, v48
v_sub_f32 v17, v17, v48
v_sub_f32 v18, v18, v48
v_sub_f32 v19, v19, v48
v_mul_f32 v8, v8, v52
v_max_f32 v49, 0, v49
v_sub_f32 v15, 0, v49
v_exp_f32 v53, v15
v_sub_f32 v68, v68, v49
v_sub_f32 v69, v69, v49
v_sub_f32 v70, v70, v49
v_sub_f32 v71, v71, v49
v_sub_f32 v84, v84, v49
v_sub_f32 v85, v85, v49
v_sub_f32 v86, v86, v49
v_sub_f32 v87, v87, v49
v_sub_f32 v20, v20, v49
v_sub_f32 v21, v21, v49
v_sub_f32 v22, v22, v49
v_sub_f32 v23, v23, v49
v_mul_f32 v9, v9, v53
v_max_f32 v50, 0, v50
v_sub_f32 v15, 0, v50
v_exp_f32 v54, v15
v_sub_f32 v72, v72, v50
v_sub_f32 v73, v73, v50
v_sub_f32 v74, v74, v50
v_sub_f32 v75, v75, v50
v_sub_f32 v88, v88, v50
v_sub_f32 v89, v89, v50
v_sub_f32 v90, v90, v50
v_sub_f32 v91, v91, v50
v_sub_f32 v24, v24, v50
v_sub_f32 v25, v25, v50
v_sub_f32 v26, v26, v50
v_sub_f32 v27, v27, v50
v_mul_f32 v10, v10, v54
v_max_f32 v51, 0, v51
v_sub_f32 v15, 0, v51
v_exp_f32 v55, v15
v_sub_f32 v76, v76, v51
v_sub_f32 v77, v77, v51
v_sub_f32 v78, v78, v51
v_sub_f32 v79, v79, v51
v_sub_f32 v92, v92, v51
v_sub_f32 v93, v93, v51
v_sub_f32 v94, v94, v51
v_sub_f32 v95, v95, v51
v_sub_f32 v28, v28, v51
v_sub_f32 v29, v29, v51
v_sub_f32 v30, v30, v51
v_sub_f32 v31, v31, v51
v_mul_f32 v11, v11, v55
ds_read_b64_tr_b16 v[102:103], v3 offset:8960
v_mfma_f32_16x16x32_bf16 a[56:59], v[40:43], v[108:111], a[56:59]
v_exp_f32 v64, v64
v_mfma_f32_16x16x32_bf16 a[36:39], v[36:39], v[104:107], a[36:39]
buffer_load_dwordx4 v7, s[36:39], s44 offen offset:3264 lds
v_mfma_f32_16x16x32_bf16 a[52:55], v[36:39], v[108:111], a[52:55]
v_exp_f32 v65, v65
v_mfma_f32_16x16x32_bf16 a[32:35], v[32:35], v[104:107], a[32:35]
v_exp_f32 v66, v66
v_mfma_f32_16x16x32_bf16 a[48:51], v[32:35], v[108:111], a[48:51]
v_add_f32 v8, v8, v64
s_waitcnt lgkmcnt(8)
v_mfma_f32_16x16x32_bf16 a[64:67], v[32:35], v[112:115], a[64:67]
v_exp_f32 v67, v67
v_mfma_f32_16x16x32_bf16 a[80:83], v[32:35], v[116:119], a[80:83]
s_add_u32 m0, s45, 39168
v_add_f32 v8, v8, v65
v_mfma_f32_16x16x32_bf16 a[68:71], v[36:39], v[112:115], a[68:71]
v_exp_f32 v68, v68
v_mfma_f32_16x16x32_bf16 a[84:87], v[36:39], v[116:119], a[84:87]
v_add_f32 v8, v8, v66
v_add_f32 v8, v8, v67
v_mfma_f32_16x16x32_bf16 a[72:75], v[40:43], v[112:115], a[72:75]
buffer_load_dwordx4 v4, s[36:39], s49 offen offset:0 lds
v_mfma_f32_16x16x32_bf16 a[88:91], v[40:43], v[116:119], a[88:91]
v_exp_f32 v69, v69
v_mfma_f32_16x16x32_bf16 a[76:79], v[44:47], v[112:115], a[76:79]
v_exp_f32 v70, v70
v_mfma_f32_16x16x32_bf16 a[92:95], v[44:47], v[116:119], a[92:95]
ds_read_b64_tr_b16 v[104:105], v2 offset:320
s_waitcnt lgkmcnt(5)
v_mfma_f32_16x16x32_bf16 a[108:111], v[44:47], v[120:123], a[108:111]
ds_read_b64_tr_b16 v[106:107], v2 offset:9024
ds_read_b64_tr_b16 v[108:109], v3 offset:320
v_mfma_f32_16x16x32_bf16 a[124:127], v[44:47], v[124:127], a[124:127]
ds_read_b64_tr_b16 v[110:111], v3 offset:9024
v_add_f32 v9, v9, v68
v_mfma_f32_16x16x32_bf16 a[104:107], v[40:43], v[120:123], a[104:107]
v_exp_f32 v71, v71
v_mfma_f32_16x16x32_bf16 a[120:123], v[40:43], v[124:127], a[120:123]
v_add_f32 v9, v9, v69
v_add_f32 v9, v9, v70
v_mfma_f32_16x16x32_bf16 a[100:103], v[36:39], v[120:123], a[100:103]
buffer_load_dwordx4 v5, s[36:39], s49 offen offset:1088 lds
v_mfma_f32_16x16x32_bf16 a[116:119], v[36:39], v[124:127], a[116:119]
v_exp_f32 v72, v72
v_mfma_f32_16x16x32_bf16 a[96:99], v[32:35], v[120:123], a[96:99]
v_exp_f32 v73, v73
v_mfma_f32_16x16x32_bf16 a[112:115], v[32:35], v[124:127], a[112:115]
ds_read_b64_tr_b16 v[112:113], v2 offset:384
s_waitcnt lgkmcnt(5)
v_mfma_f32_16x16x32_bf16 a[128:131], v[32:35], v[96:99], a[128:131]
ds_read_b64_tr_b16 v[114:115], v2 offset:9088
ds_read_b64_tr_b16 v[116:117], v3 offset:384
v_mfma_f32_16x16x32_bf16 a[144:147], v[32:35], v[100:103], a[144:147]
ds_read_b64_tr_b16 v[118:119], v3 offset:9088
v_add_f32 v9, v9, v71
v_mfma_f32_16x16x32_bf16 a[132:135], v[36:39], v[96:99], a[132:135]
ds_read_b64_tr_b16 v[120:121], v2 offset:448
ds_read_b64_tr_b16 v[122:123], v2 offset:9152
v_mfma_f32_16x16x32_bf16 a[148:151], v[36:39], v[100:103], a[148:151]
ds_read_b64_tr_b16 v[124:125], v3 offset:448
v_add_f32 v10, v10, v72
v_mfma_f32_16x16x32_bf16 a[136:139], v[40:43], v[96:99], a[136:139]
buffer_load_dwordx4 v6, s[36:39], s49 offen offset:2176 lds
v_mfma_f32_16x16x32_bf16 a[152:155], v[40:43], v[100:103], a[152:155]
ds_read_b64_tr_b16 v[126:127], v3 offset:9152
v_add_f32 v10, v10, v73
v_mfma_f32_16x16x32_bf16 a[140:143], v[44:47], v[96:99], a[140:143]
v_exp_f32 v74, v74
v_mfma_f32_16x16x32_bf16 a[156:159], v[44:47], v[100:103], a[156:159]
v_add_f32 v10, v10, v74
s_waitcnt lgkmcnt(8)
v_mfma_f32_16x16x32_bf16 a[172:175], v[44:47], v[104:107], a[172:175]
v_exp_f32 v75, v75
v_mfma_f32_16x16x32_bf16 a[188:191], v[44:47], v[108:111], a[188:191]
v_exp_f32 v76, v76
v_mfma_f32_16x16x32_bf16 a[168:171], v[40:43], v[104:107], a[168:171]
buffer_load_dwordx4 v7, s[36:39], s49 offen offset:3264 lds
v_mfma_f32_16x16x32_bf16 a[184:187], v[40:43], v[108:111], a[184:187]
v_exp_f32 v77, v77
v_mfma_f32_16x16x32_bf16 a[164:167], v[36:39], v[104:107], a[164:167]
v_add_f32 v10, v10, v75
v_add_f32 v11, v11, v76
v_mfma_f32_16x16x32_bf16 a[180:183], v[36:39], v[108:111], a[180:183]
v_exp_f32 v78, v78
v_mfma_f32_16x16x32_bf16 a[160:163], v[32:35], v[104:107], a[160:163]
v_exp_f32 v79, v79
v_mfma_f32_16x16x32_bf16 a[176:179], v[32:35], v[108:111], a[176:179]
s_waitcnt vmcnt(16) lgkmcnt(0)
s_barrier
v_mfma_f32_16x16x32_bf16 a[192:195], v[32:35], v[112:115], a[192:195]
ds_read_b128 v[96:99], v12 offset:0
v_add_f32 v11, v11, v77
v_mfma_f32_16x16x32_bf16 a[208:211], v[32:35], v[116:119], a[208:211]
ds_read_b128 v[100:103], v12 offset:64
v_add_f32 v11, v11, v78
v_mfma_f32_16x16x32_bf16 a[196:199], v[36:39], v[112:115], a[196:199]
v_exp_f32 v80, v80
v_mfma_f32_16x16x32_bf16 a[212:215], v[36:39], v[116:119], a[212:215]
v_exp_f32 v81, v81
v_mfma_f32_16x16x32_bf16 a[200:203], v[40:43], v[112:115], a[200:203]
v_add_f32 v11, v11, v79
s_add_u32 s44, s44, 32768
v_mfma_f32_16x16x32_bf16 a[216:219], v[40:43], v[116:119], a[216:219]
s_add_u32 s49, s49, 32768
v_add_f32 v8, v8, v80
v_mfma_f32_16x16x32_bf16 a[204:207], v[44:47], v[112:115], a[204:207]
ds_read_b128 v[104:107], v12 offset:128
ds_read_b128 v[108:111], v12 offset:192
v_mfma_f32_16x16x32_bf16 a[220:223], v[44:47], v[116:119], a[220:223]
v_add_f32 v8, v8, v81
v_mfma_f32_16x16x32_bf16 a[236:239], v[44:47], v[120:123], a[236:239]
v_mfma_f32_16x16x32_bf16 a[252:255], v[44:47], v[124:127], a[252:255]
v_mfma_f32_16x16x32_bf16 a[232:235], v[40:43], v[120:123], a[232:235]
v_mfma_f32_16x16x32_bf16 a[248:251], v[40:43], v[124:127], a[248:251]
v_mfma_f32_16x16x32_bf16 a[228:231], v[36:39], v[120:123], a[228:231]
v_mfma_f32_16x16x32_bf16 a[244:247], v[36:39], v[124:127], a[244:247]
v_mfma_f32_16x16x32_bf16 a[224:227], v[32:35], v[120:123], a[224:227]
s_sub_u32 s47, s47, 1
s_cmp_lg_u32 s47, 0
v_mfma_f32_16x16x32_bf16 a[240:243], v[32:35], v[124:127], a[240:243]
s_call_b64 s[50:51], L_resc1%=
s_cmp_lg_u32 s47, 0
s_branch L_l3%=_next
L_p30%=_rare:
v_max3_f32 v80, v32, v33, v34
v_max3_f32 v80, v80, v35, v48
v_max3_f32 v80, v80, v49, v50
v_max_f32 v80, v80, v51
v_mov_b32 v15, v80
s_nop 1
v_permlane32_swap_b32 v80, v15
s_nop 0
v_max_f32 v80, v80, v15
v_mov_b32 v15, v80
s_nop 1
v_permlane16_swap_b32 v80, v15
s_nop 0
v_max_f32 v80, v80, v15
v_max3_f32 v81, v36, v37, v38
v_max3_f32 v81, v81, v39, v52
v_max3_f32 v81, v81, v53, v54
v_max_f32 v81, v81, v55
v_mov_b32 v15, v81
s_nop 1
v_permlane32_swap_b32 v81, v15
s_nop 0
v_max_f32 v81, v81, v15
v_mov_b32 v15, v81
s_nop 1
v_permlane16_swap_b32 v81, v15
s_nop 0
v_max_f32 v81, v81, v15
v_max3_f32 v82, v40, v41, v42
v_max3_f32 v82, v82, v43, v56
v_max3_f32 v82, v82, v57, v58
v_max_f32 v82, v82, v59
v_mov_b32 v15, v82
s_nop 1
v_permlane32_swap_b32 v82, v15
s_nop 0
v_max_f32 v82, v82, v15
v_mov_b32 v15, v82
s_nop 1
v_permlane16_swap_b32 v82, v15
s_nop 0
v_max_f32 v82, v82, v15
v_max3_f32 v83, v44, v45, v46
v_max3_f32 v83, v83, v47, v60
v_max3_f32 v83, v83, v61, v62
v_max_f32 v83, v83, v63
v_mov_b32 v15, v83
s_nop 1
v_permlane32_swap_b32 v83, v15
s_nop 0
v_max_f32 v83, v83, v15
v_mov_b32 v15, v83
s_nop 1
v_permlane16_swap_b32 v83, v15
s_nop 0
v_max_f32 v83, v83, v15
v_max_f32 v80, 0, v80
v_sub_f32 v15, 0, v80
v_exp_f32 v84, v15
v_sub_f32 v32, v32, v80
v_sub_f32 v33, v33, v80
v_sub_f32 v34, v34, v80
v_sub_f32 v35, v35, v80
v_sub_f32 v48, v48, v80
v_sub_f32 v49, v49, v80
v_sub_f32 v50, v50, v80
v_sub_f32 v51, v51, v80
v_sub_f32 v16, v16, v80
v_sub_f32 v17, v17, v80
v_sub_f32 v18, v18, v80
v_sub_f32 v19, v19, v80
v_mul_f32 v8, v8, v84
v_max_f32 v81, 0, v81
v_sub_f32 v15, 0, v81
v_exp_f32 v85, v15
v_sub_f32 v36, v36, v81
v_sub_f32 v37, v37, v81
v_sub_f32 v38, v38, v81
v_sub_f32 v39, v39, v81
v_sub_f32 v52, v52, v81
v_sub_f32 v53, v53, v81
v_sub_f32 v54, v54, v81
v_sub_f32 v55, v55, v81
v_sub_f32 v20, v20, v81
v_sub_f32 v21, v21, v81
v_sub_f32 v22, v22, v81
v_sub_f32 v23, v23, v81
v_mul_f32 v9, v9, v85
v_max_f32 v82, 0, v82
v_sub_f32 v15, 0, v82
v_exp_f32 v86, v15
v_sub_f32 v40, v40, v82
v_sub_f32 v41, v41, v82
v_sub_f32 v42, v42, v82
v_sub_f32 v43, v43, v82
v_sub_f32 v56, v56, v82
v_sub_f32 v57, v57, v82
v_sub_f32 v58, v58, v82
v_sub_f32 v59, v59, v82
v_sub_f32 v24, v24, v82
v_sub_f32 v25, v25, v82
v_sub_f32 v26, v26, v82
v_sub_f32 v27, v27, v82
v_mul_f32 v10, v10, v86
v_max_f32 v83, 0, v83
v_sub_f32 v15, 0, v83
v_exp_f32 v87, v15
v_sub_f32 v44, v44, v83
v_sub_f32 v45, v45, v83
v_sub_f32 v46, v46, v83
v_sub_f32 v47, v47, v83
v_sub_f32 v60, v60, v83
v_sub_f32 v61, v61, v83
v_sub_f32 v62, v62, v83
v_sub_f32 v63, v63, v83
v_sub_f32 v28, v28, v83
v_sub_f32 v29, v29, v83
v_sub_f32 v30, v30, v83
v_sub_f32 v31, v31, v83
v_mul_f32 v11, v11, v87
ds_read_b64_tr_b16 v[100:101], v3 offset:35072
v_mfma_f32_16x16x32_bf16 a[36:39], v[68:71], v[104:107], a[36:39]
buffer_load_dword v0, s[12:15], s53 offen sc1 lds
v_mfma_f32_16x16x32_bf16 a[52:55], v[68:71], v[108:111], a[52:55]
v_exp_f32 v32, v32
v_mfma_f32_16x16x32_bf16 a[32:35], v[64:67], v[104:107], a[32:35]
v_exp_f32 v33, v33
v_mfma_f32_16x16x32_bf16 a[48:51], v[64:67], v[108:111], a[48:51]
ds_read_b64_tr_b16 v[102:103], v3 offset:43776
s_waitcnt lgkmcnt(8)
v_mfma_f32_16x16x32_bf16 a[64:67], v[64:67], v[112:115], a[64:67]
v_exp_f32 v34, v34
v_mfma_f32_16x16x32_bf16 a[80:83], v[64:67], v[116:119], a[80:83]
v_add_f32 v8, v8, v32
v_add_f32 v8, v8, v33
v_mfma_f32_16x16x32_bf16 a[68:71], v[68:71], v[112:115], a[68:71]
v_exp_f32 v35, v35
v_mfma_f32_16x16x32_bf16 a[84:87], v[68:71], v[116:119], a[84:87]
v_exp_f32 v36, v36
v_mfma_f32_16x16x32_bf16 a[72:75], v[72:75], v[112:115], a[72:75]
v_add_f32 v8, v8, v34
v_add_f32 v8, v8, v35
v_mfma_f32_16x16x32_bf16 a[88:91], v[72:75], v[116:119], a[88:91]
v_exp_f32 v37, v37
v_mfma_f32_16x16x32_bf16 a[76:79], v[76:79], v[112:115], a[76:79]
v_exp_f32 v38, v38
v_mfma_f32_16x16x32_bf16 a[92:95], v[76:79], v[116:119], a[92:95]
ds_read_b64_tr_b16 v[104:105], v2 offset:35136
s_waitcnt lgkmcnt(5)
v_mfma_f32_16x16x32_bf16 a[108:111], v[76:79], v[120:123], a[108:111]
ds_read_b64_tr_b16 v[106:107], v2 offset:43840
ds_read_b64_tr_b16 v[108:109], v3 offset:35136
v_mfma_f32_16x16x32_bf16 a[124:127], v[76:79], v[124:127], a[124:127]
ds_read_b64_tr_b16 v[110:111], v3 offset:43840
v_add_f32 v9, v9, v36
v_mfma_f32_16x16x32_bf16 a[104:107], v[72:75], v[120:123], a[104:107]
v_exp_f32 v39, v39
v_mfma_f32_16x16x32_bf16 a[120:123], v[72:75], v[124:127], a[120:123]
v_add_f32 v9, v9, v37
v_add_f32 v9, v9, v38
v_mfma_f32_16x16x32_bf16 a[100:103], v[68:71], v[120:123], a[100:103]
v_exp_f32 v40, v40
v_mfma_f32_16x16x32_bf16 a[116:119], v[68:71], v[124:127], a[116:119]
v_exp_f32 v41, v41
v_mfma_f32_16x16x32_bf16 a[96:99], v[64:67], v[120:123], a[96:99]
ds_read_b64_tr_b16 v[112:113], v2 offset:35200
ds_read_b64_tr_b16 v[114:115], v2 offset:43904
v_mfma_f32_16x16x32_bf16 a[112:115], v[64:67], v[124:127], a[112:115]
ds_read_b64_tr_b16 v[116:117], v3 offset:35200
s_waitcnt lgkmcnt(7)
v_mfma_f32_16x16x32_bf16 a[128:131], v[64:67], v[96:99], a[128:131]
ds_read_b64_tr_b16 v[118:119], v3 offset:43904
ds_read_b64_tr_b16 v[120:121], v2 offset:35264
v_mfma_f32_16x16x32_bf16 a[144:147], v[64:67], v[100:103], a[144:147]
ds_read_b64_tr_b16 v[122:123], v2 offset:43968
v_add_f32 v9, v9, v39
v_mfma_f32_16x16x32_bf16 a[132:135], v[68:71], v[96:99], a[132:135]
ds_read_b64_tr_b16 v[124:125], v3 offset:35264
ds_read_b64_tr_b16 v[126:127], v3 offset:43968
v_mfma_f32_16x16x32_bf16 a[148:151], v[68:71], v[100:103], a[148:151]
v_exp_f32 v42, v42
v_mfma_f32_16x16x32_bf16 a[136:139], v[72:75], v[96:99], a[136:139]
v_add_f32 v10, v10, v40
v_add_f32 v10, v10, v41
v_mfma_f32_16x16x32_bf16 a[152:155], v[72:75], v[100:103], a[152:155]
v_exp_f32 v43, v43
v_mfma_f32_16x16x32_bf16 a[140:143], v[76:79], v[96:99], a[140:143]
s_waitcnt vmcnt(2)
v_xor_b32 v88, s76, v88
v_mfma_f32_16x16x32_bf16 a[156:159], v[76:79], v[100:103], a[156:159]
v_xor_b32 v89, s77, v89
s_waitcnt lgkmcnt(8)
v_mfma_f32_16x16x32_bf16 a[172:175], v[76:79], v[104:107], a[172:175]
v_xor_b32 v90, s76, v90
v_xor_b32 v91, s77, v91
v_mfma_f32_16x16x32_bf16 a[188:191], v[76:79], v[108:111], a[188:191]
v_xor_b32 v92, s76, v92
v_xor_b32 v93, s77, v93
v_mfma_f32_16x16x32_bf16 a[168:171], v[72:75], v[104:107], a[168:171]
v_xor_b32 v94, s76, v94
v_xor_b32 v95, s77, v95
v_mfma_f32_16x16x32_bf16 a[184:187], v[72:75], v[108:111], a[184:187]
v_or3_b32 v88, v88, v89, v90
v_or3_b32 v92, v92, v93, v94
v_mfma_f32_16x16x32_bf16 a[164:167], v[68:71], v[104:107], a[164:167]
v_exp_f32 v44, v44
v_mfma_f32_16x16x32_bf16 a[180:183], v[68:71], v[108:111], a[180:183]
v_or3_b32 v88, v88, v91, v95
v_or_b32 v88, v88, v92
v_mfma_f32_16x16x32_bf16 a[160:163], v[64:67], v[104:107], a[160:163]
v_cmp_ne_u32 vcc, 0, v88
v_add_f32 v10, v10, v42
v_mfma_f32_16x16x32_bf16 a[176:179], v[64:67], v[108:111], a[176:179]
s_waitcnt vmcnt(10) lgkmcnt(0)
s_barrier
v_exp_f32 v45, v45
v_mfma_f32_16x16x32_bf16 a[192:195], v[64:67], v[112:115], a[192:195]
v_add_f32 v10, v10, v43
v_add_f32 v11, v11, v44
v_mfma_f32_16x16x32_bf16 a[208:211], v[64:67], v[116:119], a[208:211]
s_cbranch_vccz 1
s_call_b64 s[30:31], L_alr%=
v_mfma_f32_16x16x32_bf16 a[196:199], v[68:71], v[112:115], a[196:199]
v_exp_f32 v46, v46
v_mfma_f32_16x16x32_bf16 a[212:215], v[68:71], v[116:119], a[212:215]
s_add_u32 m0, s10, 0
s_add_u32 s55, s3, 0
v_mfma_f32_16x16x32_bf16 a[200:203], v[72:75], v[112:115], a[200:203]
buffer_load_dwordx4 v1, s[60:63], s55 offen offset:0 sc1 lds
v_exp_f32 v47, v47
v_mfma_f32_16x16x32_bf16 a[216:219], v[72:75], v[116:119], a[216:219]
ds_read_b128 v[96:99], v12 offset:34816
v_add_f32 v11, v11, v45
v_mfma_f32_16x16x32_bf16 a[204:207], v[76:79], v[112:115], a[204:207]
ds_read_b128 v[100:103], v12 offset:34880
buffer_load_dwordx4 v1, s[60:63], s55 offen offset:1024 sc1 lds
v_mfma_f32_16x16x32_bf16 a[220:223], v[76:79], v[116:119], a[220:223]
v_exp_f32 v48, v48
v_mfma_f32_16x16x32_bf16 a[236:239], v[76:79], v[120:123], a[236:239]
buffer_load_dwordx4 v1, s[60:63], s55 offen offset:2048 sc1 lds
v_add_f32 v11, v11, v46
v_mfma_f32_16x16x32_bf16 a[252:255], v[76:79], v[124:127], a[252:255]
v_exp_f32 v49, v49
v_mfma_f32_16x16x32_bf16 a[232:235], v[72:75], v[120:123], a[232:235]
buffer_load_dwordx4 v1, s[60:63], s55 offen offset:3072 sc1 lds
v_add_f32 v11, v11, v47
v_mfma_f32_16x16x32_bf16 a[248:251], v[72:75], v[124:127], a[248:251]
s_add_u32 m0, s11, 16384
s_add_u32 s55, s16, 16384
v_mfma_f32_16x16x32_bf16 a[228:231], v[68:71], v[120:123], a[228:231]
buffer_load_dwordx4 v1, s[60:63], s55 offen offset:0 sc1 lds
v_mfma_f32_16x16x32_bf16 a[244:247], v[68:71], v[124:127], a[244:247]
s_add_u32 s44, s44, 32768
s_add_u32 s49, s49, 32768
v_add_f32 v8, v8, v48
v_mfma_f32_16x16x32_bf16 a[224:227], v[64:67], v[120:123], a[224:227]
ds_read_b128 v[104:107], v12 offset:34944
ds_read_b128 v[108:111], v12 offset:35008
buffer_load_dwordx4 v1, s[60:63], s55 offen offset:1024 sc1 lds
v_add_f32 v8, v8, v49
v_mfma_f32_16x16x32_bf16 a[240:243], v[64:67], v[124:127], a[240:243]
s_call_b64 s[50:51], L_resc0%=
s_branch L_p30%=_next
L_p31%=_rare:
v_max3_f32 v48, v64, v65, v66
v_max3_f32 v48, v48, v67, v80
v_max3_f32 v48, v48, v81, v82
v_max_f32 v48, v48, v83
v_mov_b32 v15, v48
s_nop 1
v_permlane32_swap_b32 v48, v15
s_nop 0
v_max_f32 v48, v48, v15
v_mov_b32 v15, v48
s_nop 1
v_permlane16_swap_b32 v48, v15
s_nop 0
v_max_f32 v48, v48, v15
v_max3_f32 v49, v68, v69, v70
v_max3_f32 v49, v49, v71, v84
v_max3_f32 v49, v49, v85, v86
v_max_f32 v49, v49, v87
v_mov_b32 v15, v49
s_nop 1
v_permlane32_swap_b32 v49, v15
s_nop 0
v_max_f32 v49, v49, v15
v_mov_b32 v15, v49
s_nop 1
v_permlane16_swap_b32 v49, v15
s_nop 0
v_max_f32 v49, v49, v15
v_max3_f32 v50, v72, v73, v74
v_max3_f32 v50, v50, v75, v88
v_max3_f32 v50, v50, v89, v90
v_max_f32 v50, v50, v91
v_mov_b32 v15, v50
s_nop 1
v_permlane32_swap_b32 v50, v15
s_nop 0
v_max_f32 v50, v50, v15
v_mov_b32 v15, v50
s_nop 1
v_permlane16_swap_b32 v50, v15
s_nop 0
v_max_f32 v50, v50, v15
v_max3_f32 v51, v76, v77, v78
v_max3_f32 v51, v51, v79, v92
v_max3_f32 v51, v51, v93, v94
v_max_f32 v51, v51, v95
v_mov_b32 v15, v51
s_nop 1
v_permlane32_swap_b32 v51, v15
s_nop 0
v_max_f32 v51, v51, v15
v_mov_b32 v15, v51
s_nop 1
v_permlane16_swap_b32 v51, v15
s_nop 0
v_max_f32 v51, v51, v15
v_max_f32 v48, 0, v48
v_sub_f32 v15, 0, v48
v_exp_f32 v52, v15
v_sub_f32 v64, v64, v48
v_sub_f32 v65, v65, v48
v_sub_f32 v66, v66, v48
v_sub_f32 v67, v67, v48
v_sub_f32 v80, v80, v48
v_sub_f32 v81, v81, v48
v_sub_f32 v82, v82, v48
v_sub_f32 v83, v83, v48
v_sub_f32 v16, v16, v48
v_sub_f32 v17, v17, v48
v_sub_f32 v18, v18, v48
v_sub_f32 v19, v19, v48
v_mul_f32 v8, v8, v52
v_max_f32 v49, 0, v49
v_sub_f32 v15, 0, v49
v_exp_f32 v53, v15
v_sub_f32 v68, v68, v49
v_sub_f32 v69, v69, v49
v_sub_f32 v70, v70, v49
v_sub_f32 v71, v71, v49
v_sub_f32 v84, v84, v49
v_sub_f32 v85, v85, v49
v_sub_f32 v86, v86, v49
v_sub_f32 v87, v87, v49
v_sub_f32 v20, v20, v49
v_sub_f32 v21, v21, v49
v_sub_f32 v22, v22, v49
v_sub_f32 v23, v23, v49
v_mul_f32 v9, v9, v53
v_max_f32 v50, 0, v50
v_sub_f32 v15, 0, v50
v_exp_f32 v54, v15
v_sub_f32 v72, v72, v50
v_sub_f32 v73, v73, v50
v_sub_f32 v74, v74, v50
v_sub_f32 v75, v75, v50
v_sub_f32 v88, v88, v50
v_sub_f32 v89, v89, v50
v_sub_f32 v90, v90, v50
v_sub_f32 v91, v91, v50
v_sub_f32 v24, v24, v50
v_sub_f32 v25, v25, v50
v_sub_f32 v26, v26, v50
v_sub_f32 v27, v27, v50
v_mul_f32 v10, v10, v54
v_max_f32 v51, 0, v51
v_sub_f32 v15, 0, v51
v_exp_f32 v55, v15
v_sub_f32 v76, v76, v51
v_sub_f32 v77, v77, v51
v_sub_f32 v78, v78, v51
v_sub_f32 v79, v79, v51
v_sub_f32 v92, v92, v51
v_sub_f32 v93, v93, v51
v_sub_f32 v94, v94, v51
v_sub_f32 v95, v95, v51
v_sub_f32 v28, v28, v51
v_sub_f32 v29, v29, v51
v_sub_f32 v30, v30, v51
v_sub_f32 v31, v31, v51
v_mul_f32 v11, v11, v55
ds_read_b64_tr_b16 v[98:99], v2 offset:8960
v_mfma_f32_16x16x32_bf16 a[60:63], v[44:47], v[108:111], a[60:63]
v_exp_f32 v64, v64
v_mfma_f32_16x16x32_bf16 a[40:43], v[40:43], v[104:107], a[40:43]
v_exp_f32 v65, v65
v_mfma_f32_16x16x32_bf16 a[56:59], v[40:43], v[108:111], a[56:59]
v_exp_f32 v66, v66
v_mfma_f32_16x16x32_bf16 a[36:39], v[36:39], v[104:107], a[36:39]
v_exp_f32 v67, v67
v_mfma_f32_16x16x32_bf16 a[52:55], v[36:39], v[108:111], a[52:55]
v_exp_f32 v68, v68
v_mfma_f32_16x16x32_bf16 a[32:35], v[32:35], v[104:107], a[32:35]
ds_read_b64_tr_b16 v[100:101], v3 offset:256
ds_read_b64_tr_b16 v[102:103], v3 offset:8960
v_mfma_f32_16x16x32_bf16 a[48:51], v[32:35], v[108:111], a[48:51]
v_add_f32 v8, v8, v64
s_waitcnt lgkmcnt(8)
v_mfma_f32_16x16x32_bf16 a[64:67], v[32:35], v[112:115], a[64:67]
v_exp_f32 v69, v69
v_mfma_f32_16x16x32_bf16 a[80:83], v[32:35], v[116:119], a[80:83]
v_add_f32 v8, v8, v65
v_add_f32 v8, v8, v66
v_mfma_f32_16x16x32_bf16 a[68:71], v[36:39], v[112:115], a[68:71]
v_exp_f32 v70, v70
v_mfma_f32_16x16x32_bf16 a[84:87], v[36:39], v[116:119], a[84:87]
v_add_f32 v8, v8, v67
v_add_f32 v9, v9, v68
v_mfma_f32_16x16x32_bf16 a[72:75], v[40:43], v[112:115], a[72:75]
v_exp_f32 v71, v71
v_mfma_f32_16x16x32_bf16 a[88:91], v[40:43], v[116:119], a[88:91]
v_exp_f32 v72, v72
v_mfma_f32_16x16x32_bf16 a[76:79], v[44:47], v[112:115], a[76:79]
ds_read_b64_tr_b16 v[104:105], v2 offset:320
v_add_f32 v9, v9, v69
v_mfma_f32_16x16x32_bf16 a[92:95], v[44:47], v[116:119], a[92:95]
ds_read_b64_tr_b16 v[106:107], v2 offset:9024
s_waitcnt lgkmcnt(6)
v_mfma_f32_16x16x32_bf16 a[108:111], v[44:47], v[120:123], a[108:111]
v_exp_f32 v73, v73
v_mfma_f32_16x16x32_bf16 a[124:127], v[44:47], v[124:127], a[124:127]
ds_read_b64_tr_b16 v[108:109], v3 offset:320
ds_read_b64_tr_b16 v[110:111], v3 offset:9024
v_mfma_f32_16x16x32_bf16 a[104:107], v[40:43], v[120:123], a[104:107]
v_exp_f32 v74, v74
v_mfma_f32_16x16x32_bf16 a[120:123], v[40:43], v[124:127], a[120:123]
v_add_f32 v9, v9, v70
v_add_f32 v9, v9, v71
v_mfma_f32_16x16x32_bf16 a[100:103], v[36:39], v[120:123], a[100:103]
v_exp_f32 v75, v75
v_mfma_f32_16x16x32_bf16 a[116:119], v[36:39], v[124:127], a[116:119]
v_exp_f32 v76, v76
v_mfma_f32_16x16x32_bf16 a[96:99], v[32:35], v[120:123], a[96:99]
v_add_f32 v10, v10, v72
v_add_f32 v10, v10, v73
v_mfma_f32_16x16x32_bf16 a[112:115], v[32:35], v[124:127], a[112:115]
v_add_f32 v10, v10, v74
s_waitcnt lgkmcnt(4)
v_mfma_f32_16x16x32_bf16 a[128:131], v[32:35], v[96:99], a[128:131]
v_exp_f32 v77, v77
v_mfma_f32_16x16x32_bf16 a[144:147], v[32:35], v[100:103], a[144:147]
v_exp_f32 v78, v78
v_mfma_f32_16x16x32_bf16 a[132:135], v[36:39], v[96:99], a[132:135]
ds_read_b64_tr_b16 v[112:113], v2 offset:384
ds_read_b64_tr_b16 v[114:115], v2 offset:9088
v_mfma_f32_16x16x32_bf16 a[148:151], v[36:39], v[100:103], a[148:151]
ds_read_b64_tr_b16 v[116:117], v3 offset:384
ds_read_b64_tr_b16 v[118:119], v3 offset:9088
v_mfma_f32_16x16x32_bf16 a[136:139], v[40:43], v[96:99], a[136:139]
v_exp_f32 v79, v79
v_mfma_f32_16x16x32_bf16 a[152:155], v[40:43], v[100:103], a[152:155]
ds_read_b64_tr_b16 v[120:121], v2 offset:448
ds_read_b64_tr_b16 v[122:123], v2 offset:9152
v_mfma_f32_16x16x32_bf16 a[140:143], v[44:47], v[96:99], a[140:143]
ds_read_b64_tr_b16 v[124:125], v3 offset:448
ds_read_b64_tr_b16 v[126:127], v3 offset:9152
v_mfma_f32_16x16x32_bf16 a[156:159], v[44:47], v[100:103], a[156:159]
v_add_f32 v10, v10, v75
s_waitcnt lgkmcnt(8)
v_mfma_f32_16x16x32_bf16 a[172:175], v[44:47], v[104:107], a[172:175]
v_add_f32 v11, v11, v76
v_add_f32 v11, v11, v77
v_mfma_f32_16x16x32_bf16 a[188:191], v[44:47], v[108:111], a[188:191]
v_exp_f32 v80, v80
v_mfma_f32_16x16x32_bf16 a[168:171], v[40:43], v[104:107], a[168:171]
v_exp_f32 v81, v81
v_mfma_f32_16x16x32_bf16 a[184:187], v[40:43], v[108:111], a[184:187]
v_exp_f32 v82, v82
v_mfma_f32_16x16x32_bf16 a[164:167], v[36:39], v[104:107], a[164:167]
v_add_f32 v11, v11, v78
v_add_f32 v11, v11, v79
v_mfma_f32_16x16x32_bf16 a[180:183], v[36:39], v[108:111], a[180:183]
v_exp_f32 v83, v83
v_mfma_f32_16x16x32_bf16 a[160:163], v[32:35], v[104:107], a[160:163]
v_exp_f32 v84, v84
v_mfma_f32_16x16x32_bf16 a[176:179], v[32:35], v[108:111], a[176:179]
v_add_f32 v8, v8, v80
v_add_f32 v8, v8, v81
s_waitcnt lgkmcnt(4)
v_mfma_f32_16x16x32_bf16 a[192:195], v[32:35], v[112:115], a[192:195]
v_exp_f32 v85, v85
v_mfma_f32_16x16x32_bf16 a[208:211], v[32:35], v[116:119], a[208:211]
v_exp_f32 v86, v86
v_mfma_f32_16x16x32_bf16 a[196:199], v[36:39], v[112:115], a[196:199]
v_add_f32 v8, v8, v82
v_add_f32 v8, v8, v83
v_mfma_f32_16x16x32_bf16 a[212:215], v[36:39], v[116:119], a[212:215]
v_exp_f32 v87, v87
v_mfma_f32_16x16x32_bf16 a[200:203], v[40:43], v[112:115], a[200:203]
v_exp_f32 v88, v88
v_mfma_f32_16x16x32_bf16 a[216:219], v[40:43], v[116:119], a[216:219]
v_add_f32 v9, v9, v84
v_add_f32 v9, v9, v85
v_mfma_f32_16x16x32_bf16 a[204:207], v[44:47], v[112:115], a[204:207]
v_exp_f32 v89, v89
v_mfma_f32_16x16x32_bf16 a[220:223], v[44:47], v[116:119], a[220:223]
v_exp_f32 v90, v90
s_waitcnt lgkmcnt(0)
v_mfma_f32_16x16x32_bf16 a[236:239], v[44:47], v[120:123], a[236:239]
v_add_f32 v9, v9, v86
v_add_f32 v9, v9, v87
v_mfma_f32_16x16x32_bf16 a[252:255], v[44:47], v[124:127], a[252:255]
v_exp_f32 v91, v91
v_mfma_f32_16x16x32_bf16 a[232:235], v[40:43], v[120:123], a[232:235]
v_add_f32 v10, v10, v88
s_add_u32 s44, s44, 32768
v_mfma_f32_16x16x32_bf16 a[248:251], v[40:43], v[124:127], a[248:251]
s_add_u32 s49, s49, 32768
v_add_f32 v10, v10, v89
v_mfma_f32_16x16x32_bf16 a[228:231], v[36:39], v[120:123], a[228:231]
v_add_f32 v10, v10, v90
v_add_f32 v10, v10, v91
v_mfma_f32_16x16x32_bf16 a[244:247], v[36:39], v[124:127], a[244:247]
v_mfma_f32_16x16x32_bf16 a[224:227], v[32:35], v[120:123], a[224:227]
v_mfma_f32_16x16x32_bf16 a[240:243], v[32:35], v[124:127], a[240:243]
s_call_b64 s[50:51], L_resc1%=
s_branch L_p31%=_next
L_resc0%=:
s_nop 15
v_mbcnt_lo_u32_b32 v15, -1, 0
v_mbcnt_hi_u32_b32 v15, -1, v15
v_and_b32 v15, 48, v15
ds_bpermute_b32 v64, v15, v84 offset:0
ds_bpermute_b32 v65, v15, v84 offset:4
ds_bpermute_b32 v66, v15, v84 offset:8
ds_bpermute_b32 v67, v15, v84 offset:12
ds_bpermute_b32 v68, v15, v85 offset:0
ds_bpermute_b32 v69, v15, v85 offset:4
ds_bpermute_b32 v70, v15, v85 offset:8
ds_bpermute_b32 v71, v15, v85 offset:12
ds_bpermute_b32 v72, v15, v86 offset:0
ds_bpermute_b32 v73, v15, v86 offset:4
ds_bpermute_b32 v74, v15, v86 offset:8
ds_bpermute_b32 v75, v15, v86 offset:12
ds_bpermute_b32 v76, v15, v87 offset:0
ds_bpermute_b32 v77, v15, v87 offset:4
ds_bpermute_b32 v78, v15, v87 offset:8
ds_bpermute_b32 v79, v15, v87 offset:12
s_waitcnt lgkmcnt(0)
v_accvgpr_read_b32 v88, a0
v_accvgpr_read_b32 v89, a1
v_accvgpr_read_b32 v90, a2
v_accvgpr_read_b32 v91, a3
v_accvgpr_read_b32 v92, a4
v_accvgpr_read_b32 v93, a5
v_accvgpr_read_b32 v94, a6
v_accvgpr_read_b32 v95, a7
v_mul_f32 v88, v88, v64
v_mul_f32 v89, v89, v65
v_mul_f32 v90, v90, v66
v_mul_f32 v91, v91, v67
v_mul_f32 v92, v92, v68
v_mul_f32 v93, v93, v69
v_mul_f32 v94, v94, v70
v_mul_f32 v95, v95, v71
v_accvgpr_write_b32 a0, v88
v_accvgpr_write_b32 a1, v89
v_accvgpr_write_b32 a2, v90
v_accvgpr_write_b32 a3, v91
v_accvgpr_write_b32 a4, v92
v_accvgpr_write_b32 a5, v93
v_accvgpr_write_b32 a6, v94
v_accvgpr_write_b32 a7, v95
v_accvgpr_read_b32 v88, a8
v_accvgpr_read_b32 v89, a9
v_accvgpr_read_b32 v90, a10
v_accvgpr_read_b32 v91, a11
v_accvgpr_read_b32 v92, a12
v_accvgpr_read_b32 v93, a13
v_accvgpr_read_b32 v94, a14
v_accvgpr_read_b32 v95, a15
v_mul_f32 v88, v88, v72
v_mul_f32 v89, v89, v73
v_mul_f32 v90, v90, v74
v_mul_f32 v91, v91, v75
v_mul_f32 v92, v92, v76
v_mul_f32 v93, v93, v77
v_mul_f32 v94, v94, v78
v_mul_f32 v95, v95, v79
v_accvgpr_write_b32 a8, v88
v_accvgpr_write_b32 a9, v89
v_accvgpr_write_b32 a10, v90
v_accvgpr_write_b32 a11, v91
v_accvgpr_write_b32 a12, v92
v_accvgpr_write_b32 a13, v93
v_accvgpr_write_b32 a14, v94
v_accvgpr_write_b32 a15, v95
v_accvgpr_read_b32 v88, a16
v_accvgpr_read_b32 v89, a17
v_accvgpr_read_b32 v90, a18
v_accvgpr_read_b32 v91, a19
v_accvgpr_read_b32 v92, a20
v_accvgpr_read_b32 v93, a21
v_accvgpr_read_b32 v94, a22
v_accvgpr_read_b32 v95, a23
v_mul_f32 v88, v88, v64
v_mul_f32 v89, v89, v65
v_mul_f32 v90, v90, v66
v_mul_f32 v91, v91, v67
v_mul_f32 v92, v92, v68
v_mul_f32 v93, v93, v69
v_mul_f32 v94, v94, v70
v_mul_f32 v95, v95, v71
v_accvgpr_write_b32 a16, v88
v_accvgpr_write_b32 a17, v89
v_accvgpr_write_b32 a18, v90
v_accvgpr_write_b32 a19, v91
v_accvgpr_write_b32 a20, v92
v_accvgpr_write_b32 a21, v93
v_accvgpr_write_b32 a22, v94
v_accvgpr_write_b32 a23, v95
v_accvgpr_read_b32 v88, a24
v_accvgpr_read_b32 v89, a25
v_accvgpr_read_b32 v90, a26
v_accvgpr_read_b32 v91, a27
v_accvgpr_read_b32 v92, a28
v_accvgpr_read_b32 v93, a29
v_accvgpr_read_b32 v94, a30
v_accvgpr_read_b32 v95, a31
v_mul_f32 v88, v88, v72
v_mul_f32 v89, v89, v73
v_mul_f32 v90, v90, v74
v_mul_f32 v91, v91, v75
v_mul_f32 v92, v92, v76
v_mul_f32 v93, v93, v77
v_mul_f32 v94, v94, v78
v_mul_f32 v95, v95, v79
v_accvgpr_write_b32 a24, v88
v_accvgpr_write_b32 a25, v89
v_accvgpr_write_b32 a26, v90
v_accvgpr_write_b32 a27, v91
v_accvgpr_write_b32 a28, v92
v_accvgpr_write_b32 a29, v93
v_accvgpr_write_b32 a30, v94
v_accvgpr_write_b32 a31, v95
v_accvgpr_read_b32 v88, a32
v_accvgpr_read_b32 v89, a33
v_accvgpr_read_b32 v90, a34
v_accvgpr_read_b32 v91, a35
v_accvgpr_read_b32 v92, a36
v_accvgpr_read_b32 v93, a37
v_accvgpr_read_b32 v94, a38
v_accvgpr_read_b32 v95, a39
v_mul_f32 v88, v88, v64
v_mul_f32 v89, v89, v65
v_mul_f32 v90, v90, v66
v_mul_f32 v91, v91, v67
v_mul_f32 v92, v92, v68
v_mul_f32 v93, v93, v69
v_mul_f32 v94, v94, v70
v_mul_f32 v95, v95, v71
v_accvgpr_write_b32 a32, v88
v_accvgpr_write_b32 a33, v89
v_accvgpr_write_b32 a34, v90
v_accvgpr_write_b32 a35, v91
v_accvgpr_write_b32 a36, v92
v_accvgpr_write_b32 a37, v93
v_accvgpr_write_b32 a38, v94
v_accvgpr_write_b32 a39, v95
v_accvgpr_read_b32 v88, a40
v_accvgpr_read_b32 v89, a41
v_accvgpr_read_b32 v90, a42
v_accvgpr_read_b32 v91, a43
v_accvgpr_read_b32 v92, a44
v_accvgpr_read_b32 v93, a45
v_accvgpr_read_b32 v94, a46
v_accvgpr_read_b32 v95, a47
v_mul_f32 v88, v88, v72
v_mul_f32 v89, v89, v73
v_mul_f32 v90, v90, v74
v_mul_f32 v91, v91, v75
v_mul_f32 v92, v92, v76
v_mul_f32 v93, v93, v77
v_mul_f32 v94, v94, v78
v_mul_f32 v95, v95, v79
v_accvgpr_write_b32 a40, v88
v_accvgpr_write_b32 a41, v89
v_accvgpr_write_b32 a42, v90
v_accvgpr_write_b32 a43, v91
v_accvgpr_write_b32 a44, v92
v_accvgpr_write_b32 a45, v93
v_accvgpr_write_b32 a46, v94
v_accvgpr_write_b32 a47, v95
v_accvgpr_read_b32 v88, a48
v_accvgpr_read_b32 v89, a49
v_accvgpr_read_b32 v90, a50
v_accvgpr_read_b32 v91, a51
v_accvgpr_read_b32 v92, a52
v_accvgpr_read_b32 v93, a53
v_accvgpr_read_b32 v94, a54
v_accvgpr_read_b32 v95, a55
v_mul_f32 v88, v88, v64
v_mul_f32 v89, v89, v65
v_mul_f32 v90, v90, v66
v_mul_f32 v91, v91, v67
v_mul_f32 v92, v92, v68
v_mul_f32 v93, v93, v69
v_mul_f32 v94, v94, v70
v_mul_f32 v95, v95, v71
v_accvgpr_write_b32 a48, v88
v_accvgpr_write_b32 a49, v89
v_accvgpr_write_b32 a50, v90
v_accvgpr_write_b32 a51, v91
v_accvgpr_write_b32 a52, v92
v_accvgpr_write_b32 a53, v93
v_accvgpr_write_b32 a54, v94
v_accvgpr_write_b32 a55, v95
v_accvgpr_read_b32 v88, a56
v_accvgpr_read_b32 v89, a57
v_accvgpr_read_b32 v90, a58
v_accvgpr_read_b32 v91, a59
v_accvgpr_read_b32 v92, a60
v_accvgpr_read_b32 v93, a61
v_accvgpr_read_b32 v94, a62
v_accvgpr_read_b32 v95, a63
v_mul_f32 v88, v88, v72
v_mul_f32 v89, v89, v73
v_mul_f32 v90, v90, v74
v_mul_f32 v91, v91, v75
v_mul_f32 v92, v92, v76
v_mul_f32 v93, v93, v77
v_mul_f32 v94, v94, v78
v_mul_f32 v95, v95, v79
v_accvgpr_write_b32 a56, v88
v_accvgpr_write_b32 a57, v89
v_accvgpr_write_b32 a58, v90
v_accvgpr_write_b32 a59, v91
v_accvgpr_write_b32 a60, v92
v_accvgpr_write_b32 a61, v93
v_accvgpr_write_b32 a62, v94
v_accvgpr_write_b32 a63, v95
v_accvgpr_read_b32 v88, a64
v_accvgpr_read_b32 v89, a65
v_accvgpr_read_b32 v90, a66
v_accvgpr_read_b32 v91, a67
v_accvgpr_read_b32 v92, a68
v_accvgpr_read_b32 v93, a69
v_accvgpr_read_b32 v94, a70
v_accvgpr_read_b32 v95, a71
v_mul_f32 v88, v88, v64
v_mul_f32 v89, v89, v65
v_mul_f32 v90, v90, v66
v_mul_f32 v91, v91, v67
v_mul_f32 v92, v92, v68
v_mul_f32 v93, v93, v69
v_mul_f32 v94, v94, v70
v_mul_f32 v95, v95, v71
v_accvgpr_write_b32 a64, v88
v_accvgpr_write_b32 a65, v89
v_accvgpr_write_b32 a66, v90
v_accvgpr_write_b32 a67, v91
v_accvgpr_write_b32 a68, v92
v_accvgpr_write_b32 a69, v93
v_accvgpr_write_b32 a70, v94
v_accvgpr_write_b32 a71, v95
v_accvgpr_read_b32 v88, a72
v_accvgpr_read_b32 v89, a73
v_accvgpr_read_b32 v90, a74
v_accvgpr_read_b32 v91, a75
v_accvgpr_read_b32 v92, a76
v_accvgpr_read_b32 v93, a77
v_accvgpr_read_b32 v94, a78
v_accvgpr_read_b32 v95, a79
v_mul_f32 v88, v88, v72
v_mul_f32 v89, v89, v73
v_mul_f32 v90, v90, v74
v_mul_f32 v91, v91, v75
v_mul_f32 v92, v92, v76
v_mul_f32 v93, v93, v77
v_mul_f32 v94, v94, v78
v_mul_f32 v95, v95, v79
v_accvgpr_write_b32 a72, v88
v_accvgpr_write_b32 a73, v89
v_accvgpr_write_b32 a74, v90
v_accvgpr_write_b32 a75, v91
v_accvgpr_write_b32 a76, v92
v_accvgpr_write_b32 a77, v93
v_accvgpr_write_b32 a78, v94
v_accvgpr_write_b32 a79, v95
v_accvgpr_read_b32 v88, a80
v_accvgpr_read_b32 v89, a81
v_accvgpr_read_b32 v90, a82
v_accvgpr_read_b32 v91, a83
v_accvgpr_read_b32 v92, a84
v_accvgpr_read_b32 v93, a85
v_accvgpr_read_b32 v94, a86
v_accvgpr_read_b32 v95, a87
v_mul_f32 v88, v88, v64
v_mul_f32 v89, v89, v65
v_mul_f32 v90, v90, v66
v_mul_f32 v91, v91, v67
v_mul_f32 v92, v92, v68
v_mul_f32 v93, v93, v69
v_mul_f32 v94, v94, v70
v_mul_f32 v95, v95, v71
v_accvgpr_write_b32 a80, v88
v_accvgpr_write_b32 a81, v89
v_accvgpr_write_b32 a82, v90
v_accvgpr_write_b32 a83, v91
v_accvgpr_write_b32 a84, v92
v_accvgpr_write_b32 a85, v93
v_accvgpr_write_b32 a86, v94
v_accvgpr_write_b32 a87, v95
v_accvgpr_read_b32 v88, a88
v_accvgpr_read_b32 v89, a89
v_accvgpr_read_b32 v90, a90
v_accvgpr_read_b32 v91, a91
v_accvgpr_read_b32 v92, a92
v_accvgpr_read_b32 v93, a93
v_accvgpr_read_b32 v94, a94
v_accvgpr_read_b32 v95, a95
v_mul_f32 v88, v88, v72
v_mul_f32 v89, v89, v73
v_mul_f32 v90, v90, v74
v_mul_f32 v91, v91, v75
v_mul_f32 v92, v92, v76
v_mul_f32 v93, v93, v77
v_mul_f32 v94, v94, v78
v_mul_f32 v95, v95, v79
v_accvgpr_write_b32 a88, v88
v_accvgpr_write_b32 a89, v89
v_accvgpr_write_b32 a90, v90
v_accvgpr_write_b32 a91, v91
v_accvgpr_write_b32 a92, v92
v_accvgpr_write_b32 a93, v93
v_accvgpr_write_b32 a94, v94
v_accvgpr_write_b32 a95, v95
v_accvgpr_read_b32 v88, a96
v_accvgpr_read_b32 v89, a97
v_accvgpr_read_b32 v90, a98
v_accvgpr_read_b32 v91, a99
v_accvgpr_read_b32 v92, a100
v_accvgpr_read_b32 v93, a101
v_accvgpr_read_b32 v94, a102
v_accvgpr_read_b32 v95, a103
v_mul_f32 v88, v88, v64
v_mul_f32 v89, v89, v65
v_mul_f32 v90, v90, v66
v_mul_f32 v91, v91, v67
v_mul_f32 v92, v92, v68
v_mul_f32 v93, v93, v69
v_mul_f32 v94, v94, v70
v_mul_f32 v95, v95, v71
v_accvgpr_write_b32 a96, v88
v_accvgpr_write_b32 a97, v89
v_accvgpr_write_b32 a98, v90
v_accvgpr_write_b32 a99, v91
v_accvgpr_write_b32 a100, v92
v_accvgpr_write_b32 a101, v93
v_accvgpr_write_b32 a102, v94
v_accvgpr_write_b32 a103, v95
v_accvgpr_read_b32 v88, a104
v_accvgpr_read_b32 v89, a105
v_accvgpr_read_b32 v90, a106
v_accvgpr_read_b32 v91, a107
v_accvgpr_read_b32 v92, a108
v_accvgpr_read_b32 v93, a109
v_accvgpr_read_b32 v94, a110
v_accvgpr_read_b32 v95, a111
v_mul_f32 v88, v88, v72
v_mul_f32 v89, v89, v73
v_mul_f32 v90, v90, v74
v_mul_f32 v91, v91, v75
v_mul_f32 v92, v92, v76
v_mul_f32 v93, v93, v77
v_mul_f32 v94, v94, v78
v_mul_f32 v95, v95, v79
v_accvgpr_write_b32 a104, v88
v_accvgpr_write_b32 a105, v89
v_accvgpr_write_b32 a106, v90
v_accvgpr_write_b32 a107, v91
v_accvgpr_write_b32 a108, v92
v_accvgpr_write_b32 a109, v93
v_accvgpr_write_b32 a110, v94
v_accvgpr_write_b32 a111, v95
v_accvgpr_read_b32 v88, a112
v_accvgpr_read_b32 v89, a113
v_accvgpr_read_b32 v90, a114
v_accvgpr_read_b32 v91, a115
v_accvgpr_read_b32 v92, a116
v_accvgpr_read_b32 v93, a117
v_accvgpr_read_b32 v94, a118
v_accvgpr_read_b32 v95, a119
v_mul_f32 v88, v88, v64
v_mul_f32 v89, v89, v65
v_mul_f32 v90, v90, v66
v_mul_f32 v91, v91, v67
v_mul_f32 v92, v92, v68
v_mul_f32 v93, v93, v69
v_mul_f32 v94, v94, v70
v_mul_f32 v95, v95, v71
v_accvgpr_write_b32 a112, v88
v_accvgpr_write_b32 a113, v89
v_accvgpr_write_b32 a114, v90
v_accvgpr_write_b32 a115, v91
v_accvgpr_write_b32 a116, v92
v_accvgpr_write_b32 a117, v93
v_accvgpr_write_b32 a118, v94
v_accvgpr_write_b32 a119, v95
v_accvgpr_read_b32 v88, a120
v_accvgpr_read_b32 v89, a121
v_accvgpr_read_b32 v90, a122
v_accvgpr_read_b32 v91, a123
v_accvgpr_read_b32 v92, a124
v_accvgpr_read_b32 v93, a125
v_accvgpr_read_b32 v94, a126
v_accvgpr_read_b32 v95, a127
v_mul_f32 v88, v88, v72
v_mul_f32 v89, v89, v73
v_mul_f32 v90, v90, v74
v_mul_f32 v91, v91, v75
v_mul_f32 v92, v92, v76
v_mul_f32 v93, v93, v77
v_mul_f32 v94, v94, v78
v_mul_f32 v95, v95, v79
v_accvgpr_write_b32 a120, v88
v_accvgpr_write_b32 a121, v89
v_accvgpr_write_b32 a122, v90
v_accvgpr_write_b32 a123, v91
v_accvgpr_write_b32 a124, v92
v_accvgpr_write_b32 a125, v93
v_accvgpr_write_b32 a126, v94
v_accvgpr_write_b32 a127, v95
v_accvgpr_read_b32 v88, a128
v_accvgpr_read_b32 v89, a129
v_accvgpr_read_b32 v90, a130
v_accvgpr_read_b32 v91, a131
v_accvgpr_read_b32 v92, a132
v_accvgpr_read_b32 v93, a133
v_accvgpr_read_b32 v94, a134
v_accvgpr_read_b32 v95, a135
v_mul_f32 v88, v88, v64
v_mul_f32 v89, v89, v65
v_mul_f32 v90, v90, v66
v_mul_f32 v91, v91, v67
v_mul_f32 v92, v92, v68
v_mul_f32 v93, v93, v69
v_mul_f32 v94, v94, v70
v_mul_f32 v95, v95, v71
v_accvgpr_write_b32 a128, v88
v_accvgpr_write_b32 a129, v89
v_accvgpr_write_b32 a130, v90
v_accvgpr_write_b32 a131, v91
v_accvgpr_write_b32 a132, v92
v_accvgpr_write_b32 a133, v93
v_accvgpr_write_b32 a134, v94
v_accvgpr_write_b32 a135, v95
v_accvgpr_read_b32 v88, a136
v_accvgpr_read_b32 v89, a137
v_accvgpr_read_b32 v90, a138
v_accvgpr_read_b32 v91, a139
v_accvgpr_read_b32 v92, a140
v_accvgpr_read_b32 v93, a141
v_accvgpr_read_b32 v94, a142
v_accvgpr_read_b32 v95, a143
v_mul_f32 v88, v88, v72
v_mul_f32 v89, v89, v73
v_mul_f32 v90, v90, v74
v_mul_f32 v91, v91, v75
v_mul_f32 v92, v92, v76
v_mul_f32 v93, v93, v77
v_mul_f32 v94, v94, v78
v_mul_f32 v95, v95, v79
v_accvgpr_write_b32 a136, v88
v_accvgpr_write_b32 a137, v89
v_accvgpr_write_b32 a138, v90
v_accvgpr_write_b32 a139, v91
v_accvgpr_write_b32 a140, v92
v_accvgpr_write_b32 a141, v93
v_accvgpr_write_b32 a142, v94
v_accvgpr_write_b32 a143, v95
v_accvgpr_read_b32 v88, a144
v_accvgpr_read_b32 v89, a145
v_accvgpr_read_b32 v90, a146
v_accvgpr_read_b32 v91, a147
v_accvgpr_read_b32 v92, a148
v_accvgpr_read_b32 v93, a149
v_accvgpr_read_b32 v94, a150
v_accvgpr_read_b32 v95, a151
v_mul_f32 v88, v88, v64
v_mul_f32 v89, v89, v65
v_mul_f32 v90, v90, v66
v_mul_f32 v91, v91, v67
v_mul_f32 v92, v92, v68
v_mul_f32 v93, v93, v69
v_mul_f32 v94, v94, v70
v_mul_f32 v95, v95, v71
v_accvgpr_write_b32 a144, v88
v_accvgpr_write_b32 a145, v89
v_accvgpr_write_b32 a146, v90
v_accvgpr_write_b32 a147, v91
v_accvgpr_write_b32 a148, v92
v_accvgpr_write_b32 a149, v93
v_accvgpr_write_b32 a150, v94
v_accvgpr_write_b32 a151, v95
v_accvgpr_read_b32 v88, a152
v_accvgpr_read_b32 v89, a153
v_accvgpr_read_b32 v90, a154
v_accvgpr_read_b32 v91, a155
v_accvgpr_read_b32 v92, a156
v_accvgpr_read_b32 v93, a157
v_accvgpr_read_b32 v94, a158
v_accvgpr_read_b32 v95, a159
v_mul_f32 v88, v88, v72
v_mul_f32 v89, v89, v73
v_mul_f32 v90, v90, v74
v_mul_f32 v91, v91, v75
v_mul_f32 v92, v92, v76
v_mul_f32 v93, v93, v77
v_mul_f32 v94, v94, v78
v_mul_f32 v95, v95, v79
v_accvgpr_write_b32 a152, v88
v_accvgpr_write_b32 a153, v89
v_accvgpr_write_b32 a154, v90
v_accvgpr_write_b32 a155, v91
v_accvgpr_write_b32 a156, v92
v_accvgpr_write_b32 a157, v93
v_accvgpr_write_b32 a158, v94
v_accvgpr_write_b32 a159, v95
v_accvgpr_read_b32 v88, a160
v_accvgpr_read_b32 v89, a161
v_accvgpr_read_b32 v90, a162
v_accvgpr_read_b32 v91, a163
v_accvgpr_read_b32 v92, a164
v_accvgpr_read_b32 v93, a165
v_accvgpr_read_b32 v94, a166
v_accvgpr_read_b32 v95, a167
v_mul_f32 v88, v88, v64
v_mul_f32 v89, v89, v65
v_mul_f32 v90, v90, v66
v_mul_f32 v91, v91, v67
v_mul_f32 v92, v92, v68
v_mul_f32 v93, v93, v69
v_mul_f32 v94, v94, v70
v_mul_f32 v95, v95, v71
v_accvgpr_write_b32 a160, v88
v_accvgpr_write_b32 a161, v89
v_accvgpr_write_b32 a162, v90
v_accvgpr_write_b32 a163, v91
v_accvgpr_write_b32 a164, v92
v_accvgpr_write_b32 a165, v93
v_accvgpr_write_b32 a166, v94
v_accvgpr_write_b32 a167, v95
v_accvgpr_read_b32 v88, a168
v_accvgpr_read_b32 v89, a169
v_accvgpr_read_b32 v90, a170
v_accvgpr_read_b32 v91, a171
v_accvgpr_read_b32 v92, a172
v_accvgpr_read_b32 v93, a173
v_accvgpr_read_b32 v94, a174
v_accvgpr_read_b32 v95, a175
v_mul_f32 v88, v88, v72
v_mul_f32 v89, v89, v73
v_mul_f32 v90, v90, v74
v_mul_f32 v91, v91, v75
v_mul_f32 v92, v92, v76
v_mul_f32 v93, v93, v77
v_mul_f32 v94, v94, v78
v_mul_f32 v95, v95, v79
v_accvgpr_write_b32 a168, v88
v_accvgpr_write_b32 a169, v89
v_accvgpr_write_b32 a170, v90
v_accvgpr_write_b32 a171, v91
v_accvgpr_write_b32 a172, v92
v_accvgpr_write_b32 a173, v93
v_accvgpr_write_b32 a174, v94
v_accvgpr_write_b32 a175, v95
v_accvgpr_read_b32 v88, a176
v_accvgpr_read_b32 v89, a177
v_accvgpr_read_b32 v90, a178
v_accvgpr_read_b32 v91, a179
v_accvgpr_read_b32 v92, a180
v_accvgpr_read_b32 v93, a181
v_accvgpr_read_b32 v94, a182
v_accvgpr_read_b32 v95, a183
v_mul_f32 v88, v88, v64
v_mul_f32 v89, v89, v65
v_mul_f32 v90, v90, v66
v_mul_f32 v91, v91, v67
v_mul_f32 v92, v92, v68
v_mul_f32 v93, v93, v69
v_mul_f32 v94, v94, v70
v_mul_f32 v95, v95, v71
v_accvgpr_write_b32 a176, v88
v_accvgpr_write_b32 a177, v89
v_accvgpr_write_b32 a178, v90
v_accvgpr_write_b32 a179, v91
v_accvgpr_write_b32 a180, v92
v_accvgpr_write_b32 a181, v93
v_accvgpr_write_b32 a182, v94
v_accvgpr_write_b32 a183, v95
v_accvgpr_read_b32 v88, a184
v_accvgpr_read_b32 v89, a185
v_accvgpr_read_b32 v90, a186
v_accvgpr_read_b32 v91, a187
v_accvgpr_read_b32 v92, a188
v_accvgpr_read_b32 v93, a189
v_accvgpr_read_b32 v94, a190
v_accvgpr_read_b32 v95, a191
v_mul_f32 v88, v88, v72
v_mul_f32 v89, v89, v73
v_mul_f32 v90, v90, v74
v_mul_f32 v91, v91, v75
v_mul_f32 v92, v92, v76
v_mul_f32 v93, v93, v77
v_mul_f32 v94, v94, v78
v_mul_f32 v95, v95, v79
v_accvgpr_write_b32 a184, v88
v_accvgpr_write_b32 a185, v89
v_accvgpr_write_b32 a186, v90
v_accvgpr_write_b32 a187, v91
v_accvgpr_write_b32 a188, v92
v_accvgpr_write_b32 a189, v93
v_accvgpr_write_b32 a190, v94
v_accvgpr_write_b32 a191, v95
v_accvgpr_read_b32 v88, a192
v_accvgpr_read_b32 v89, a193
v_accvgpr_read_b32 v90, a194
v_accvgpr_read_b32 v91, a195
v_accvgpr_read_b32 v92, a196
v_accvgpr_read_b32 v93, a197
v_accvgpr_read_b32 v94, a198
v_accvgpr_read_b32 v95, a199
v_mul_f32 v88, v88, v64
v_mul_f32 v89, v89, v65
v_mul_f32 v90, v90, v66
v_mul_f32 v91, v91, v67
v_mul_f32 v92, v92, v68
v_mul_f32 v93, v93, v69
v_mul_f32 v94, v94, v70
v_mul_f32 v95, v95, v71
v_accvgpr_write_b32 a192, v88
v_accvgpr_write_b32 a193, v89
v_accvgpr_write_b32 a194, v90
v_accvgpr_write_b32 a195, v91
v_accvgpr_write_b32 a196, v92
v_accvgpr_write_b32 a197, v93
v_accvgpr_write_b32 a198, v94
v_accvgpr_write_b32 a199, v95
v_accvgpr_read_b32 v88, a200
v_accvgpr_read_b32 v89, a201
v_accvgpr_read_b32 v90, a202
v_accvgpr_read_b32 v91, a203
v_accvgpr_read_b32 v92, a204
v_accvgpr_read_b32 v93, a205
v_accvgpr_read_b32 v94, a206
v_accvgpr_read_b32 v95, a207
v_mul_f32 v88, v88, v72
v_mul_f32 v89, v89, v73
v_mul_f32 v90, v90, v74
v_mul_f32 v91, v91, v75
v_mul_f32 v92, v92, v76
v_mul_f32 v93, v93, v77
v_mul_f32 v94, v94, v78
v_mul_f32 v95, v95, v79
v_accvgpr_write_b32 a200, v88
v_accvgpr_write_b32 a201, v89
v_accvgpr_write_b32 a202, v90
v_accvgpr_write_b32 a203, v91
v_accvgpr_write_b32 a204, v92
v_accvgpr_write_b32 a205, v93
v_accvgpr_write_b32 a206, v94
v_accvgpr_write_b32 a207, v95
v_accvgpr_read_b32 v88, a208
v_accvgpr_read_b32 v89, a209
v_accvgpr_read_b32 v90, a210
v_accvgpr_read_b32 v91, a211
v_accvgpr_read_b32 v92, a212
v_accvgpr_read_b32 v93, a213
v_accvgpr_read_b32 v94, a214
v_accvgpr_read_b32 v95, a215
v_mul_f32 v88, v88, v64
v_mul_f32 v89, v89, v65
v_mul_f32 v90, v90, v66
v_mul_f32 v91, v91, v67
v_mul_f32 v92, v92, v68
v_mul_f32 v93, v93, v69
v_mul_f32 v94, v94, v70
v_mul_f32 v95, v95, v71
v_accvgpr_write_b32 a208, v88
v_accvgpr_write_b32 a209, v89
v_accvgpr_write_b32 a210, v90
v_accvgpr_write_b32 a211, v91
v_accvgpr_write_b32 a212, v92
v_accvgpr_write_b32 a213, v93
v_accvgpr_write_b32 a214, v94
v_accvgpr_write_b32 a215, v95
v_accvgpr_read_b32 v88, a216
v_accvgpr_read_b32 v89, a217
v_accvgpr_read_b32 v90, a218
v_accvgpr_read_b32 v91, a219
v_accvgpr_read_b32 v92, a220
v_accvgpr_read_b32 v93, a221
v_accvgpr_read_b32 v94, a222
v_accvgpr_read_b32 v95, a223
v_mul_f32 v88, v88, v72
v_mul_f32 v89, v89, v73
v_mul_f32 v90, v90, v74
v_mul_f32 v91, v91, v75
v_mul_f32 v92, v92, v76
v_mul_f32 v93, v93, v77
v_mul_f32 v94, v94, v78
v_mul_f32 v95, v95, v79
v_accvgpr_write_b32 a216, v88
v_accvgpr_write_b32 a217, v89
v_accvgpr_write_b32 a218, v90
v_accvgpr_write_b32 a219, v91
v_accvgpr_write_b32 a220, v92
v_accvgpr_write_b32 a221, v93
v_accvgpr_write_b32 a222, v94
v_accvgpr_write_b32 a223, v95
v_accvgpr_read_b32 v88, a224
v_accvgpr_read_b32 v89, a225
v_accvgpr_read_b32 v90, a226
v_accvgpr_read_b32 v91, a227
v_accvgpr_read_b32 v92, a228
v_accvgpr_read_b32 v93, a229
v_accvgpr_read_b32 v94, a230
v_accvgpr_read_b32 v95, a231
v_mul_f32 v88, v88, v64
v_mul_f32 v89, v89, v65
v_mul_f32 v90, v90, v66
v_mul_f32 v91, v91, v67
v_mul_f32 v92, v92, v68
v_mul_f32 v93, v93, v69
v_mul_f32 v94, v94, v70
v_mul_f32 v95, v95, v71
v_accvgpr_write_b32 a224, v88
v_accvgpr_write_b32 a225, v89
v_accvgpr_write_b32 a226, v90
v_accvgpr_write_b32 a227, v91
v_accvgpr_write_b32 a228, v92
v_accvgpr_write_b32 a229, v93
v_accvgpr_write_b32 a230, v94
v_accvgpr_write_b32 a231, v95
v_accvgpr_read_b32 v88, a232
v_accvgpr_read_b32 v89, a233
v_accvgpr_read_b32 v90, a234
v_accvgpr_read_b32 v91, a235
v_accvgpr_read_b32 v92, a236
v_accvgpr_read_b32 v93, a237
v_accvgpr_read_b32 v94, a238
v_accvgpr_read_b32 v95, a239
v_mul_f32 v88, v88, v72
v_mul_f32 v89, v89, v73
v_mul_f32 v90, v90, v74
v_mul_f32 v91, v91, v75
v_mul_f32 v92, v92, v76
v_mul_f32 v93, v93, v77
v_mul_f32 v94, v94, v78
v_mul_f32 v95, v95, v79
v_accvgpr_write_b32 a232, v88
v_accvgpr_write_b32 a233, v89
v_accvgpr_write_b32 a234, v90
v_accvgpr_write_b32 a235, v91
v_accvgpr_write_b32 a236, v92
v_accvgpr_write_b32 a237, v93
v_accvgpr_write_b32 a238, v94
v_accvgpr_write_b32 a239, v95
v_accvgpr_read_b32 v88, a240
v_accvgpr_read_b32 v89, a241
v_accvgpr_read_b32 v90, a242
v_accvgpr_read_b32 v91, a243
v_accvgpr_read_b32 v92, a244
v_accvgpr_read_b32 v93, a245
v_accvgpr_read_b32 v94, a246
v_accvgpr_read_b32 v95, a247
v_mul_f32 v88, v88, v64
v_mul_f32 v89, v89, v65
v_mul_f32 v90, v90, v66
v_mul_f32 v91, v91, v67
v_mul_f32 v92, v92, v68
v_mul_f32 v93, v93, v69
v_mul_f32 v94, v94, v70
v_mul_f32 v95, v95, v71
v_accvgpr_write_b32 a240, v88
v_accvgpr_write_b32 a241, v89
v_accvgpr_write_b32 a242, v90
v_accvgpr_write_b32 a243, v91
v_accvgpr_write_b32 a244, v92
v_accvgpr_write_b32 a245, v93
v_accvgpr_write_b32 a246, v94
v_accvgpr_write_b32 a247, v95
v_accvgpr_read_b32 v88, a248
v_accvgpr_read_b32 v89, a249
v_accvgpr_read_b32 v90, a250
v_accvgpr_read_b32 v91, a251
v_accvgpr_read_b32 v92, a252
v_accvgpr_read_b32 v93, a253
v_accvgpr_read_b32 v94, a254
v_accvgpr_read_b32 v95, a255
v_mul_f32 v88, v88, v72
v_mul_f32 v89, v89, v73
v_mul_f32 v90, v90, v74
v_mul_f32 v91, v91, v75
v_mul_f32 v92, v92, v76
v_mul_f32 v93, v93, v77
v_mul_f32 v94, v94, v78
v_mul_f32 v95, v95, v79
v_accvgpr_write_b32 a248, v88
v_accvgpr_write_b32 a249, v89
v_accvgpr_write_b32 a250, v90
v_accvgpr_write_b32 a251, v91
v_accvgpr_write_b32 a252, v92
v_accvgpr_write_b32 a253, v93
v_accvgpr_write_b32 a254, v94
v_accvgpr_write_b32 a255, v95
s_nop 1
s_setpc_b64 s[50:51]
L_resc1%=:
s_nop 15
v_mbcnt_lo_u32_b32 v15, -1, 0
v_mbcnt_hi_u32_b32 v15, -1, v15
v_and_b32 v15, 48, v15
ds_bpermute_b32 v32, v15, v52 offset:0
ds_bpermute_b32 v33, v15, v52 offset:4
ds_bpermute_b32 v34, v15, v52 offset:8
ds_bpermute_b32 v35, v15, v52 offset:12
ds_bpermute_b32 v36, v15, v53 offset:0
ds_bpermute_b32 v37, v15, v53 offset:4
ds_bpermute_b32 v38, v15, v53 offset:8
ds_bpermute_b32 v39, v15, v53 offset:12
ds_bpermute_b32 v40, v15, v54 offset:0
ds_bpermute_b32 v41, v15, v54 offset:4
ds_bpermute_b32 v42, v15, v54 offset:8
ds_bpermute_b32 v43, v15, v54 offset:12
ds_bpermute_b32 v44, v15, v55 offset:0
ds_bpermute_b32 v45, v15, v55 offset:4
ds_bpermute_b32 v46, v15, v55 offset:8
ds_bpermute_b32 v47, v15, v55 offset:12
s_waitcnt lgkmcnt(0)
v_accvgpr_read_b32 v56, a0
v_accvgpr_read_b32 v57, a1
v_accvgpr_read_b32 v58, a2
v_accvgpr_read_b32 v59, a3
v_accvgpr_read_b32 v60, a4
v_accvgpr_read_b32 v61, a5
v_accvgpr_read_b32 v62, a6
v_accvgpr_read_b32 v63, a7
v_mul_f32 v56, v56, v32
v_mul_f32 v57, v57, v33
v_mul_f32 v58, v58, v34
v_mul_f32 v59, v59, v35
v_mul_f32 v60, v60, v36
v_mul_f32 v61, v61, v37
v_mul_f32 v62, v62, v38
v_mul_f32 v63, v63, v39
v_accvgpr_write_b32 a0, v56
v_accvgpr_write_b32 a1, v57
v_accvgpr_write_b32 a2, v58
v_accvgpr_write_b32 a3, v59
v_accvgpr_write_b32 a4, v60
v_accvgpr_write_b32 a5, v61
v_accvgpr_write_b32 a6, v62
v_accvgpr_write_b32 a7, v63
v_accvgpr_read_b32 v56, a8
v_accvgpr_read_b32 v57, a9
v_accvgpr_read_b32 v58, a10
v_accvgpr_read_b32 v59, a11
v_accvgpr_read_b32 v60, a12
v_accvgpr_read_b32 v61, a13
v_accvgpr_read_b32 v62, a14
v_accvgpr_read_b32 v63, a15
v_mul_f32 v56, v56, v40
v_mul_f32 v57, v57, v41
v_mul_f32 v58, v58, v42
v_mul_f32 v59, v59, v43
v_mul_f32 v60, v60, v44
v_mul_f32 v61, v61, v45
v_mul_f32 v62, v62, v46
v_mul_f32 v63, v63, v47
v_accvgpr_write_b32 a8, v56
v_accvgpr_write_b32 a9, v57
v_accvgpr_write_b32 a10, v58
v_accvgpr_write_b32 a11, v59
v_accvgpr_write_b32 a12, v60
v_accvgpr_write_b32 a13, v61
v_accvgpr_write_b32 a14, v62
v_accvgpr_write_b32 a15, v63
v_accvgpr_read_b32 v56, a16
v_accvgpr_read_b32 v57, a17
v_accvgpr_read_b32 v58, a18
v_accvgpr_read_b32 v59, a19
v_accvgpr_read_b32 v60, a20
v_accvgpr_read_b32 v61, a21
v_accvgpr_read_b32 v62, a22
v_accvgpr_read_b32 v63, a23
v_mul_f32 v56, v56, v32
v_mul_f32 v57, v57, v33
v_mul_f32 v58, v58, v34
v_mul_f32 v59, v59, v35
v_mul_f32 v60, v60, v36
v_mul_f32 v61, v61, v37
v_mul_f32 v62, v62, v38
v_mul_f32 v63, v63, v39
v_accvgpr_write_b32 a16, v56
v_accvgpr_write_b32 a17, v57
v_accvgpr_write_b32 a18, v58
v_accvgpr_write_b32 a19, v59
v_accvgpr_write_b32 a20, v60
v_accvgpr_write_b32 a21, v61
v_accvgpr_write_b32 a22, v62
v_accvgpr_write_b32 a23, v63
v_accvgpr_read_b32 v56, a24
v_accvgpr_read_b32 v57, a25
v_accvgpr_read_b32 v58, a26
v_accvgpr_read_b32 v59, a27
v_accvgpr_read_b32 v60, a28
v_accvgpr_read_b32 v61, a29
v_accvgpr_read_b32 v62, a30
v_accvgpr_read_b32 v63, a31
v_mul_f32 v56, v56, v40
v_mul_f32 v57, v57, v41
v_mul_f32 v58, v58, v42
v_mul_f32 v59, v59, v43
v_mul_f32 v60, v60, v44
v_mul_f32 v61, v61, v45
v_mul_f32 v62, v62, v46
v_mul_f32 v63, v63, v47
v_accvgpr_write_b32 a24, v56
v_accvgpr_write_b32 a25, v57
v_accvgpr_write_b32 a26, v58
v_accvgpr_write_b32 a27, v59
v_accvgpr_write_b32 a28, v60
v_accvgpr_write_b32 a29, v61
v_accvgpr_write_b32 a30, v62
v_accvgpr_write_b32 a31, v63
v_accvgpr_read_b32 v56, a32
v_accvgpr_read_b32 v57, a33
v_accvgpr_read_b32 v58, a34
v_accvgpr_read_b32 v59, a35
v_accvgpr_read_b32 v60, a36
v_accvgpr_read_b32 v61, a37
v_accvgpr_read_b32 v62, a38
v_accvgpr_read_b32 v63, a39
v_mul_f32 v56, v56, v32
v_mul_f32 v57, v57, v33
v_mul_f32 v58, v58, v34
v_mul_f32 v59, v59, v35
v_mul_f32 v60, v60, v36
v_mul_f32 v61, v61, v37
v_mul_f32 v62, v62, v38
v_mul_f32 v63, v63, v39
v_accvgpr_write_b32 a32, v56
v_accvgpr_write_b32 a33, v57
v_accvgpr_write_b32 a34, v58
v_accvgpr_write_b32 a35, v59
v_accvgpr_write_b32 a36, v60
v_accvgpr_write_b32 a37, v61
v_accvgpr_write_b32 a38, v62
v_accvgpr_write_b32 a39, v63
v_accvgpr_read_b32 v56, a40
v_accvgpr_read_b32 v57, a41
v_accvgpr_read_b32 v58, a42
v_accvgpr_read_b32 v59, a43
v_accvgpr_read_b32 v60, a44
v_accvgpr_read_b32 v61, a45
v_accvgpr_read_b32 v62, a46
v_accvgpr_read_b32 v63, a47
v_mul_f32 v56, v56, v40
v_mul_f32 v57, v57, v41
v_mul_f32 v58, v58, v42
v_mul_f32 v59, v59, v43
v_mul_f32 v60, v60, v44
v_mul_f32 v61, v61, v45
v_mul_f32 v62, v62, v46
v_mul_f32 v63, v63, v47
v_accvgpr_write_b32 a40, v56
v_accvgpr_write_b32 a41, v57
v_accvgpr_write_b32 a42, v58
v_accvgpr_write_b32 a43, v59
v_accvgpr_write_b32 a44, v60
v_accvgpr_write_b32 a45, v61
v_accvgpr_write_b32 a46, v62
v_accvgpr_write_b32 a47, v63
v_accvgpr_read_b32 v56, a48
v_accvgpr_read_b32 v57, a49
v_accvgpr_read_b32 v58, a50
v_accvgpr_read_b32 v59, a51
v_accvgpr_read_b32 v60, a52
v_accvgpr_read_b32 v61, a53
v_accvgpr_read_b32 v62, a54
v_accvgpr_read_b32 v63, a55
v_mul_f32 v56, v56, v32
v_mul_f32 v57, v57, v33
v_mul_f32 v58, v58, v34
v_mul_f32 v59, v59, v35
v_mul_f32 v60, v60, v36
v_mul_f32 v61, v61, v37
v_mul_f32 v62, v62, v38
v_mul_f32 v63, v63, v39
v_accvgpr_write_b32 a48, v56
v_accvgpr_write_b32 a49, v57
v_accvgpr_write_b32 a50, v58
v_accvgpr_write_b32 a51, v59
v_accvgpr_write_b32 a52, v60
v_accvgpr_write_b32 a53, v61
v_accvgpr_write_b32 a54, v62
v_accvgpr_write_b32 a55, v63
v_accvgpr_read_b32 v56, a56
v_accvgpr_read_b32 v57, a57
v_accvgpr_read_b32 v58, a58
v_accvgpr_read_b32 v59, a59
v_accvgpr_read_b32 v60, a60
v_accvgpr_read_b32 v61, a61
v_accvgpr_read_b32 v62, a62
v_accvgpr_read_b32 v63, a63
v_mul_f32 v56, v56, v40
v_mul_f32 v57, v57, v41
v_mul_f32 v58, v58, v42
v_mul_f32 v59, v59, v43
v_mul_f32 v60, v60, v44
v_mul_f32 v61, v61, v45
v_mul_f32 v62, v62, v46
v_mul_f32 v63, v63, v47
v_accvgpr_write_b32 a56, v56
v_accvgpr_write_b32 a57, v57
v_accvgpr_write_b32 a58, v58
v_accvgpr_write_b32 a59, v59
v_accvgpr_write_b32 a60, v60
v_accvgpr_write_b32 a61, v61
v_accvgpr_write_b32 a62, v62
v_accvgpr_write_b32 a63, v63
v_accvgpr_read_b32 v56, a64
v_accvgpr_read_b32 v57, a65
v_accvgpr_read_b32 v58, a66
v_accvgpr_read_b32 v59, a67
v_accvgpr_read_b32 v60, a68
v_accvgpr_read_b32 v61, a69
v_accvgpr_read_b32 v62, a70
v_accvgpr_read_b32 v63, a71
v_mul_f32 v56, v56, v32
v_mul_f32 v57, v57, v33
v_mul_f32 v58, v58, v34
v_mul_f32 v59, v59, v35
v_mul_f32 v60, v60, v36
v_mul_f32 v61, v61, v37
v_mul_f32 v62, v62, v38
v_mul_f32 v63, v63, v39
v_accvgpr_write_b32 a64, v56
v_accvgpr_write_b32 a65, v57
v_accvgpr_write_b32 a66, v58
v_accvgpr_write_b32 a67, v59
v_accvgpr_write_b32 a68, v60
v_accvgpr_write_b32 a69, v61
v_accvgpr_write_b32 a70, v62
v_accvgpr_write_b32 a71, v63
v_accvgpr_read_b32 v56, a72
v_accvgpr_read_b32 v57, a73
v_accvgpr_read_b32 v58, a74
v_accvgpr_read_b32 v59, a75
v_accvgpr_read_b32 v60, a76
v_accvgpr_read_b32 v61, a77
v_accvgpr_read_b32 v62, a78
v_accvgpr_read_b32 v63, a79
v_mul_f32 v56, v56, v40
v_mul_f32 v57, v57, v41
v_mul_f32 v58, v58, v42
v_mul_f32 v59, v59, v43
v_mul_f32 v60, v60, v44
v_mul_f32 v61, v61, v45
v_mul_f32 v62, v62, v46
v_mul_f32 v63, v63, v47
v_accvgpr_write_b32 a72, v56
v_accvgpr_write_b32 a73, v57
v_accvgpr_write_b32 a74, v58
v_accvgpr_write_b32 a75, v59
v_accvgpr_write_b32 a76, v60
v_accvgpr_write_b32 a77, v61
v_accvgpr_write_b32 a78, v62
v_accvgpr_write_b32 a79, v63
v_accvgpr_read_b32 v56, a80
v_accvgpr_read_b32 v57, a81
v_accvgpr_read_b32 v58, a82
v_accvgpr_read_b32 v59, a83
v_accvgpr_read_b32 v60, a84
v_accvgpr_read_b32 v61, a85
v_accvgpr_read_b32 v62, a86
v_accvgpr_read_b32 v63, a87
v_mul_f32 v56, v56, v32
v_mul_f32 v57, v57, v33
v_mul_f32 v58, v58, v34
v_mul_f32 v59, v59, v35
v_mul_f32 v60, v60, v36
v_mul_f32 v61, v61, v37
v_mul_f32 v62, v62, v38
v_mul_f32 v63, v63, v39
v_accvgpr_write_b32 a80, v56
v_accvgpr_write_b32 a81, v57
v_accvgpr_write_b32 a82, v58
v_accvgpr_write_b32 a83, v59
v_accvgpr_write_b32 a84, v60
v_accvgpr_write_b32 a85, v61
v_accvgpr_write_b32 a86, v62
v_accvgpr_write_b32 a87, v63
v_accvgpr_read_b32 v56, a88
v_accvgpr_read_b32 v57, a89
v_accvgpr_read_b32 v58, a90
v_accvgpr_read_b32 v59, a91
v_accvgpr_read_b32 v60, a92
v_accvgpr_read_b32 v61, a93
v_accvgpr_read_b32 v62, a94
v_accvgpr_read_b32 v63, a95
v_mul_f32 v56, v56, v40
v_mul_f32 v57, v57, v41
v_mul_f32 v58, v58, v42
v_mul_f32 v59, v59, v43
v_mul_f32 v60, v60, v44
v_mul_f32 v61, v61, v45
v_mul_f32 v62, v62, v46
v_mul_f32 v63, v63, v47
v_accvgpr_write_b32 a88, v56
v_accvgpr_write_b32 a89, v57
v_accvgpr_write_b32 a90, v58
v_accvgpr_write_b32 a91, v59
v_accvgpr_write_b32 a92, v60
v_accvgpr_write_b32 a93, v61
v_accvgpr_write_b32 a94, v62
v_accvgpr_write_b32 a95, v63
v_accvgpr_read_b32 v56, a96
v_accvgpr_read_b32 v57, a97
v_accvgpr_read_b32 v58, a98
v_accvgpr_read_b32 v59, a99
v_accvgpr_read_b32 v60, a100
v_accvgpr_read_b32 v61, a101
v_accvgpr_read_b32 v62, a102
v_accvgpr_read_b32 v63, a103
v_mul_f32 v56, v56, v32
v_mul_f32 v57, v57, v33
v_mul_f32 v58, v58, v34
v_mul_f32 v59, v59, v35
v_mul_f32 v60, v60, v36
v_mul_f32 v61, v61, v37
v_mul_f32 v62, v62, v38
v_mul_f32 v63, v63, v39
v_accvgpr_write_b32 a96, v56
v_accvgpr_write_b32 a97, v57
v_accvgpr_write_b32 a98, v58
v_accvgpr_write_b32 a99, v59
v_accvgpr_write_b32 a100, v60
v_accvgpr_write_b32 a101, v61
v_accvgpr_write_b32 a102, v62
v_accvgpr_write_b32 a103, v63
v_accvgpr_read_b32 v56, a104
v_accvgpr_read_b32 v57, a105
v_accvgpr_read_b32 v58, a106
v_accvgpr_read_b32 v59, a107
v_accvgpr_read_b32 v60, a108
v_accvgpr_read_b32 v61, a109
v_accvgpr_read_b32 v62, a110
v_accvgpr_read_b32 v63, a111
v_mul_f32 v56, v56, v40
v_mul_f32 v57, v57, v41
v_mul_f32 v58, v58, v42
v_mul_f32 v59, v59, v43
v_mul_f32 v60, v60, v44
v_mul_f32 v61, v61, v45
v_mul_f32 v62, v62, v46
v_mul_f32 v63, v63, v47
v_accvgpr_write_b32 a104, v56
v_accvgpr_write_b32 a105, v57
v_accvgpr_write_b32 a106, v58
v_accvgpr_write_b32 a107, v59
v_accvgpr_write_b32 a108, v60
v_accvgpr_write_b32 a109, v61
v_accvgpr_write_b32 a110, v62
v_accvgpr_write_b32 a111, v63
v_accvgpr_read_b32 v56, a112
v_accvgpr_read_b32 v57, a113
v_accvgpr_read_b32 v58, a114
v_accvgpr_read_b32 v59, a115
v_accvgpr_read_b32 v60, a116
v_accvgpr_read_b32 v61, a117
v_accvgpr_read_b32 v62, a118
v_accvgpr_read_b32 v63, a119
v_mul_f32 v56, v56, v32
v_mul_f32 v57, v57, v33
v_mul_f32 v58, v58, v34
v_mul_f32 v59, v59, v35
v_mul_f32 v60, v60, v36
v_mul_f32 v61, v61, v37
v_mul_f32 v62, v62, v38
v_mul_f32 v63, v63, v39
v_accvgpr_write_b32 a112, v56
v_accvgpr_write_b32 a113, v57
v_accvgpr_write_b32 a114, v58
v_accvgpr_write_b32 a115, v59
v_accvgpr_write_b32 a116, v60
v_accvgpr_write_b32 a117, v61
v_accvgpr_write_b32 a118, v62
v_accvgpr_write_b32 a119, v63
v_accvgpr_read_b32 v56, a120
v_accvgpr_read_b32 v57, a121
v_accvgpr_read_b32 v58, a122
v_accvgpr_read_b32 v59, a123
v_accvgpr_read_b32 v60, a124
v_accvgpr_read_b32 v61, a125
v_accvgpr_read_b32 v62, a126
v_accvgpr_read_b32 v63, a127
v_mul_f32 v56, v56, v40
v_mul_f32 v57, v57, v41
v_mul_f32 v58, v58, v42
v_mul_f32 v59, v59, v43
v_mul_f32 v60, v60, v44
v_mul_f32 v61, v61, v45
v_mul_f32 v62, v62, v46
v_mul_f32 v63, v63, v47
v_accvgpr_write_b32 a120, v56
v_accvgpr_write_b32 a121, v57
v_accvgpr_write_b32 a122, v58
v_accvgpr_write_b32 a123, v59
v_accvgpr_write_b32 a124, v60
v_accvgpr_write_b32 a125, v61
v_accvgpr_write_b32 a126, v62
v_accvgpr_write_b32 a127, v63
v_accvgpr_read_b32 v56, a128
v_accvgpr_read_b32 v57, a129
v_accvgpr_read_b32 v58, a130
v_accvgpr_read_b32 v59, a131
v_accvgpr_read_b32 v60, a132
v_accvgpr_read_b32 v61, a133
v_accvgpr_read_b32 v62, a134
v_accvgpr_read_b32 v63, a135
v_mul_f32 v56, v56, v32
v_mul_f32 v57, v57, v33
v_mul_f32 v58, v58, v34
v_mul_f32 v59, v59, v35
v_mul_f32 v60, v60, v36
v_mul_f32 v61, v61, v37
v_mul_f32 v62, v62, v38
v_mul_f32 v63, v63, v39
v_accvgpr_write_b32 a128, v56
v_accvgpr_write_b32 a129, v57
v_accvgpr_write_b32 a130, v58
v_accvgpr_write_b32 a131, v59
v_accvgpr_write_b32 a132, v60
v_accvgpr_write_b32 a133, v61
v_accvgpr_write_b32 a134, v62
v_accvgpr_write_b32 a135, v63
v_accvgpr_read_b32 v56, a136
v_accvgpr_read_b32 v57, a137
v_accvgpr_read_b32 v58, a138
v_accvgpr_read_b32 v59, a139
v_accvgpr_read_b32 v60, a140
v_accvgpr_read_b32 v61, a141
v_accvgpr_read_b32 v62, a142
v_accvgpr_read_b32 v63, a143
v_mul_f32 v56, v56, v40
v_mul_f32 v57, v57, v41
v_mul_f32 v58, v58, v42
v_mul_f32 v59, v59, v43
v_mul_f32 v60, v60, v44
v_mul_f32 v61, v61, v45
v_mul_f32 v62, v62, v46
v_mul_f32 v63, v63, v47
v_accvgpr_write_b32 a136, v56
v_accvgpr_write_b32 a137, v57
v_accvgpr_write_b32 a138, v58
v_accvgpr_write_b32 a139, v59
v_accvgpr_write_b32 a140, v60
v_accvgpr_write_b32 a141, v61
v_accvgpr_write_b32 a142, v62
v_accvgpr_write_b32 a143, v63
v_accvgpr_read_b32 v56, a144
v_accvgpr_read_b32 v57, a145
v_accvgpr_read_b32 v58, a146
v_accvgpr_read_b32 v59, a147
v_accvgpr_read_b32 v60, a148
v_accvgpr_read_b32 v61, a149
v_accvgpr_read_b32 v62, a150
v_accvgpr_read_b32 v63, a151
v_mul_f32 v56, v56, v32
v_mul_f32 v57, v57, v33
v_mul_f32 v58, v58, v34
v_mul_f32 v59, v59, v35
v_mul_f32 v60, v60, v36
v_mul_f32 v61, v61, v37
v_mul_f32 v62, v62, v38
v_mul_f32 v63, v63, v39
v_accvgpr_write_b32 a144, v56
v_accvgpr_write_b32 a145, v57
v_accvgpr_write_b32 a146, v58
v_accvgpr_write_b32 a147, v59
v_accvgpr_write_b32 a148, v60
v_accvgpr_write_b32 a149, v61
v_accvgpr_write_b32 a150, v62
v_accvgpr_write_b32 a151, v63
v_accvgpr_read_b32 v56, a152
v_accvgpr_read_b32 v57, a153
v_accvgpr_read_b32 v58, a154
v_accvgpr_read_b32 v59, a155
v_accvgpr_read_b32 v60, a156
v_accvgpr_read_b32 v61, a157
v_accvgpr_read_b32 v62, a158
v_accvgpr_read_b32 v63, a159
v_mul_f32 v56, v56, v40
v_mul_f32 v57, v57, v41
v_mul_f32 v58, v58, v42
v_mul_f32 v59, v59, v43
v_mul_f32 v60, v60, v44
v_mul_f32 v61, v61, v45
v_mul_f32 v62, v62, v46
v_mul_f32 v63, v63, v47
v_accvgpr_write_b32 a152, v56
v_accvgpr_write_b32 a153, v57
v_accvgpr_write_b32 a154, v58
v_accvgpr_write_b32 a155, v59
v_accvgpr_write_b32 a156, v60
v_accvgpr_write_b32 a157, v61
v_accvgpr_write_b32 a158, v62
v_accvgpr_write_b32 a159, v63
v_accvgpr_read_b32 v56, a160
v_accvgpr_read_b32 v57, a161
v_accvgpr_read_b32 v58, a162
v_accvgpr_read_b32 v59, a163
v_accvgpr_read_b32 v60, a164
v_accvgpr_read_b32 v61, a165
v_accvgpr_read_b32 v62, a166
v_accvgpr_read_b32 v63, a167
v_mul_f32 v56, v56, v32
v_mul_f32 v57, v57, v33
v_mul_f32 v58, v58, v34
v_mul_f32 v59, v59, v35
v_mul_f32 v60, v60, v36
v_mul_f32 v61, v61, v37
v_mul_f32 v62, v62, v38
v_mul_f32 v63, v63, v39
v_accvgpr_write_b32 a160, v56
v_accvgpr_write_b32 a161, v57
v_accvgpr_write_b32 a162, v58
v_accvgpr_write_b32 a163, v59
v_accvgpr_write_b32 a164, v60
v_accvgpr_write_b32 a165, v61
v_accvgpr_write_b32 a166, v62
v_accvgpr_write_b32 a167, v63
v_accvgpr_read_b32 v56, a168
v_accvgpr_read_b32 v57, a169
v_accvgpr_read_b32 v58, a170
v_accvgpr_read_b32 v59, a171
v_accvgpr_read_b32 v60, a172
v_accvgpr_read_b32 v61, a173
v_accvgpr_read_b32 v62, a174
v_accvgpr_read_b32 v63, a175
v_mul_f32 v56, v56, v40
v_mul_f32 v57, v57, v41
v_mul_f32 v58, v58, v42
v_mul_f32 v59, v59, v43
v_mul_f32 v60, v60, v44
v_mul_f32 v61, v61, v45
v_mul_f32 v62, v62, v46
v_mul_f32 v63, v63, v47
v_accvgpr_write_b32 a168, v56
v_accvgpr_write_b32 a169, v57
v_accvgpr_write_b32 a170, v58
v_accvgpr_write_b32 a171, v59
v_accvgpr_write_b32 a172, v60
v_accvgpr_write_b32 a173, v61
v_accvgpr_write_b32 a174, v62
v_accvgpr_write_b32 a175, v63
v_accvgpr_read_b32 v56, a176
v_accvgpr_read_b32 v57, a177
v_accvgpr_read_b32 v58, a178
v_accvgpr_read_b32 v59, a179
v_accvgpr_read_b32 v60, a180
v_accvgpr_read_b32 v61, a181
v_accvgpr_read_b32 v62, a182
v_accvgpr_read_b32 v63, a183
v_mul_f32 v56, v56, v32
v_mul_f32 v57, v57, v33
v_mul_f32 v58, v58, v34
v_mul_f32 v59, v59, v35
v_mul_f32 v60, v60, v36
v_mul_f32 v61, v61, v37
v_mul_f32 v62, v62, v38
v_mul_f32 v63, v63, v39
v_accvgpr_write_b32 a176, v56
v_accvgpr_write_b32 a177, v57
v_accvgpr_write_b32 a178, v58
v_accvgpr_write_b32 a179, v59
v_accvgpr_write_b32 a180, v60
v_accvgpr_write_b32 a181, v61
v_accvgpr_write_b32 a182, v62
v_accvgpr_write_b32 a183, v63
v_accvgpr_read_b32 v56, a184
v_accvgpr_read_b32 v57, a185
v_accvgpr_read_b32 v58, a186
v_accvgpr_read_b32 v59, a187
v_accvgpr_read_b32 v60, a188
v_accvgpr_read_b32 v61, a189
v_accvgpr_read_b32 v62, a190
v_accvgpr_read_b32 v63, a191
v_mul_f32 v56, v56, v40
v_mul_f32 v57, v57, v41
v_mul_f32 v58, v58, v42
v_mul_f32 v59, v59, v43
v_mul_f32 v60, v60, v44
v_mul_f32 v61, v61, v45
v_mul_f32 v62, v62, v46
v_mul_f32 v63, v63, v47
v_accvgpr_write_b32 a184, v56
v_accvgpr_write_b32 a185, v57
v_accvgpr_write_b32 a186, v58
v_accvgpr_write_b32 a187, v59
v_accvgpr_write_b32 a188, v60
v_accvgpr_write_b32 a189, v61
v_accvgpr_write_b32 a190, v62
v_accvgpr_write_b32 a191, v63
v_accvgpr_read_b32 v56, a192
v_accvgpr_read_b32 v57, a193
v_accvgpr_read_b32 v58, a194
v_accvgpr_read_b32 v59, a195
v_accvgpr_read_b32 v60, a196
v_accvgpr_read_b32 v61, a197
v_accvgpr_read_b32 v62, a198
v_accvgpr_read_b32 v63, a199
v_mul_f32 v56, v56, v32
v_mul_f32 v57, v57, v33
v_mul_f32 v58, v58, v34
v_mul_f32 v59, v59, v35
v_mul_f32 v60, v60, v36
v_mul_f32 v61, v61, v37
v_mul_f32 v62, v62, v38
v_mul_f32 v63, v63, v39
v_accvgpr_write_b32 a192, v56
v_accvgpr_write_b32 a193, v57
v_accvgpr_write_b32 a194, v58
v_accvgpr_write_b32 a195, v59
v_accvgpr_write_b32 a196, v60
v_accvgpr_write_b32 a197, v61
v_accvgpr_write_b32 a198, v62
v_accvgpr_write_b32 a199, v63
v_accvgpr_read_b32 v56, a200
v_accvgpr_read_b32 v57, a201
v_accvgpr_read_b32 v58, a202
v_accvgpr_read_b32 v59, a203
v_accvgpr_read_b32 v60, a204
v_accvgpr_read_b32 v61, a205
v_accvgpr_read_b32 v62, a206
v_accvgpr_read_b32 v63, a207
v_mul_f32 v56, v56, v40
v_mul_f32 v57, v57, v41
v_mul_f32 v58, v58, v42
v_mul_f32 v59, v59, v43
v_mul_f32 v60, v60, v44
v_mul_f32 v61, v61, v45
v_mul_f32 v62, v62, v46
v_mul_f32 v63, v63, v47
v_accvgpr_write_b32 a200, v56
v_accvgpr_write_b32 a201, v57
v_accvgpr_write_b32 a202, v58
v_accvgpr_write_b32 a203, v59
v_accvgpr_write_b32 a204, v60
v_accvgpr_write_b32 a205, v61
v_accvgpr_write_b32 a206, v62
v_accvgpr_write_b32 a207, v63
v_accvgpr_read_b32 v56, a208
v_accvgpr_read_b32 v57, a209
v_accvgpr_read_b32 v58, a210
v_accvgpr_read_b32 v59, a211
v_accvgpr_read_b32 v60, a212
v_accvgpr_read_b32 v61, a213
v_accvgpr_read_b32 v62, a214
v_accvgpr_read_b32 v63, a215
v_mul_f32 v56, v56, v32
v_mul_f32 v57, v57, v33
v_mul_f32 v58, v58, v34
v_mul_f32 v59, v59, v35
v_mul_f32 v60, v60, v36
v_mul_f32 v61, v61, v37
v_mul_f32 v62, v62, v38
v_mul_f32 v63, v63, v39
v_accvgpr_write_b32 a208, v56
v_accvgpr_write_b32 a209, v57
v_accvgpr_write_b32 a210, v58
v_accvgpr_write_b32 a211, v59
v_accvgpr_write_b32 a212, v60
v_accvgpr_write_b32 a213, v61
v_accvgpr_write_b32 a214, v62
v_accvgpr_write_b32 a215, v63
v_accvgpr_read_b32 v56, a216
v_accvgpr_read_b32 v57, a217
v_accvgpr_read_b32 v58, a218
v_accvgpr_read_b32 v59, a219
v_accvgpr_read_b32 v60, a220
v_accvgpr_read_b32 v61, a221
v_accvgpr_read_b32 v62, a222
v_accvgpr_read_b32 v63, a223
v_mul_f32 v56, v56, v40
v_mul_f32 v57, v57, v41
v_mul_f32 v58, v58, v42
v_mul_f32 v59, v59, v43
v_mul_f32 v60, v60, v44
v_mul_f32 v61, v61, v45
v_mul_f32 v62, v62, v46
v_mul_f32 v63, v63, v47
v_accvgpr_write_b32 a216, v56
v_accvgpr_write_b32 a217, v57
v_accvgpr_write_b32 a218, v58
v_accvgpr_write_b32 a219, v59
v_accvgpr_write_b32 a220, v60
v_accvgpr_write_b32 a221, v61
v_accvgpr_write_b32 a222, v62
v_accvgpr_write_b32 a223, v63
v_accvgpr_read_b32 v56, a224
v_accvgpr_read_b32 v57, a225
v_accvgpr_read_b32 v58, a226
v_accvgpr_read_b32 v59, a227
v_accvgpr_read_b32 v60, a228
v_accvgpr_read_b32 v61, a229
v_accvgpr_read_b32 v62, a230
v_accvgpr_read_b32 v63, a231
v_mul_f32 v56, v56, v32
v_mul_f32 v57, v57, v33
v_mul_f32 v58, v58, v34
v_mul_f32 v59, v59, v35
v_mul_f32 v60, v60, v36
v_mul_f32 v61, v61, v37
v_mul_f32 v62, v62, v38
v_mul_f32 v63, v63, v39
v_accvgpr_write_b32 a224, v56
v_accvgpr_write_b32 a225, v57
v_accvgpr_write_b32 a226, v58
v_accvgpr_write_b32 a227, v59
v_accvgpr_write_b32 a228, v60
v_accvgpr_write_b32 a229, v61
v_accvgpr_write_b32 a230, v62
v_accvgpr_write_b32 a231, v63
v_accvgpr_read_b32 v56, a232
v_accvgpr_read_b32 v57, a233
v_accvgpr_read_b32 v58, a234
v_accvgpr_read_b32 v59, a235
v_accvgpr_read_b32 v60, a236
v_accvgpr_read_b32 v61, a237
v_accvgpr_read_b32 v62, a238
v_accvgpr_read_b32 v63, a239
v_mul_f32 v56, v56, v40
v_mul_f32 v57, v57, v41
v_mul_f32 v58, v58, v42
v_mul_f32 v59, v59, v43
v_mul_f32 v60, v60, v44
v_mul_f32 v61, v61, v45
v_mul_f32 v62, v62, v46
v_mul_f32 v63, v63, v47
v_accvgpr_write_b32 a232, v56
v_accvgpr_write_b32 a233, v57
v_accvgpr_write_b32 a234, v58
v_accvgpr_write_b32 a235, v59
v_accvgpr_write_b32 a236, v60
v_accvgpr_write_b32 a237, v61
v_accvgpr_write_b32 a238, v62
v_accvgpr_write_b32 a239, v63
v_accvgpr_read_b32 v56, a240
v_accvgpr_read_b32 v57, a241
v_accvgpr_read_b32 v58, a242
v_accvgpr_read_b32 v59, a243
v_accvgpr_read_b32 v60, a244
v_accvgpr_read_b32 v61, a245
v_accvgpr_read_b32 v62, a246
v_accvgpr_read_b32 v63, a247
v_mul_f32 v56, v56, v32
v_mul_f32 v57, v57, v33
v_mul_f32 v58, v58, v34
v_mul_f32 v59, v59, v35
v_mul_f32 v60, v60, v36
v_mul_f32 v61, v61, v37
v_mul_f32 v62, v62, v38
v_mul_f32 v63, v63, v39
v_accvgpr_write_b32 a240, v56
v_accvgpr_write_b32 a241, v57
v_accvgpr_write_b32 a242, v58
v_accvgpr_write_b32 a243, v59
v_accvgpr_write_b32 a244, v60
v_accvgpr_write_b32 a245, v61
v_accvgpr_write_b32 a246, v62
v_accvgpr_write_b32 a247, v63
v_accvgpr_read_b32 v56, a248
v_accvgpr_read_b32 v57, a249
v_accvgpr_read_b32 v58, a250
v_accvgpr_read_b32 v59, a251
v_accvgpr_read_b32 v60, a252
v_accvgpr_read_b32 v61, a253
v_accvgpr_read_b32 v62, a254
v_accvgpr_read_b32 v63, a255
v_mul_f32 v56, v56, v40
v_mul_f32 v57, v57, v41
v_mul_f32 v58, v58, v42
v_mul_f32 v59, v59, v43
v_mul_f32 v60, v60, v44
v_mul_f32 v61, v61, v45
v_mul_f32 v62, v62, v46
v_mul_f32 v63, v63, v47
v_accvgpr_write_b32 a248, v56
v_accvgpr_write_b32 a249, v57
v_accvgpr_write_b32 a250, v58
v_accvgpr_write_b32 a251, v59
v_accvgpr_write_b32 a252, v60
v_accvgpr_write_b32 a253, v61
v_accvgpr_write_b32 a254, v62
v_accvgpr_write_b32 a255, v63
s_nop 1
s_setpc_b64 s[50:51]
L_end%=:
)ASM" :: [ka] "s"(__builtin_amdgcn_kernarg_segment_ptr()), [wave] "s"(wave), [xg] "s"(xg), [jj] "s"(jj) : "v0", "v1", "v2", "v3", "v4", "v5", "v6", "v7", "v8", "v9", "v10", "v11", "v12", "v13", "v14", "v15", "v16", "v17", "v18", "v19", "v20", "v21", "v22", "v23", "v24", "v25", "v26", "v27", "v28", "v29", "v30", "v31", "v32", "v33", "v34", "v35", "v36", "v37", "v38", "v39", "v40", "v41", "v42", "v43", "v44", "v45", "v46", "v47", "v48", "v49", "v50", "v51", "v52", "v53", "v54", "v55", "v56", "v57", "v58", "v59", "v60", "v61", "v62", "v63", "v64", "v65", "v66", "v67", "v68", "v69", "v70", "v71", "v72", "v73", "v74", "v75", "v76", "v77", "v78", "v79", "v80", "v81", "v82", "v83", "v84", "v85", "v86", "v87", "v88", "v89", "v90", "v91", "v92", "v93", "v94", "v95", "v96", "v97", "v98", "v99", "v100", "v101", "v102", "v103", "v104", "v105", "v106", "v107", "v108", "v109", "v110", "v111", "v112", "v113", "v114", "v115", "v116", "v117", "v118", "v119", "v120", "v121", "v122", "v123", "v124", "v125", "v126", "v127", "v128", "v129", "v130", "v131", "v132", "v133", "v134", "v135", "v136", "v137", "v138", "v139", "v140", "v141", "v142", "v143", "v144", "v145", "v146", "v147", "v148", "v149", "v150", "v151", "v152", "v153", "v154", "v155", "v156", "v157", "v158", "v159", "v160", "v161", "v162", "v163", "v164", "v165", "v166", "v167", "v168", "v169", "v170", "v171", "v172", "v173", "v174", "v175", "v176", "v177", "v178", "v179", "v180", "v181", "v182", "v183", "v184", "v185", "v186", "v187", "v188", "v189", "v190", "v191", "v192", "v193", "v194", "v195", "v196", "v197", "v198", "v199", "v200", "v201", "v202", "v203", "v204", "v205", "v206", "v207", "v208", "v209", "v210", "v211", "v212", "v213", "v214", "v215", "v216", "v217", "v218", "v219", "v220", "v221", "v222", "v223", "v224", "v225", "v226", "v227", "v228", "v229", "v230", "v231", "v232", "v233", "v234", "v235", "v236", "v237", "v238", "v239", "v240", "v241", "v242", "v243", "v244", "v245", "v246", "v247", "v248", "v249", "v250", "v251", "v252", "v253", "v254", "v255", "a0", "a1", "a2", "a3", "a4", "a5", "a6", "a7", "a8", "a9", "a10", "a11", "a12", "a13", "a14", "a15", "a16", "a17", "a18", "a19", "a20", "a21", "a22", "a23", "a24", "a25", "a26", "a27", "a28", "a29", "a30", "a31", "a32", "a33", "a34", "a35", "a36", "a37", "a38", "a39", "a40", "a41", "a42", "a43", "a44", "a45", "a46", "a47", "a48", "a49", "a50", "a51", "a52", "a53", "a54", "a55", "a56", "a57", "a58", "a59", "a60", "a61", "a62", "a63", "a64", "a65", "a66", "a67", "a68", "a69", "a70", "a71", "a72", "a73", "a74", "a75", "a76", "a77", "a78", "a79", "a80", "a81", "a82", "a83", "a84", "a85", "a86", "a87", "a88", "a89", "a90", "a91", "a92", "a93", "a94", "a95", "a96", "a97", "a98", "a99", "a100", "a101", "a102", "a103", "a104", "a105", "a106", "a107", "a108", "a109", "a110", "a111", "a112", "a113", "a114", "a115", "a116", "a117", "a118", "a119", "a120", "a121", "a122", "a123", "a124", "a125", "a126", "a127", "a128", "a129", "a130", "a131", "a132", "a133", "a134", "a135", "a136", "a137", "a138", "a139", "a140", "a141", "a142", "a143", "a144", "a145", "a146", "a147", "a148", "a149", "a150", "a151", "a152", "a153", "a154", "a155", "a156", "a157", "a158", "a159", "a160", "a161", "a162", "a163", "a164", "a165", "a166", "a167", "a168", "a169", "a170", "a171", "a172", "a173", "a174", "a175", "a176", "a177", "a178", "a179", "a180", "a181", "a182", "a183", "a184", "a185", "a186", "a187", "a188", "a189", "a190", "a191", "a192", "a193", "a194", "a195", "a196", "a197", "a198", "a199", "a200", "a201", "a202", "a203", "a204", "a205", "a206", "a207", "a208", "a209", "a210", "a211", "a212", "a213", "a214", "a215", "a216", "a217", "a218", "a219", "a220", "a221", "a222", "a223", "a224", "a225", "a226", "a227", "a228", "a229", "a230", "a231", "a232", "a233", "a234", "a235", "a236", "a237", "a238", "a239", "a240", "a241", "a242", "a243", "a244", "a245", "a246", "a247", "a248", "a249", "a250", "a251", "a252", "a253", "a254", "a255", "s8", "s9", "s10", "s11", "s12", "s13", "s14", "s15", "s16", "s17", "s18", "s19", "s20", "s21", "s22", "s23", "s24", "s25", "s26", "s27", "s28", "s29", "s30", "s31", "s32", "s33", "s34", "s35", "s36", "s37", "s38", "s39", "s40", "s41", "s42", "s43", "s44", "s45", "s46", "s47", "s48", "s49", "s50", "s51", "s52", "s53", "s54", "s55", "s56", "s57", "s58", "s59", "s60", "s61", "s62", "s63", "s64", "s65", "s66", "s67", "s68", "s69", "s70", "s71", "s72", "s73", "s74", "s75", "s76", "s77", "s78", "s79", "s80", "s81", "s82", "s83", "s84", "s85", "s86", "s87", "s88", "s89", "s90", "s91", "s92", "s93", "s94", "s95", "s96", "s97", "s98", "s99", "vcc", "scc", "m0", "memory");
}
extern "C" void kernel_launch(void* const* d_in, const int* in_sizes, int n_in, void* d_out, int out_size, void* d_ws, size_t ws_size, hipStream_t stream) {
  (void)hipFuncSetAttribute((const void*)fused_block, hipFuncAttributeMaxDynamicSharedMemorySize, 163840);
  fused_block<<<256, 256, 163840, stream>>>(d_in[0], d_in[1], d_in[2], d_in[3], d_in[4], d_in[6], d_in[7], d_in[8], d_in[9], d_out, d_ws);
}
```
